```python
import torch

def _softmax(x, axis=-1):
    return torch.softmax(x, dim=axis)
def _gelu(x):
    return torch.nn.functional.gelu(x, approximate="tanh")
def _silu(x):
    return torch.nn.functional.silu(x)
def _sigmoid(x):
    return torch.sigmoid(x)
def _layernorm(x, axis=-1, eps=1e-5):
    m = x.mean(dim=axis, keepdim=True)
    v = x.var(dim=axis, keepdim=True, unbiased=False)
    return (x - m) / torch.sqrt(v + eps)
def _scatter_add(tgt, idx, src, axis=0):
    return tgt.clone().index_add_(axis, idx, src)
def _segment_sum(src, seg, n):
    out = torch.zeros((n,) + src.shape[1:], dtype=src.dtype)
    return out.index_add_(0, seg, src)
def _pad(x, pw):
    flat = tuple(v for pair in reversed(pw) for v in pair)
    return torch.nn.functional.pad(x, flat)

kernel_name = 'concat_heads__gqa_block_cached__mha_block_cached__near_ridge'

def setup_inputs():

    _w = (lambda a: (lambda t: t.to(torch.bfloat16) if t.is_floating_point() else t)(torch.as_tensor(a)))
    def _A_setup():
        B, Sq, D = 16, 160, 512
        Hq, Hkv, Sk, Dh = 32, 32, 512, 128
        assert Hq % Hkv == 0, (Hq, Hkv)
        return {
            "x": _w((lambda *s: torch.randn(*s, dtype=torch.bfloat16) * 0.8)(B, Sq, D)),
            "k_cache": _w((lambda *s: torch.randn(*s, dtype=torch.bfloat16) * 0.8)(B, Hkv, Sk, Dh)),
            "v_cache": _w((lambda *s: torch.randn(*s, dtype=torch.bfloat16) * 0.05)(B, Hkv, Sk, Dh)),
            "wq": _w((lambda *s: torch.randn(*s, dtype=torch.bfloat16) * 0.8)(D, Hq * Dh) * (D ** -0.5)),
            "wo": _w((lambda *s: torch.randn(*s, dtype=torch.bfloat16) * 0.05)(Hq * Dh, D) * ((Hq * Dh) ** -0.5)),
        }
    _A_in = _A_setup()

    def _B_setup():
        B, Sq, D = 16, 160, 512
        H, Sk, Dh = 32, 512, 128
        return {
            "x": _w((lambda *s: torch.randn(*s, dtype=torch.bfloat16) * 0.8)(B, Sq, D)),
            "k_cache": _w((lambda *s: torch.randn(*s, dtype=torch.bfloat16) * 0.8)(B, H, Sk, Dh)),
            "v_cache": _w((lambda *s: torch.randn(*s, dtype=torch.bfloat16) * 0.05)(B, H, Sk, Dh)),
            "wq": _w((lambda *s: torch.randn(*s, dtype=torch.bfloat16) * 0.8)(D, H * Dh) * (D ** -0.5)),
            "wo": _w((lambda *s: torch.randn(*s, dtype=torch.bfloat16) * 0.05)(H * Dh, D) * ((H * Dh) ** -0.5)),
        }
    _B_in = _B_setup()

    return {
        "A_x": _A_in["x"],
        "A_k_cache": _A_in["k_cache"],
        "A_v_cache": _A_in["v_cache"],
        "A_wq": _A_in["wq"],
        "A_wo": _A_in["wo"],
        "B_x": _B_in["x"],
        "B_k_cache": _B_in["k_cache"],
        "B_v_cache": _B_in["v_cache"],
        "B_wq": _B_in["wq"],
        "B_wo": _B_in["wo"],
    }

def reference(A_x, A_k_cache, A_v_cache, A_wq, A_wo, B_x, B_k_cache, B_v_cache, B_wq, B_wo):
    def _A_ref(x, k_cache, v_cache, wq, wo):
        B, Sq, D = x.shape
        Hq, Dh = 32, 128
        Hkv = k_cache.shape[1]
        rep = Hq // Hkv
        q = (lambda x, *d: x.permute(*d))((x @ wq).reshape(B, Sq, Hq, Dh), 0, 2, 1, 3)
        k = (lambda x, n, axis: torch.repeat_interleave(x, n, dim=axis))(k_cache, rep, axis=1)
        v = (lambda x, n, axis: torch.repeat_interleave(x, n, dim=axis))(v_cache, rep, axis=1)
        scores = torch.einsum("bhqd,bhkd->bhqk", q, k) * (Dh ** -0.5)
        p = _softmax(scores, axis=-1)
        o = torch.einsum("bhqk,bhkd->bhqd", p, v)
        return (lambda x, *d: x.permute(*d))(o, 0, 2, 1, 3).reshape(B, Sq, Hq * Dh) @ wo

    def _B_ref(x, k_cache, v_cache, wq, wo):
        B, Sq, D = x.shape
        H, Dh = 32, 128
        q = (lambda x, *d: x.permute(*d))((x @ wq).reshape(B, Sq, H, Dh), 0, 2, 1, 3)
        scores = torch.einsum("bhqd,bhkd->bhqk", q, k_cache) * (Dh ** -0.5)
        p = _softmax(scores, axis=-1)
        o = torch.einsum("bhqk,bhkd->bhqd", p, v_cache)
        return (lambda x, *d: x.permute(*d))(o, 0, 2, 1, 3).reshape(B, Sq, H * Dh) @ wo

    a = _A_ref(A_x, A_k_cache, A_v_cache, A_wq, A_wo)
    b = _B_ref(B_x, B_k_cache, B_v_cache, B_wq, B_wo)
    return torch.concatenate([a, b], axis=-1)
```

```cpp
#include <hip/hip_runtime.h>
#include <hip/hip_bf16.h>
typedef unsigned short u16;
__device__ __forceinline__ float b2f(u16 x){ return __uint_as_float(((unsigned)x)<<16); }
__device__ __forceinline__ u16 f2b(float x){ unsigned u=__float_as_uint(x); u += 0x7fffu + ((u>>16)&1u); return (u16)(u>>16); }
constexpr int B=16,SQ=160,D=512,H=32,SK=512,DH=128;
__global__ __launch_bounds__(256) void k_attn(const u16* x,const u16* kc,const u16* vc,const u16* wq,u16* o){
  __shared__ float q[16][DH]; __shared__ float s[16][SK];
  int blk=blockIdx.x; int qt=blk%10; int h=(blk/10)%H; int b=blk/(10*H);
  int t=threadIdx.x;
  for(int i=t;i<16*DH;i+=256){ int r=i/DH,d=i%DH; float a=0; const u16* xr=x+(size_t)(b*SQ+qt*16+r)*D;
    for(int k=0;k<D;k++) a+=b2f(xr[k])*b2f(wq[(size_t)k*(H*DH)+h*DH+d]); q[r][d]=b2f(f2b(a)); }
  __syncthreads();
  const u16* K=kc+(size_t)(b*H+h)*SK*DH; const u16* V=vc+(size_t)(b*H+h)*SK*DH;
  for(int i=t;i<16*SK;i+=256){ int r=i/SK,k=i%SK; float a=0; for(int d=0;d<DH;d++) a+=q[r][d]*b2f(K[k*DH+d]); s[r][k]=a*0.08838834764831845f; }
  __syncthreads();
  if(t<16){ float m=-INFINITY; for(int k=0;k<SK;k++) m=fmaxf(m,s[t][k]); float l=0; for(int k=0;k<SK;k++){ float p=__expf(s[t][k]-m); s[t][k]=p; l+=p;} float il=1.f/l; for(int k=0;k<SK;k++) s[t][k]*=il; }
  __syncthreads();
  for(int i=t;i<16*DH;i+=256){ int r=i/DH,d=i%DH; float a=0; for(int k=0;k<SK;k++) a+=s[r][k]*b2f(V[k*DH+d]); o[(size_t)(b*SQ+qt*16+r)*(H*DH)+h*DH+d]=f2b(a); }
}
__global__ __launch_bounds__(256) void k_out(const u16* o,const u16* wo,u16* out,int coff){
  int row=blockIdx.x;
  for(int n=threadIdx.x;n<D;n+=256){ float a=0; const u16* orow=o+(size_t)row*(H*DH); for(int k=0;k<H*DH;k++) a+=b2f(orow[k])*b2f(wo[(size_t)k*D+n]); out[(size_t)row*(2*D)+coff+n]=f2b(a);}
}
extern "C" void kernel_launch(void* const* d_in, const int* in_sizes, int n_in, void* d_out, int out_size, void* d_ws, size_t ws_size, hipStream_t stream) {
  for(int br=0;br<2;br++){
    u16* o=(u16*)d_ws+(size_t)br*B*SQ*H*DH;
    k_attn<<<B*H*10,256,0,stream>>>((const u16*)d_in[5*br],(const u16*)d_in[5*br+1],(const u16*)d_in[5*br+2],(const u16*)d_in[5*br+3],o);
    k_out<<<B*SQ,256,0,stream>>>(o,(const u16*)d_in[5*br+4],(u16*)d_out,br*D);
  }
}
```

```cpp
#include <hip/hip_runtime.h>
#include <hip/hip_bf16.h>
typedef unsigned short u16;
constexpr int NB=16,SQ=160,DM=512,NH=32,SK=512,DH=128;
constexpr size_t XG_STRIDE=1167360;
constexpr size_t XG_OFF=0, O_OFF=(size_t)48<<20;
__device__ __forceinline__ float b2f(u16 x){ return __uint_as_float(((unsigned)x)<<16); }
__device__ __forceinline__ u16 f2b(float x){ typedef float f2 __attribute__((ext_vector_type(2))); typedef __bf16 b2 __attribute__((ext_vector_type(2)));
  f2 v={x,0.f}; b2 r=__builtin_convertvector(v,b2); return (u16)(__builtin_bit_cast(unsigned,r)&0xffffu); }
__device__ __forceinline__ size_t o_index(int row,int k){
  return ((size_t)(k>>5)*10+(row>>4))*512+((row&15)+16*((k>>2)&3))*8+4*((k>>4)&1)+(k&3);
}
struct Rec{ const char* x; const char* k; const char* v; const char* wq; const char* wo; char* ws; char* out; char* ws2; };
struct P1{ Rec r[2]; };
typedef unsigned long long u64x8 __attribute__((ext_vector_type(8)));

__device__ __forceinline__ void slow_head(const u16* x,const u16* wq,const u16* K,const u16* V,u16* o,int head,int row0,float* sm,int lane){
  float* qv=sm; float* pv=sm+128;
  for(int r=0;r<80;r++){
    const int row=row0+r;
    float a0=0.f,a1=0.f;
    for(int k=0;k<DM;k++){ const float xv=b2f(x[row*DM+k]); const u16* w=wq+(size_t)k*(NH*DH)+head*DH; a0+=xv*b2f(w[lane]); a1+=xv*b2f(w[lane+64]); }
    qv[lane]=b2f(f2b(a0)); qv[lane+64]=b2f(f2b(a1));
    __builtin_amdgcn_wave_barrier(); asm volatile("s_waitcnt lgkmcnt(0)":::"memory");
    float m=-INFINITY;
    for(int i=0;i<8;i++){ const u16* kr=K+(size_t)(lane+64*i)*DH; float s=0.f; for(int d=0;d<DH;d++) s+=qv[d]*b2f(kr[d]); s*=0.08838834764831845f; pv[lane+64*i]=s; m=fmaxf(m,s); }
    for(int off=32;off;off>>=1) m=fmaxf(m,__shfl_xor(m,off));
    float l=0.f;
    for(int i=0;i<8;i++){ const float p=__expf(pv[lane+64*i]-m); pv[lane+64*i]=p; l+=p; }
    for(int off=32;off;off>>=1) l+=__shfl_xor(l,off);
    __builtin_amdgcn_wave_barrier(); asm volatile("s_waitcnt lgkmcnt(0)":::"memory");
    a0=0.f;a1=0.f;
    for(int k=0;k<SK;k++){ const float p=pv[k]; a0+=p*b2f(V[(size_t)k*DH+lane]); a1+=p*b2f(V[(size_t)k*DH+lane+64]); }
    const float il=1.f/l;
    o[o_index(row,head*DH+lane)]=f2b(a0*il); o[o_index(row,head*DH+lane+64)]=f2b(a1*il);
    __builtin_amdgcn_wave_barrier(); asm volatile("s_waitcnt lgkmcnt(0)":::"memory");
  }
}

typedef const __attribute__((address_space(4))) P1* KArg;
#define FA_DECODE \
      asm volatile("" : "+s"(a),"+s"(id),"+s"(w)); \
      const unsigned xcd=id&7u, j=id>>3, br=xcd>>2, xq=xcd&3u; \
      u64x8 rec; asm volatile("s_load_dwordx16 %0, %1, %2\n\ts_waitcnt lgkmcnt(0)":"=&s"(rec):"s"(a),"s"(br*64u):"memory"); \
      const unsigned b=(xq&1u)*8u+(j&7u), hg=(xq>>1)*4u+(j>>3), p=w>>1, r=w&1u, h0=hg*4u+p;
#define FA_OPERANDS \
      const unsigned ty=__builtin_amdgcn_readfirstlane(((id&1u)^1u)|(((((id*4u+p)*2654435761u)>>20)&15u)<<13)|(((xcd)&7u)<<19)); \
      const char* xb=((const char*)rec[0])+(size_t)b*(SQ*DM*2); \
      const char* wqb=((const char*)rec[3])+h0*256u; \
      const unsigned wq_n=4194304u-h0*256u; \
      const size_t kvo=(size_t)(b*NH+h0)*131072u+r*4096u; \
      const char* kb=((const char*)rec[1])+kvo; \
      const char* vb=((const char*)rec[2])+kvo; \
      const char* wob=((const char*)rec[4])+h0*131072u+r*32768u; \
      char* xg=((char*)rec[5])+XG_OFF+(size_t)(br*NB+b)*XG_STRIDE; \
      const unsigned ooff=(b*SQ+80u*r)*2048u+br*1024u+hg*128u; \
      char* outb=((char*)rec[6])+ooff; const unsigned outn=NB*SQ*2048u-ooff; \
      char* dump=((char*)rec[5])+O_OFF+(size_t)(br*NB+b)*(128*10*1024)+(size_t)(hg*160u+5u*r)*1024u;
#define FA_IO : "=s"(status) : "s"(xb),"s"(wqb),"s"(kb),"s"(vb),"s"(wob),"s"(xg),"s"(outb),"s"(dump),"s"(w),"s"(hg),"s"(wq_n),"s"(outn),"s"(ty),"s"(dump),"s"(j) : "v0","v1","v2","v3","v4","v5","v6","v7","v8","v9","v10","v11","v12","v13","v14","v15","v16","v17","v18","v19","v20","v21","v22","v23","v24","v25","v26","v27","v28","v29","v30","v31","v32","v33","v34","v35","v36","v37","v38","v39","v40","v41","v42","v43","v44","v45","v46","v47","v48","v49","v50","v51","v52","v53","v54","v55","v56","v57","v58","v59","v60","v61","v62","v63","v64","v65","v66","v67","v68","v69","v70","v71","v72","v73","v74","v75","v76","v77","v78","v79","v80","v81","v82","v83","v84","v85","v86","v87","v88","v89","v90","v91","v92","v93","v94","v95","v96","v97","v98","v99","v100","v101","v102","v103","v104","v105","v106","v107","v108","v109","v110","v111","v112","v113","v114","v115","v116","v117","v118","v119","v120","v121","v122","v123","v124","v125","v126","v127","v128","v129","v130","v131","v132","v133","v134","v135","v136","v137","v138","v139","v140","v141","v142","v143","v144","v145","v146","v147","v148","v149","v150","v151","v152","v153","v154","v155","v156","v157","v158","v159","v160","v161","v162","v163","v164","v165","v166","v167","v168","v169","v170","v171","v172","v173","v174","v175","v176","v177","v178","v179","v180","v181","v182","v183","v184","v185","v186","v187","v188","v189","v190","v191","v192","v193","v194","v195","v196","v197","v198","v199","v200","v201","v202","v203","v204","v205","v206","v207","v208","v209","v210","v211","v212","v213","v214","v215","v216","v217","v218","v219","v220","v221","v222","v223","v224","v225","v226","v227","v228","v229","v230","v231","v232","v233","v234","v235","v236","v237","v238","v239","v240","v241","v242","v243","v244","v245","v246","v247","v248","v249","v250","v251","v252","v253","v254","v255","a0","a1","a2","a3","a4","a5","a6","a7","a8","a9","a10","a11","a12","a13","a14","a15","a16","a17","a18","a19","a20","a21","a22","a23","a24","a25","a26","a27","a28","a29","a30","a31","a32","a33","a34","a35","a36","a37","a38","a39","a40","a41","a42","a43","a44","a45","a46","a47","a48","a49","a50","a51","a52","a53","a54","a55","a56","a57","a58","a59","a60","a61","a62","a63","a64","a65","a66","a67","a68","a69","a70","a71","a72","a73","a74","a75","a76","a77","a78","a79","a80","a81","a82","a83","a84","a85","a86","a87","a88","a89","a90","a91","a92","a93","a94","a95","a96","a97","a98","a99","a100","a101","a102","a103","a104","a105","a106","a107","a108","a109","a110","a111","a112","a113","a114","a115","a116","a117","a118","a119","a120","a121","a122","a123","a124","a125","a126","a127","a128","a129","a130","a131","a132","a133","a134","a135","a136","a137","a138","a139","a140","a141","a142","a143","a144","a145","a146","a147","a148","a149","a150","a151","a152","a153","a154","a155","a156","a157","a158","a159","a160","a161","a162","a163","a164","a165","a166","a167","a168","a169","a170","a171","a172","a173","a174","a175","a176","a177","a178","a179","a180","a181","a182","a183","a184","a185","a186","a187","a188","a189","a190","a191","a192","a193","a194","a195","a196","a197","a198","a199","a200","a201","a202","a203","a204","a205","a206","a207","a208","a209","a210","a211","a212","a213","a214","a215","a216","a217","a218","a219","a220","a221","a222","a223","a224","a225","a226","a227","a228","a229","a230","a231","a232","a233","a234","a235","a236","a237","a238","a239","a240","a241","a242","a243","a244","a245","a246","a247","a248","a249","a250","a251","a252","a253","a254","a255","s36","s37","s38","s39","s40","s41","s42","s43","s44","s45","s46","s47","s48","s49","s50","s51","s52","s53","s54","s55","s56","s57","s58","s59","s60","s61","s62","s63","s64","s65","s66","s67","s68","s69","s70","s71","s72","s73","s74","s75","s76","s77","s78","s79","s80","s81","s82","s83","s84","s85","s86","s87","s88","s89","s90","s91","s92","s93","s94","s95","s96","s97","s98","s99","vcc","scc","memory"
__global__ __launch_bounds__(256,1) void fused_attn(P1 a_unused){
  extern __shared__ __attribute__((aligned(16))) char lds[];
  KArg a=(KArg)__builtin_amdgcn_kernarg_segment_ptr();
  unsigned id=blockIdx.x, w=__builtin_amdgcn_readfirstlane(threadIdx.x>>6), status;
  {
    FA_DECODE FA_OPERANDS
    asm volatile(
".macro DL v,r,s,o\nbuffer_load_dwordx4 v[\\v], s[\\r:\\r+3], s[\\s] offen offset:\\o lds\n.endm\n.macro AW d,s\nv_accvgpr_write_b32 a[\\d], v[\\s]\n"
".endm\n.macro Ra d,a,o\nds_read_b128 a[\\d:\\d+3], v[\\a] offset:\\o\n.endm\n.macro Ta d,a,o\nds_read_b64_tr_b16 a[\\d:\\d+1], v[\\a] offset:\\o\n"
".endm\n.macro Zvaa d,a,b\nv_mfma_f32_16x16x32_bf16 v[\\d:\\d+3], a[\\a:\\a+3], a[\\b:\\b+3], 0\n.endm\n.macro Mvaa d,a,b\n"
"v_mfma_f32_16x16x32_bf16 v[\\d:\\d+3], a[\\a:\\a+3], a[\\b:\\b+3], v[\\d:\\d+3]\n.endm\n.macro Tv d,a,o\n"
"ds_read_b64_tr_b16 v[\\d:\\d+1], v[\\a] offset:\\o\n.endm\n.macro Mvva d,a,b\n"
"v_mfma_f32_16x16x32_bf16 v[\\d:\\d+3], v[\\a:\\a+3], a[\\b:\\b+3], v[\\d:\\d+3]\n.endm\n.macro CV d,s\nv_cvt_pk_bf16_f32 v[\\d], v[\\s], v[\\s+1]\n"
".endm\n.macro PS d,s\nv_pk_mul_f32 v[\\d:\\d+1], v[\\d:\\d+1], s[\\s:\\s+1] op_sel_hi:[1,0]\n.endm\n.macro Zvva d,a,b\n"
"v_mfma_f32_16x16x32_bf16 v[\\d:\\d+3], v[\\a:\\a+3], a[\\b:\\b+3], 0\n.endm\n.macro DtL v,r,s,o\n"
"buffer_load_dwordx4 v[\\v], s[\\r:\\r+3], s[\\s] offen offset:\\o nt lds\n.endm\n.macro Rv d,a,o\nds_read_b128 v[\\d:\\d+3], v[\\a] offset:\\o\n"
".endm\n.macro Cvaa d,a,b,c\nv_mfma_f32_16x16x32_bf16 v[\\d:\\d+3], a[\\a:\\a+3], a[\\b:\\b+3], v[\\c:\\c+3]\n.endm\n.macro Cvva d,a,b,c\n"
"v_mfma_f32_16x16x32_bf16 v[\\d:\\d+3], v[\\a:\\a+3], a[\\b:\\b+3], v[\\c:\\c+3]\n.endm\n.macro EX d\nv_exp_f32 v[\\d], v[\\d]\n.endm\n"
".macro Zvav d,a,b\nv_mfma_f32_16x16x32_bf16 v[\\d:\\d+3], a[\\a:\\a+3], v[\\b:\\b+3], 0\n.endm\n.macro Mvav d,a,b\n"
"v_mfma_f32_16x16x32_bf16 v[\\d:\\d+3], a[\\a:\\a+3], v[\\b:\\b+3], v[\\d:\\d+3]\n.endm\n.macro Mvvv d,a,b\n"
"v_mfma_f32_16x16x32_bf16 v[\\d:\\d+3], v[\\a:\\a+3], v[\\b:\\b+3], v[\\d:\\d+3]\n.endm\n.macro AD d,s\nv_add_f32 v[\\d], v[\\d], v[\\s]\n.endm\n"
".macro PV d,s\nv_pk_mul_f32 v[\\d:\\d+1], v[\\d:\\d+1], v[\\s:\\s+1] op_sel_hi:[1,0]\n.endm\n.macro Zvvv d,a,b\n"
"v_mfma_f32_16x16x32_bf16 v[\\d:\\d+3], v[\\a:\\a+3], v[\\b:\\b+3], 0\n.endm\n.Lfa_c0:\ns_mov_b64 s[40:41], %2\ns_mov_b32 s42, %11\n"
"s_mov_b32 s43, 0x20000\ns_mov_b64 s[36:37], %1\ns_mov_b32 s38, 163840\ns_mov_b32 s39, 0x20000\ns_lshr_b32 s83, %9, 1\ns_and_b32 s84, %9, 1\n"
"s_lshl_b32 s88, s83, 14\ns_lshl_b32 s89, s84, 12\ns_add_u32 s72, s88, s89\ns_mul_i32 s89, s84, 3072\ns_add_u32 s73, s88, s89\n"
"s_add_u32 s73, s73, 10240\ns_add_u32 s74, s72, 10240\ns_lshl3_add_u32 s63, s84, s72\ns_and_b32 s90, %13, 0x380000\ns_lshr_b32 s91, s90, 12\n"
"s_bitcmp0_b32 %13, 0\ns_cselect_b32 s75, 1, 1\ns_cselect_b32 s78, 0, 2\ns_cselect_b32 s86, 4, 3\ns_lshl_b32 s75, s75, 15\ns_lshl_b32 s78, s78, 15\n"
"s_lshl_b32 s86, s86, 15\nv_mbcnt_lo_u32_b32 v0, -1, 0\nv_mbcnt_hi_u32_b32 v0, -1, v0\nv_and_b32 v1, 15, v0\nv_lshrrev_b32 v2, 4, v0\n"
"v_bfe_u32 v3, v0, 2, 2\nv_and_b32 v4, 3, v0\ns_lshl_b32 s89, s84, 16\nv_lshrrev_b32 v6, 5, v0\nv_bfe_u32 v7, v0, 4, 1\nv_lshlrev_b32 v5, 4, v4\n"
"v_lshl_add_u32 v5, v7, 6, v5\nv_lshl_add_u32 v5, v3, 13, v5\nv_lshl_add_u32 v5, v6, 15, v5\nv_add_u32 v242, s89, v5\nv_add_u32 v243, 0x1fc00, v242\n"
"v_add_u32 v244, 0x3f800, v242\nv_add_u32 v245, 0x5f400, v242\nv_lshrrev_b32 v6, 3, v0\nv_and_b32 v7, 7, v0\nv_xor_b32 v7, v7, v2\n"
"s_mul_i32 s88, s83, 81920\ns_mul_i32 s89, s84, 24576\ns_add_u32 s89, s89, s88\ns_lshl_b32 s95, s84, 2\nv_xor_b32 v5, s95, v7\n"
"v_lshlrev_b32 v5, 4, v5\nv_lshl_add_u32 v5, v6, 10, v5\nv_add_u32 v5, s89, v5\nv_add_u32 v246, 0x0, v5\nv_xor_b32 v5, s95, v7\nv_xor_b32 v5, 4, v5\n"
"v_lshlrev_b32 v5, 4, v5\nv_lshl_add_u32 v5, v6, 10, v5\nv_add_u32 v5, s89, v5\nv_add_u32 v247, 0x1c00, v5\nv_xor_b32 v5, s95, v7\n"
"v_lshlrev_b32 v5, 4, v5\nv_lshl_add_u32 v5, v6, 10, v5\nv_add_u32 v5, s89, v5\nv_add_u32 v248, 0x3800, v5\ns_lshl_b32 s89, s84, 14\n"
"s_add_u32 s89, s89, s88\ns_add_u32 s89, s89, 49152\nv_xor_b32 v5, 0, v7\nv_lshlrev_b32 v5, 4, v5\nv_lshl_add_u32 v5, v6, 10, v5\n"
"v_add_u32 v5, s89, v5\nv_add_u32 v249, 0x0, v5\nv_xor_b32 v5, 4, v7\nv_lshlrev_b32 v5, 4, v5\nv_lshl_add_u32 v5, v6, 10, v5\nv_add_u32 v5, s89, v5\n"
"v_add_u32 v250, 0x1c00, v5\ns_add_u32 s76, s90, 0x0\ns_and_b32 s76, s76, 0x3fffff\ns_add_u32 m0, s63, s75\ns_nop 0\nDL 242,40,76,0\n"
"DL 243,40,76,1024\nDL 244,40,76,2048\nDL 245,40,76,3072\ns_add_u32 s77, s91, 0x0\ns_and_b32 s77, s77, 0x3ff\ns_add_u32 m0, s73, s75\ns_nop 0\n"
"DL 246,36,77,0\nDL 247,36,77,1024\nDL 248,36,77,2048\ns_add_u32 m0, s72, s78\ns_add_u32 m0, m0, 0x2000\ns_nop 0\nDL 249,36,77,0\nDL 250,36,77,1024\n"
"s_add_u32 s76, s90, 0x80\ns_and_b32 s76, s76, 0x3fffff\ns_add_u32 m0, s72, s78\ns_nop 0\nDL 242,40,76,0\nDL 243,40,76,1024\nDL 244,40,76,2048\n"
"DL 245,40,76,3072\ns_add_u32 s77, s91, 0x80\ns_and_b32 s77, s77, 0x3ff\ns_add_u32 m0, s74, s78\ns_nop 0\nDL 249,36,77,0\nDL 250,36,77,1024\n"
"s_mov_b32 s60, %9\ns_mov_b32 s61, %10\ns_mov_b32 s62, %15\ns_getpc_b64 s[68:69]\n.Lfa_pc1:\n"
"s_sub_u32 s68, s68, .Lfa_pc1-.Lfa_c0\ns_subb_u32 s69, s69, 0\ns_mov_b32 s70, .Lfa_c1-.Lfa_c0\ns_mov_b32 s71, 0x20000\ns_lshl_b32 s96, s62, 2\n"
"s_add_u32 s96, s96, s60\ns_lshl_b32 s96, s96, 11\ns_lshl_b32 s94, s60, 12\ns_mov_b64 s[44:45], %3\ns_mov_b32 s46, 389120\ns_mov_b32 s47, 0x20000\n"
"s_mov_b64 s[48:49], %4\ns_mov_b32 s50, 389120\ns_mov_b32 s51, 0x20000\ns_mov_b64 s[52:53], %5\ns_mov_b32 s54, 360448\ns_mov_b32 s55, 0x20000\n"
"s_mov_b64 s[56:57], %6\ns_mov_b32 s58, 1163520\ns_mov_b32 s59, 0x20000\ns_mov_b64 s[64:65], %7\ns_mov_b32 s66, %12\ns_mov_b32 s67, 0x20000\n"
"s_mov_b32 s79, 0\ns_mov_b32 s80, 0x3e0293ee\ns_mov_b32 s81, 0x80008000\ns_mov_b32 s82, 0x6c800000\ns_lshl_b32 s85, s61, 7\n"
"s_and_b32 s97, %13, 0x1e000\nv_xor_b32 v5, v1, v2\nv_xor_b32 v5, 0, v5\nv_lshlrev_b32 v5, 4, v5\nv_lshl_add_u32 v210, v2, 8, v5\n"
"v_xor_b32 v5, v1, v2\nv_xor_b32 v5, 4, v5\nv_lshlrev_b32 v5, 4, v5\nv_lshl_add_u32 v211, v2, 8, v5\nv_xor_b32 v5, v1, v2\nv_xor_b32 v5, 8, v5\n"
"v_lshlrev_b32 v5, 4, v5\nv_lshl_add_u32 v212, v2, 8, v5\nv_xor_b32 v5, v1, v2\nv_xor_b32 v5, 12, v5\nv_lshlrev_b32 v5, 4, v5\n"
"v_lshl_add_u32 v213, v2, 8, v5\nv_xor_b32 v5, 0, v4\nv_lshlrev_b32 v5, 4, v5\nv_lshl_add_u32 v5, v2, 6, v5\nv_lshl_add_u32 v214, v3, 8, v5\n"
"v_xor_b32 v5, 2, v4\nv_lshlrev_b32 v5, 4, v5\nv_lshl_add_u32 v5, v2, 6, v5\nv_lshl_add_u32 v215, v3, 8, v5\n"
"buffer_load_dword v252, v210, s[68:71], s96 offen offset:0\nbuffer_load_dword v252, v210, s[68:71], s96 offen offset:1024\n"
"s_add_u32 s76, s90, 0x80000\ns_and_b32 s76, s76, 0x3fffff\ns_add_u32 m0, s63, s86\ns_nop 0\nDL 242,40,76,0\nDL 243,40,76,1024\nDL 244,40,76,2048\n"
"DL 245,40,76,3072\ns_add_u32 s77, s91, 0x80\ns_and_b32 s77, s77, 0x3ff\ns_add_u32 m0, s73, s86\ns_nop 0\nDL 246,36,77,0\nDL 247,36,77,1024\n"
"DL 248,36,77,2048\ns_mov_b32 s88, 0x11c000\nbuffer_load_dwordx2 v[180:181], off, s[56:59], s88 sc1\ns_lshl_b32 s88, s83, 14\nv_bfe_u32 v7, v0, 4, 1\n"
"v_add_u32 v5, 0, v2\nv_xor_b32 v5, v5, v1\nv_lshlrev_b32 v5, 4, v5\nv_lshl_add_u32 v5, v1, 8, v5\nv_add_u32 v204, s88, v5\nv_add_u32 v5, 4, v2\n"
"v_xor_b32 v5, v5, v1\nv_lshlrev_b32 v5, 4, v5\nv_lshl_add_u32 v5, v1, 8, v5\nv_add_u32 v205, s88, v5\nv_add_u32 v5, 8, v2\nv_xor_b32 v5, v5, v1\n"
"v_lshlrev_b32 v5, 4, v5\nv_lshl_add_u32 v5, v1, 8, v5\nv_add_u32 v206, s88, v5\nv_add_u32 v5, 12, v2\nv_xor_b32 v5, v5, v1\nv_lshlrev_b32 v5, 4, v5\n"
"v_lshl_add_u32 v5, v1, 8, v5\nv_add_u32 v207, s88, v5\nv_xor_b32 v5, 0, v7\nv_lshlrev_b32 v5, 5, v5\nv_lshl_add_u32 v5, v4, 3, v5\n"
"v_lshl_add_u32 v5, v3, 6, v5\nv_add_u32 v5, s88, v5\nv_lshl_add_u32 v208, v2, 10, v5\nv_xor_b32 v5, 1, v7\nv_lshlrev_b32 v5, 5, v5\n"
"v_lshl_add_u32 v5, v4, 3, v5\nv_lshl_add_u32 v5, v3, 6, v5\nv_add_u32 v5, s88, v5\nv_lshl_add_u32 v209, v2, 10, v5\ns_lshl_b32 s89, s84, 14\n"
"s_add_u32 s89, s89, 8192\nv_lshrrev_b32 v6, 1, v1\nv_add_u32 v5, 0, v2\nv_xor_b32 v5, v5, v6\nv_lshlrev_b32 v5, 4, v5\nv_lshl_add_u32 v5, v1, 7, v5\n"
"v_add_u32 v240, s89, v5\nv_add_u32 v5, 4, v2\nv_xor_b32 v5, v5, v6\nv_lshlrev_b32 v5, 4, v5\nv_lshl_add_u32 v5, v1, 7, v5\nv_add_u32 v241, s89, v5\n"
"v_lshrrev_b32 v5, 4, v0\nv_xor_b32 v5, v5, v0\nv_bfe_u32 v5, v5, 1, 1\nv_subrev_u32 v5, 1, v5\nv_and_b32 v5, 0x3f803f80, v5\nAW 180,5\nAW 181,5\n"
"AW 182,5\nAW 183,5\nv_and_b32 v5, 1, v0\nv_mul_u32_u24 v5, 0xaaaa, v5\nv_add_u32 v251, 0x4444, v5\ns_bitcmp0_b32 %13, 0\ns_cbranch_scc1 .Lfa_x\n"
"s_branch .Lfa_y\n.p2align 12\n.Lfa_y:\ns_and_b32 s88, s62, 7\ns_lshl_b32 s89, s88, 1\ns_add_u32 s89, s89, s84\ns_lshl_b32 s89, s89, 18\n"
"v_lshrrev_b32 v5, 1, v0\nv_lshlrev_b32 v5, 13, v5\nv_and_b32 v6, 1, v0\nv_lshl_add_u32 v5, v6, 7, v5\nv_add_u32 v160, s89, v5\nv_xor_b32 v6, 0, v2\n"
"v_lshl_add_u32 v164, v6, 24, v160\nv_xor_b32 v6, 1, v2\nv_lshl_add_u32 v165, v6, 24, v160\nv_xor_b32 v6, 2, v2\nv_lshl_add_u32 v166, v6, 24, v160\n"
"v_xor_b32 v6, 3, v2\nv_lshl_add_u32 v167, v6, 24, v160\ns_lshl_b32 s89, s88, 12\nv_and_b32 v5, 31, v0\nv_lshlrev_b32 v5, 7, v5\n"
"v_lshrrev_b32 v6, 5, v0\nv_lshl_add_u32 v5, v6, 16, v5\nv_add_u32 v161, s89, v5\nv_add_u32 v162, 0x40000, v161\ns_lshl_b32 s88, s83, 12\n"
"s_xor_b32 s89, s88, 0x1000\nv_add_u32 v196, s88, v240\nv_add_u32 v198, s89, v240\nv_add_u32 v197, s88, v241\nv_add_u32 v199, s89, v241\n"
"v_mbcnt_lo_u32_b32 v253, -1, 0\nv_mbcnt_hi_u32_b32 v253, -1, v253\ns_lshl_b32 s88, s83, 14\nv_and_b32 v202, 15, v253\n"
"v_lshl_add_u32 v203, v202, 4, s88\nv_lshrrev_b32 v202, 5, v253\nv_lshl_add_u32 v203, v202, 10, v203\nv_bfe_u32 v253, v253, 4, 1\n"
"v_lshl_add_u32 v203, v253, 12, v203\nv_lshl_add_u32 v202, v253, 3, v203\n"
"s_mov_b64 exec, 0\ns_cmp_eq_u32 s60, 0\ns_cbranch_scc1 .Lfa_w0_0\ns_cmp_eq_u32 s60, 1\ns_cbranch_scc1 .Lfa_w0_1\ns_cmp_eq_u32 s60, 2\ns_cbranch_scc1 .Lfa_w0_2\ns_branch .Lfa_w0_3\n.Lfa_wr0:\ns_mov_b64 exec, -1\ns_nop 4\n"
"s_waitcnt vmcnt(16) lgkmcnt(0)\ns_barrier\nRa 80,196,34816\nRa 84,240,36864\nRa 88,198,34816\nRa 100,197,34816\nRa 104,241,36864\nRa 108,199,34816\n"
"v_add_u32 v196, 0x10000, v196\nRa 92,196,0\nv_add_u32 v198, 0x10000, v198\nRa 96,198,0\nv_add_u32 v197, 0x10000, v197\nRa 112,197,0\n"
"v_add_u32 v199, 0x10000, v199\nRa 116,199,0\nTa 184,202,32768\nTa 186,202,33280\nTa 188,202,34816\nTa 190,202,35328\nTa 192,202,32776\n"
"Ta 194,202,33288\nTa 196,202,34824\nTa 198,202,35336\nTa 200,202,33024\nTa 202,202,33536\nTa 204,202,35072\nTa 206,202,35584\nTa 208,202,33032\n"
"Ta 210,202,33544\nTa 212,202,35080\nTa 214,202,35592\ns_add_u32 s76, s90, 0x80080\ns_and_b32 s76, s76, 0x3fffff\ns_add_u32 m0, s63, 0x20000\ns_nop 0\n"
"DL 242,40,76,0\nDL 243,40,76,1024\nDL 244,40,76,2048\nDL 245,40,76,3072\ns_add_u32 s77, s91, 0x100\ns_and_b32 s77, s77, 0x3ff\n"
"s_add_u32 m0, s74, 0x20000\ns_nop 0\nDL 249,36,77,0\nDL 250,36,77,1024\ns_waitcnt vmcnt(16) lgkmcnt(0)\ns_barrier\nZvaa 0,184,80\n"
"s_add_u32 s76, s90, 0x100000\nRa 132,196,2048\nv_add_u32 v203, 0x10000, v203\nMvaa 0,188,100\ns_and_b32 s76, s76, 0x3fffff\nRa 136,198,2048\n"
"v_add_u32 v240, 0x10000, v240\nZvaa 32,184,84\ns_add_u32 m0, s63, 0x0\nRa 152,197,2048\nv_add_u32 v241, 0x10000, v241\nMvaa 32,188,104\n"
"DL 242,40,76,0\nZvaa 64,184,88\nRa 156,199,2048\nv_add_u32 v202, 0x10000, v202\nMvaa 64,188,108\nDL 243,40,76,1024\nZvaa 96,184,92\nTa 216,203,0\n"
"Mvaa 96,188,112\nDL 244,40,76,2048\nZvaa 128,184,96\nTa 218,203,512\nMvaa 128,188,116\nDL 245,40,76,3072\nZvaa 4,192,80\ns_add_u32 s77, s91, 0x100\n"
"Ta 220,203,2048\nMvaa 4,196,100\ns_and_b32 s77, s77, 0x3ff\nTa 222,203,2560\nZvaa 36,192,84\ns_add_u32 m0, s73, 0x0\nTa 224,203,8\nMvaa 36,196,104\n"
"DL 246,36,77,0\nZvaa 68,192,88\nTa 226,203,520\nMvaa 68,196,108\nDL 247,36,77,1024\nZvaa 100,192,92\nTa 228,203,2056\nMvaa 100,196,112\n"
"DL 248,36,77,2048\nZvaa 132,192,96\ns_add_u32 s76, s90, 0x100080\nTa 230,203,2568\nMvaa 132,196,116\ns_and_b32 s76, s76, 0x3fffff\nTa 232,203,256\n"
"Zvaa 8,200,80\ns_add_u32 m0, s63, 0x8000\nTa 234,203,768\nMvaa 8,204,100\nDL 242,40,76,0\nZvaa 40,200,84\nTa 236,203,2304\nMvaa 40,204,104\n"
"DL 243,40,76,1024\nZvaa 72,200,88\nTa 238,203,2816\nMvaa 72,204,108\nDL 244,40,76,2048\nZvaa 104,200,92\nTa 240,203,264\nMvaa 104,204,112\n"
"DL 245,40,76,3072\nZvaa 136,200,96\ns_add_u32 s77, s91, 0x180\nTa 242,203,776\nMvaa 136,204,116\ns_and_b32 s77, s77, 0x3ff\nTa 244,203,2312\n"
"Zvaa 12,208,80\ns_add_u32 m0, s74, 0x8000\nTa 246,203,2824\nMvaa 12,212,100\nDL 249,36,77,0\nZvaa 44,208,84\nMvaa 44,212,104\nDL 250,36,77,1024\n"
"Zvaa 76,208,88\nMvaa 76,212,108\nZvaa 108,208,92\nMvaa 108,212,112\nZvaa 140,208,96\ns_waitcnt vmcnt(20) lgkmcnt(0)\nMvaa 140,212,116\ns_barrier\n"
"Zvaa 16,216,80\ns_add_u32 s76, s90, 0x180000\nRa 120,196,34816\nMvaa 16,220,100\ns_and_b32 s76, s76, 0x3fffff\nRa 124,240,36864\nZvaa 48,216,84\n"
"s_add_u32 m0, s63, 0x10000\nRa 128,198,34816\nv_add_u32 v196, 0x10000, v196\nMvaa 48,220,104\nDL 242,40,76,0\nZvaa 80,216,88\nRa 140,197,34816\n"
"v_add_u32 v198, 0x10000, v198\nMvaa 80,220,108\nRa 144,241,36864\nZvaa 112,216,92\nRa 148,199,34816\nv_add_u32 v197, 0x10000, v197\nMvaa 112,220,112\n"
"DL 243,40,76,1024\nZvaa 144,216,96\nTa 248,202,32768\nv_add_u32 v199, 0x10000, v199\nMvaa 144,220,116\nTa 250,202,33280\nZvaa 20,224,80\n"
"Ta 252,202,34816\nMvaa 20,228,100\nDL 244,40,76,2048\nZvaa 52,224,84\nTa 254,202,35328\nMvaa 52,228,104\nTv 216,202,32776\nZvaa 84,224,88\n"
"Tv 218,202,33288\nMvaa 84,228,108\nDL 245,40,76,3072\nZvaa 116,224,92\ns_add_u32 s77, s91, 0x180\nTv 220,202,34824\nMvaa 116,228,112\n"
"s_and_b32 s77, s77, 0x3ff\nTv 222,202,35336\nZvaa 148,224,96\ns_add_u32 m0, s73, 0x10000\nTv 224,202,33024\nMvaa 148,228,116\nDL 246,36,77,0\n"
"Zvaa 24,232,80\nTv 226,202,33536\nMvaa 24,236,100\nTv 228,202,35072\nZvaa 56,232,84\nTv 230,202,35584\nMvaa 56,236,104\nDL 247,36,77,1024\n"
"Zvaa 88,232,88\nTv 232,202,33032\nMvaa 88,236,108\nTv 234,202,33544\nZvaa 120,232,92\nTv 236,202,35080\nMvaa 120,236,112\nDL 248,36,77,2048\n"
"Zvaa 152,232,96\nTv 238,202,35592\nMvaa 152,236,116\nZvaa 28,240,80\nv_add_u32 v202, 0x10000, v202\nMvaa 28,244,100\n"
"buffer_load_dword v252, v164, s[40:43], 0 offen offset:512\nZvaa 60,240,84\nMvaa 60,244,104\nZvaa 92,240,88\nMvaa 92,244,108\nZvaa 124,240,92\n"
"Mvaa 124,244,112\nZvaa 156,240,96\ns_waitcnt vmcnt(21) lgkmcnt(0)\nMvaa 156,244,116\ns_barrier\nMvaa 0,248,120\ns_add_u32 s76, s90, 0x180080\n"
"Ra 92,196,2048\nv_add_u32 v240, 0xffff0000, v240\nMvaa 0,252,140\ns_and_b32 s76, s76, 0x3fffff\nRa 96,198,2048\nv_add_u32 v241, 0xffff0000, v241\n"
"Mvaa 32,248,124\ns_add_u32 m0, s63, 0x18000\nRa 112,197,2048\nv_add_u32 v196, 0xfffe0000, v196\nMvaa 32,252,144\nDL 242,40,76,0\nMvaa 64,248,128\n"
"Ra 116,199,2048\nv_add_u32 v198, 0xfffe0000, v198\nMvaa 64,252,148\nTa 184,202,0\nv_add_u32 v197, 0xfffe0000, v197\nMvaa 96,248,132\nTa 186,202,512\n"
"v_add_u32 v199, 0xfffe0000, v199\nMvaa 96,252,152\nDL 243,40,76,1024\nMvaa 128,248,136\nTa 188,202,2048\nMvaa 128,252,156\nTa 190,202,2560\n"
"Mvva 4,216,120\nTa 192,202,8\nMvva 4,220,140\nDL 244,40,76,2048\nMvva 36,216,124\nTa 194,202,520\nMvva 36,220,144\nTa 196,202,2056\nMvva 68,216,128\n"
"Ta 198,202,2568\nMvva 68,220,148\nDL 245,40,76,3072\nMvva 100,216,132\ns_add_u32 s77, s91, 0x200\nTa 200,202,256\nMvva 100,220,152\n"
"s_and_b32 s77, s77, 0x3ff\nTa 202,202,768\nMvva 132,216,136\ns_add_u32 m0, s74, 0x18000\nTa 204,202,2304\nMvva 132,220,156\nDL 249,36,77,0\n"
"Mvva 8,224,120\nTa 206,202,2816\nMvva 8,228,140\nTa 208,202,264\nMvva 40,224,124\nTa 210,202,776\nMvva 40,228,144\nDL 250,36,77,1024\n"
"Mvva 72,224,128\nTa 212,202,2312\nMvva 72,228,148\nTa 214,202,2824\nMvva 104,224,132\nMvva 104,228,152\nv_add_u32 v202, 0xfffe0000, v202\n"
"Mvva 136,224,136\nMvva 136,228,156\nMvva 12,232,120\nMvva 12,236,140\nMvva 44,232,124\nMvva 44,236,144\nMvva 76,232,128\nMvva 76,236,148\n"
"Mvva 108,232,132\nMvva 108,236,152\nMvva 140,232,136\ns_waitcnt vmcnt(20) lgkmcnt(0)\nMvva 140,236,156\ns_barrier\nMvaa 16,184,120\n"
"s_add_u32 s76, s90, 0x200000\nRa 80,196,2048\nMvaa 16,188,140\ns_and_b32 s76, s76, 0x3fffff\nRa 84,240,4096\nMvaa 48,184,124\n"
"s_add_u32 m0, s63, 0x20000\nRa 88,198,2048\nMvaa 48,188,144\nDL 242,40,76,0\nMvaa 80,184,128\nRa 100,197,2048\nMvaa 80,188,148\nRa 104,241,4096\n"
"Mvaa 112,184,132\nRa 108,199,2048\nMvaa 112,188,152\nDL 243,40,76,1024\nMvaa 144,184,136\nTa 216,202,0\nMvaa 144,188,156\nTa 218,202,512\n"
"Mvaa 20,192,120\nTa 220,202,2048\nMvaa 20,196,140\nDL 244,40,76,2048\nMvaa 52,192,124\nTa 222,202,2560\nMvaa 52,196,144\nTa 224,202,8\n"
"Mvaa 84,192,128\nTa 226,202,520\nMvaa 84,196,148\nDL 245,40,76,3072\nMvaa 116,192,132\ns_add_u32 s77, s91, 0x200\nTa 228,202,2056\nMvaa 116,196,152\n"
"s_and_b32 s77, s77, 0x3ff\nTa 230,202,2568\nMvaa 148,192,136\ns_add_u32 m0, s73, 0x20000\nTa 232,202,256\nMvaa 148,196,156\nDL 246,36,77,0\n"
"Mvaa 24,200,120\nTa 234,202,768\nMvaa 24,204,140\nTa 236,202,2304\nMvaa 56,200,124\nTa 238,202,2816\nMvaa 56,204,144\nDL 247,36,77,1024\n"
"Mvaa 88,200,128\nTa 240,202,264\nMvaa 88,204,148\nTa 242,202,776\nMvaa 120,200,132\nTa 244,202,2312\nMvaa 120,204,152\nDL 248,36,77,2048\n"
"Mvaa 152,200,136\nTa 246,202,2824\nMvaa 152,204,156\nMvaa 28,208,120\nMvaa 28,212,140\nbuffer_load_dword v252, v165, s[40:43], 0 offen offset:512\n"
"Mvaa 60,208,124\nMvaa 60,212,144\nMvaa 92,208,128\nMvaa 92,212,148\nMvaa 124,208,132\nMvaa 124,212,152\nMvaa 156,208,136\n"
"s_waitcnt vmcnt(22) lgkmcnt(0)\nMvaa 156,212,156\ns_barrier\nMvaa 0,216,80\ns_add_u32 s76, s90, 0x200080\nRa 132,196,34816\n"
"v_add_u32 v240, 0x10000, v240\nMvaa 0,220,100\ns_and_b32 s76, s76, 0x3fffff\nRa 136,198,34816\nv_add_u32 v241, 0x10000, v241\nMvaa 32,216,84\n"
"s_add_u32 m0, s63, 0x0\nRa 152,197,34816\nv_add_u32 v196, 0x10000, v196\nMvaa 32,220,104\nDL 242,40,76,0\nMvaa 64,216,88\nRa 156,199,34816\n"
"v_add_u32 v198, 0x10000, v198\nMvaa 64,220,108\nTa 248,202,32768\nv_add_u32 v197, 0x10000, v197\nMvaa 96,216,92\nTa 250,202,33280\n"
"v_add_u32 v199, 0x10000, v199\nMvaa 96,220,112\nDL 243,40,76,1024\nMvaa 128,216,96\nTa 252,202,34816\nMvaa 128,220,116\nTa 254,202,35328\n"
"Mvaa 4,224,80\nTv 216,202,32776\nMvaa 4,228,100\nDL 244,40,76,2048\nMvaa 36,224,84\nTv 218,202,33288\nMvaa 36,228,104\nTv 220,202,34824\n"
"Mvaa 68,224,88\nTv 222,202,35336\nMvaa 68,228,108\nDL 245,40,76,3072\nMvaa 100,224,92\ns_add_u32 s77, s91, 0x280\nTv 224,202,33024\nMvaa 100,228,112\n"
"s_and_b32 s77, s77, 0x3ff\nTv 226,202,33536\nMvaa 132,224,96\ns_add_u32 m0, s74, 0x0\nTv 228,202,35072\nMvaa 132,228,116\nDL 249,36,77,0\n"
"Mvaa 8,232,80\nTv 230,202,35584\nMvaa 8,236,100\nTv 232,202,33032\nMvaa 40,232,84\nTv 234,202,33544\nMvaa 40,236,104\nDL 250,36,77,1024\n"
"Mvaa 72,232,88\nTv 236,202,35080\nMvaa 72,236,108\nTv 238,202,35592\nMvaa 104,232,92\nMvaa 104,236,112\n"
"buffer_load_dword v252, v210, s[68:71], s96 offen offset:0\nMvaa 136,232,96\nv_add_u32 v202, 0x10000, v202\nMvaa 136,236,116\nMvaa 12,240,80\n"
"Mvaa 12,244,100\nbuffer_load_dword v252, v210, s[68:71], s96 offen offset:1024\nMvaa 44,240,84\nMvaa 44,244,104\nMvaa 76,240,88\nMvaa 76,244,108\n"
"Mvaa 108,240,92\nMvaa 108,244,112\nMvaa 140,240,96\ns_waitcnt vmcnt(23) lgkmcnt(0)\nMvaa 140,244,116\ns_barrier\nMvaa 16,248,80\n"
"s_add_u32 s76, s90, 0x280000\ns_waitcnt vmcnt(49)\nMvaa 16,252,100\ns_and_b32 s76, s76, 0x3fffff\nRa 120,196,2048\nv_readfirstlane_b32 s86, v180\n"
"Mvaa 48,248,84\ns_add_u32 m0, s63, 0x8000\nRa 124,240,4096\nv_readfirstlane_b32 s87, v181\nMvaa 48,252,104\nDL 242,40,76,0\nMvaa 80,248,88\n"
"Ra 128,198,2048\ns_nop 3\nMvaa 80,252,108\ns_mul_i32 s87, s87, 0x19660d\nRa 140,197,2048\nMvaa 112,248,92\ns_add_u32 s87, s87, 0x3c6ef35f\n"
"Ra 144,241,4096\nMvaa 112,252,112\ns_mul_i32 s86, s86, 0x41c64e6d\nDL 243,40,76,1024\nMvaa 144,248,96\ns_add_u32 s86, s86, 0x9e3779b9\n"
"Ra 148,199,2048\nMvaa 144,252,116\n.Lfa_pd785:\ns_cbranch_execz .Lfa_wr0\nTa 184,202,0\nMvva 20,216,80\nTa 186,202,512\nMvva 20,220,100\n"
"DL 244,40,76,2048\nMvva 52,216,84\nTa 188,202,2048\nMvva 52,220,104\nTa 190,202,2560\nMvva 84,216,88\nTa 192,202,8\nMvva 84,220,108\n"
"DL 245,40,76,3072\nMvva 116,216,92\ns_add_u32 s77, s91, 0x280\nTa 194,202,520\nMvva 116,220,112\ns_and_b32 s77, s77, 0x3ff\nTa 196,202,2056\n"
"Mvva 148,216,96\ns_add_u32 m0, s73, 0x8000\nTa 198,202,2568\nMvva 148,220,116\nDL 246,36,77,0\nMvva 24,224,80\nTa 200,202,256\nMvva 24,228,100\n"
"Ta 202,202,768\nMvva 56,224,84\nTa 204,202,2304\nMvva 56,228,104\nDL 247,36,77,1024\nMvva 88,224,88\nTa 206,202,2816\nMvva 88,228,108\n"
"Ta 208,202,264\nMvva 120,224,92\nTa 210,202,776\nMvva 120,228,112\nDL 248,36,77,2048\nMvva 152,224,96\nTa 212,202,2312\nMvva 152,228,116\n"
"Ta 214,202,2824\nMvva 28,232,80\nMvva 28,236,100\nbuffer_load_dword v252, v166, s[40:43], 0 offen offset:512\nMvva 60,232,84\nMvva 60,236,104\n"
"Mvva 92,232,88\nMvva 92,236,108\nMvva 124,232,92\nMvva 124,236,112\nMvva 156,232,96\ns_waitcnt vmcnt(24) lgkmcnt(0)\nMvva 156,236,116\ns_barrier\n"
"s_mov_b64 exec, 0\ns_cmp_eq_u32 s60, 0\ns_cbranch_scc1 .Lfa_w1_0\ns_cmp_eq_u32 s60, 1\ns_cbranch_scc1 .Lfa_w1_1\ns_cmp_eq_u32 s60, 2\ns_cbranch_scc1 .Lfa_w1_2\ns_branch .Lfa_w1_3\n.Lfa_wr1:\ns_mov_b64 exec, -1\ns_nop 4\n"
"Mvaa 0,184,120\ns_add_u32 s76, s90, 0x280080\nRa 92,196,34816\nv_add_u32 v240, 0x10000, v240\nMvaa 0,188,140\ns_and_b32 s76, s76, 0x3fffff\n"
"Ra 96,198,34816\nv_add_u32 v241, 0x10000, v241\nMvaa 32,184,124\ns_add_u32 m0, s63, 0x10000\nRa 112,197,34816\nv_add_u32 v196, 0x10000, v196\n"
"Mvaa 32,188,144\nDL 242,40,76,0\nMvaa 64,184,128\nRa 116,199,34816\nv_add_u32 v198, 0x10000, v198\nMvaa 64,188,148\nTa 216,202,32768\n"
"v_add_u32 v197, 0x10000, v197\nMvaa 96,184,132\nTa 218,202,33280\nv_add_u32 v199, 0x10000, v199\nMvaa 96,188,152\nDL 243,40,76,1024\n"
"Mvaa 128,184,136\nTa 220,202,34816\nMvaa 128,188,156\nTa 222,202,35328\nMvaa 4,192,120\nTa 224,202,32776\nMvaa 4,196,140\nDL 244,40,76,2048\n"
"Mvaa 36,192,124\nTa 226,202,33288\nMvaa 36,196,144\nTa 228,202,34824\nMvaa 68,192,128\nTa 230,202,35336\nMvaa 68,196,148\nDL 245,40,76,3072\n"
"Mvaa 100,192,132\ns_add_u32 s77, s91, 0x300\nTa 232,202,33024\nMvaa 100,196,152\ns_and_b32 s77, s77, 0x3ff\nTa 234,202,33536\nMvaa 132,192,136\n"
"s_add_u32 m0, s74, 0x10000\nTa 236,202,35072\nMvaa 132,196,156\nDL 249,36,77,0\nMvaa 8,200,120\nTa 238,202,35584\nMvaa 8,204,140\nTa 240,202,33032\n"
"Mvaa 40,200,124\nTa 242,202,33544\nMvaa 40,204,144\nDL 250,36,77,1024\nMvaa 72,200,128\nTa 244,202,35080\nMvaa 72,204,148\nTa 246,202,35592\n"
"Mvaa 104,200,132\nMvaa 104,204,152\nv_add_u32 v202, 0x10000, v202\nMvaa 136,200,136\nMvaa 136,204,156\nMvaa 12,208,120\nMvaa 12,212,140\n"
"Mvaa 44,208,124\nMvaa 44,212,144\nMvaa 76,208,128\nMvaa 76,212,148\nMvaa 108,208,132\nMvaa 108,212,152\nMvaa 140,208,136\n"
"s_waitcnt vmcnt(23) lgkmcnt(0)\nMvaa 140,212,156\ns_barrier\nMvaa 16,216,120\ns_add_u32 s76, s90, 0x300000\nRa 80,196,2048\nMvaa 16,220,140\n"
"s_and_b32 s76, s76, 0x3fffff\nRa 84,240,4096\nMvaa 48,216,124\ns_add_u32 m0, s63, 0x18000\nRa 88,198,2048\nv_add_u32 v196, 0xfffe0000, v196\n"
"Mvaa 48,220,144\nDL 242,40,76,0\nMvaa 80,216,128\nRa 100,197,2048\nv_add_u32 v198, 0xfffe0000, v198\nMvaa 80,220,148\nRa 104,241,4096\n"
"Mvaa 112,216,132\nRa 108,199,2048\nv_add_u32 v197, 0xfffe0000, v197\nMvaa 112,220,152\nDL 243,40,76,1024\nMvaa 144,216,136\nTa 248,202,0\n"
"v_add_u32 v199, 0xfffe0000, v199\nMvaa 144,220,156\nTa 250,202,512\nMvaa 20,224,120\nTa 252,202,2048\nMvaa 20,228,140\nDL 244,40,76,2048\n"
"Mvaa 52,224,124\nTa 254,202,2560\nMvaa 52,228,144\nTv 216,202,8\nMvaa 84,224,128\nTv 218,202,520\nMvaa 84,228,148\nDL 245,40,76,3072\n"
"Mvaa 116,224,132\ns_add_u32 s77, s91, 0x300\nTv 220,202,2056\nMvaa 116,228,152\ns_and_b32 s77, s77, 0x3ff\nTv 222,202,2568\nMvaa 148,224,136\n"
"s_add_u32 m0, s73, 0x18000\nTv 224,202,256\nMvaa 148,228,156\nDL 246,36,77,0\nMvaa 24,232,120\nTv 226,202,768\nMvaa 24,236,140\nTv 228,202,2304\n"
"Mvaa 56,232,124\nTv 230,202,2816\nMvaa 56,236,144\nDL 247,36,77,1024\nMvaa 88,232,128\nTv 232,202,264\nMvaa 88,236,148\nTv 234,202,776\n"
"Mvaa 120,232,132\nTv 236,202,2312\nMvaa 120,236,152\nDL 248,36,77,2048\nMvaa 152,232,136\nTv 238,202,2824\nMvaa 152,236,156\nMvaa 28,240,120\n"
"v_add_u32 v202, 0xfffe0000, v202\nMvaa 28,244,140\nbuffer_load_dword v252, v167, s[40:43], 0 offen offset:512\nMvaa 60,240,124\nMvaa 60,244,144\n"
"Mvaa 92,240,128\nMvaa 92,244,148\nMvaa 124,240,132\nMvaa 124,244,152\nMvaa 156,240,136\ns_waitcnt vmcnt(24) lgkmcnt(0)\nMvaa 156,244,156\ns_barrier\n"
"Mvaa 0,248,80\ns_add_u32 s76, s90, 0x300080\nRa 132,196,2048\nv_add_u32 v240, 0xfffe0000, v240\nMvaa 0,252,100\ns_and_b32 s76, s76, 0x3fffff\n"
"Ra 136,198,2048\nv_add_u32 v241, 0xfffe0000, v241\nMvaa 32,248,84\ns_add_u32 m0, s63, 0x20000\nRa 152,197,2048\nMvaa 32,252,104\nDL 242,40,76,0\n"
"Mvaa 64,248,88\nRa 156,199,2048\nMvaa 64,252,108\nTa 184,202,0\nMvaa 96,248,92\nTa 186,202,512\nMvaa 96,252,112\nDL 243,40,76,1024\nMvaa 128,248,96\n"
"Ta 188,202,2048\nMvaa 128,252,116\nTa 190,202,2560\nMvva 4,216,80\nTa 192,202,8\nMvva 4,220,100\nDL 244,40,76,2048\nMvva 36,216,84\nTa 194,202,520\n"
"Mvva 36,220,104\nTa 196,202,2056\nMvva 68,216,88\nTa 198,202,2568\nMvva 68,220,108\nDL 245,40,76,3072\nMvva 100,216,92\ns_add_u32 s77, s91, 0x380\n"
"Ta 200,202,256\nMvva 100,220,112\ns_and_b32 s77, s77, 0x3ff\nTa 202,202,768\nMvva 132,216,96\ns_add_u32 m0, s74, 0x20000\nTa 204,202,2304\n"
"Mvva 132,220,116\nDL 249,36,77,0\nMvva 8,224,80\nTa 206,202,2816\nMvva 8,228,100\nTa 208,202,264\nMvva 40,224,84\nTa 210,202,776\nMvva 40,228,104\n"
"DL 250,36,77,1024\nMvva 72,224,88\nTa 212,202,2312\nMvva 72,228,108\nTa 214,202,2824\nMvva 104,224,92\nMvva 104,228,112\nMvva 136,224,96\n"
"Mvva 136,228,116\nMvva 12,232,80\nMvva 12,236,100\nMvva 44,232,84\nMvva 44,236,104\nMvva 76,232,88\nMvva 76,236,108\nMvva 108,232,92\n"
"Mvva 108,236,112\nMvva 140,232,96\ns_waitcnt vmcnt(21) lgkmcnt(0)\nMvva 140,236,116\ns_barrier\nMvaa 16,184,80\ns_add_u32 s76, s90, 0x380000\n"
"Ra 120,196,34816\nMvaa 16,188,100\ns_and_b32 s76, s76, 0x3fffff\nRa 124,240,36864\nMvaa 48,184,84\ns_add_u32 m0, s63, 0x0\nRa 128,198,34816\n"
"v_add_u32 v196, 0x10000, v196\nMvaa 48,188,104\nDL 242,40,76,0\nMvaa 80,184,88\nRa 140,197,34816\nv_add_u32 v198, 0x10000, v198\nMvaa 80,188,108\n"
"Ra 144,241,36864\nMvaa 112,184,92\nRa 148,199,34816\nv_add_u32 v197, 0x10000, v197\nMvaa 112,188,112\nDL 243,40,76,1024\nMvaa 144,184,96\n"
"Ta 216,202,32768\nv_add_u32 v199, 0x10000, v199\nMvaa 144,188,116\nTa 218,202,33280\nMvaa 20,192,80\nTa 220,202,34816\nMvaa 20,196,100\n"
"DL 244,40,76,2048\nMvaa 52,192,84\nTa 222,202,35328\nMvaa 52,196,104\nTa 224,202,32776\nMvaa 84,192,88\nTa 226,202,33288\nMvaa 84,196,108\n"
"DL 245,40,76,3072\nMvaa 116,192,92\ns_add_u32 s77, s91, 0x380\nTa 228,202,34824\nMvaa 116,196,112\ns_and_b32 s77, s77, 0x3ff\nTa 230,202,35336\n"
"Mvaa 148,192,96\ns_add_u32 m0, s73, 0x0\nTa 232,202,33024\nMvaa 148,196,116\nDL 246,36,77,0\nMvaa 24,200,80\nTa 234,202,33536\nMvaa 24,204,100\n"
"Ta 236,202,35072\nMvaa 56,200,84\nTa 238,202,35584\nMvaa 56,204,104\nDL 247,36,77,1024\nMvaa 88,200,88\nTa 240,202,33032\nMvaa 88,204,108\n"
"Ta 242,202,33544\nMvaa 120,200,92\nTa 244,202,35080\nMvaa 120,204,112\nDL 248,36,77,2048\nMvaa 152,200,96\nTa 246,202,35592\nMvaa 152,204,116\n"
"Mvaa 28,208,80\nv_add_u32 v202, 0x10000, v202\nMvaa 28,212,100\nMvaa 60,208,84\nMvaa 60,212,104\nMvaa 92,208,88\nMvaa 92,212,108\nMvaa 124,208,92\n"
"Mvaa 124,212,112\nMvaa 156,208,96\ns_waitcnt vmcnt(21) lgkmcnt(0)\nMvaa 156,212,116\ns_barrier\nMvaa 0,216,120\ns_add_u32 s76, s90, 0x380080\n"
"Ra 92,196,2048\nv_add_u32 v240, 0x10000, v240\nMvaa 0,220,140\ns_and_b32 s76, s76, 0x3fffff\nRa 96,198,2048\nv_add_u32 v241, 0x10000, v241\n"
"Mvaa 32,216,124\ns_add_u32 m0, s63, 0x8000\nRa 112,197,2048\nMvaa 32,220,144\nDL 242,40,76,0\nMvaa 64,216,128\nRa 116,199,2048\nMvaa 64,220,148\n"
"Ta 248,202,0\nMvaa 96,216,132\nTa 250,202,512\nMvaa 96,220,152\nDL 243,40,76,1024\nMvaa 128,216,136\nTa 252,202,2048\nMvaa 128,220,156\n"
"Ta 254,202,2560\nMvaa 4,224,120\nTv 216,202,8\nMvaa 4,228,140\nDL 244,40,76,2048\nMvaa 36,224,124\nTv 218,202,520\nMvaa 36,228,144\nTv 220,202,2056\n"
"Mvaa 68,224,128\nTv 222,202,2568\nMvaa 68,228,148\nDL 245,40,76,3072\nMvaa 100,224,132\nTv 224,202,256\nMvaa 100,228,152\nTv 226,202,768\n"
"Mvaa 132,224,136\nTv 228,202,2304\nMvaa 132,228,156\nTv 230,202,2816\nMvaa 8,232,120\nTv 232,202,264\nMvaa 8,236,140\nTv 234,202,776\n"
"Mvaa 40,232,124\nTv 236,202,2312\nMvaa 40,236,144\nTv 238,202,2824\nMvaa 72,232,128\nMvaa 72,236,148\nMvaa 104,232,132\nMvaa 104,236,152\n"
"Mvaa 136,232,136\nMvaa 136,236,156\nMvaa 12,240,120\nMvaa 12,244,140\nMvaa 44,240,124\nMvaa 44,244,144\nMvaa 76,240,128\nMvaa 76,244,148\n"
"Mvaa 108,240,132\nMvaa 108,244,152\nMvaa 140,240,136\ns_waitcnt vmcnt(18) lgkmcnt(0)\nMvaa 140,244,156\ns_barrier\nMvaa 16,248,120\n"
"s_add_u32 s78, s97, 0x0\nRa 80,196,34816\nv_mbcnt_lo_u32_b32 v200, -1, 0\nMvaa 16,252,140\ns_and_b32 s78, s78, 0x1ffff\nRa 84,240,36864\n"
"v_mbcnt_hi_u32_b32 v200, -1, v200\nMvaa 48,248,124\nbuffer_load_dwordx4 a[160:163], v210, s[44:47], s78 offen offset:0 nt\nMvaa 48,252,144\n"
"Ra 88,198,34816\nv_add_u32 v196, 0x10000, v196\nMvaa 80,248,128\nRa 100,197,34816\nv_lshlrev_b32 v200, 4, v200\nMvaa 80,252,148\nRa 104,241,36864\n"
"v_add_u32 v198, 0x10000, v198\nMvaa 112,248,132\nRa 108,199,34816\nv_add_u32 v197, 0x10000, v197\nMvaa 112,252,152\nTa 184,202,32768\n"
"Mvaa 144,248,136\nTa 186,202,33280\nv_add_u32 v199, 0x10000, v199\nMvaa 144,252,156\nTa 188,202,34816\nMvva 20,216,120\nTa 190,202,35328\n"
"Mvva 20,220,140\nTa 192,202,32776\nMvva 52,216,124\nTa 194,202,33288\nMvva 52,220,144\nTa 196,202,34824\nMvva 84,216,128\nTa 198,202,35336\n"
"Mvva 84,220,148\nTa 200,202,33024\nMvva 116,216,132\nTa 202,202,33536\nMvva 116,220,152\nTa 204,202,35072\nMvva 148,216,136\nTa 206,202,35584\n"
"Mvva 148,220,156\nTa 208,202,33032\nMvva 24,224,120\nTa 210,202,33544\nMvva 24,228,140\nTa 212,202,35080\nMvva 56,224,124\nTa 214,202,35592\n"
"Mvva 56,228,144\nMvva 88,224,128\nv_add_u32 v202, 0x10000, v202\nMvva 88,228,148\nMvva 120,224,132\nMvva 120,228,152\nMvva 152,224,136\n"
"Mvva 152,228,156\n.Lfa_pd1300:\ns_cbranch_execz .Lfa_wr0\nMvva 28,232,120\nMvva 28,236,140\nMvva 60,232,124\nMvva 60,236,144\nMvva 92,232,128\n"
"Mvva 92,236,148\nMvva 124,232,132\nMvva 124,236,152\nMvva 156,232,136\ns_waitcnt vmcnt(12) lgkmcnt(0)\nMvva 156,236,156\ns_barrier\nMvaa 0,184,80\n"
"s_add_u32 s76, s90, 0x200\nRa 132,196,2048\nv_add_u32 v240, 0xffff0000, v240\nMvaa 0,188,100\ns_and_b32 s76, s76, 0x3fffff\nRa 136,198,2048\n"
"v_add_u32 v241, 0xffff0000, v241\nMvaa 32,184,84\ns_add_u32 m0, s63, 0x10000\nRa 152,197,2048\nv_add_u32 v196, 0xfffe0000, v196\nMvaa 32,188,104\n"
"DL 242,40,76,0\nMvaa 64,184,88\nRa 156,199,2048\nv_add_u32 v198, 0xfffe0000, v198\nMvaa 64,188,108\nDL 243,40,76,1024\nMvaa 96,184,92\nTa 248,202,0\n"
"v_add_u32 v197, 0xfffe0000, v197\nMvaa 96,188,112\nDL 244,40,76,2048\nMvaa 128,184,96\nTa 250,202,512\nv_add_u32 v199, 0xfffe0000, v199\n"
"Mvaa 128,188,116\nDL 245,40,76,3072\nMvaa 4,192,80\ns_add_u32 s77, s91, 0x0\nTa 252,202,2048\nMvaa 4,196,100\ns_and_b32 s77, s77, 0x3ff\n"
"Ta 254,202,2560\nMvaa 36,192,84\ns_add_u32 m0, s73, 0x10000\nTv 216,202,8\nMvaa 36,196,104\nDL 246,36,77,0\nMvaa 68,192,88\nTv 218,202,520\n"
"Mvaa 68,196,108\nDL 247,36,77,1024\nMvaa 100,192,92\nTv 220,202,2056\nMvaa 100,196,112\nDL 248,36,77,2048\nMvaa 132,192,96\n"
"s_add_u32 m0, s72, 0x1a000\nTv 222,202,2568\nMvaa 132,196,116\nDL 249,36,77,0\nMvaa 8,200,80\nTv 224,202,256\nMvaa 8,204,100\nDL 250,36,77,1024\n"
"Mvaa 40,200,84\ns_add_u32 s76, s90, 0x280\nTv 226,202,768\nMvaa 40,204,104\ns_and_b32 s76, s76, 0x3fffff\nTv 228,202,2304\nMvaa 72,200,88\n"
"s_add_u32 m0, s72, 0x18000\nTv 230,202,2816\nMvaa 72,204,108\nDL 242,40,76,0\nMvaa 104,200,92\nTv 232,202,264\nMvaa 104,204,112\nDL 243,40,76,1024\n"
"Mvaa 136,200,96\nTv 234,202,776\nMvaa 136,204,116\nDL 244,40,76,2048\nMvaa 12,208,80\nTv 236,202,2312\nMvaa 12,212,100\nDL 245,40,76,3072\n"
"Mvaa 44,208,84\ns_add_u32 s77, s91, 0x80\nTv 238,202,2824\nMvaa 44,212,104\ns_and_b32 s77, s77, 0x3ff\nMvaa 76,208,88\ns_add_u32 m0, s74, 0x18000\n"
"v_add_u32 v202, 0xfffe0000, v202\nMvaa 76,212,108\nDL 249,36,77,0\nMvaa 108,208,92\nMvaa 108,212,112\nDL 250,36,77,1024\nMvaa 140,208,96\n"
"Mvaa 140,212,116\nbuffer_load_dwordx4 a[164:167], v211, s[44:47], s78 offen offset:1024 nt\ns_waitcnt vmcnt(21) lgkmcnt(0)\ns_barrier\n"
"Mvaa 16,248,80\ns_add_u32 s76, s90, 0x80200\nRa 120,196,2048\nMvaa 16,252,100\ns_and_b32 s76, s76, 0x3fffff\nRa 124,240,4096\nMvaa 48,248,84\n"
"s_add_u32 m0, s63, 0x20000\nRa 128,198,2048\nMvaa 48,252,104\nDL 242,40,76,0\nMvaa 80,248,88\nRa 140,197,2048\nMvaa 80,252,108\nRa 144,241,4096\n"
"Mvaa 112,248,92\nRa 148,199,2048\nMvaa 112,252,112\nDL 243,40,76,1024\nMvaa 144,248,96\nTa 184,202,0\nMvaa 144,252,116\nTa 186,202,512\n"
"Mvva 20,216,80\nTa 188,202,2048\nMvva 20,220,100\nDL 244,40,76,2048\nMvva 52,216,84\nTa 190,202,2560\nMvva 52,220,104\nTa 192,202,8\nMvva 84,216,88\n"
"Ta 194,202,520\nMvva 84,220,108\nDL 245,40,76,3072\nMvva 116,216,92\ns_add_u32 s77, s91, 0x80\nTa 196,202,2056\nMvva 116,220,112\n"
"s_and_b32 s77, s77, 0x3ff\nTa 198,202,2568\nMvva 148,216,96\ns_add_u32 m0, s73, 0x20000\nTa 200,202,256\nMvva 148,220,116\nDL 246,36,77,0\n"
"Mvva 24,224,80\nTa 202,202,768\nMvva 24,228,100\nTa 204,202,2304\nMvva 56,224,84\nTa 206,202,2816\nMvva 56,228,104\nDL 247,36,77,1024\n"
"Mvva 88,224,88\nTa 208,202,264\nMvva 88,228,108\nTa 210,202,776\nMvva 120,224,92\nTa 212,202,2312\nMvva 120,228,112\nDL 248,36,77,2048\n"
"Mvva 152,224,96\nTa 214,202,2824\nMvva 152,228,116\nMvva 28,232,80\nMvva 28,236,100\n"
"buffer_load_dwordx4 a[168:171], v212, s[44:47], s78 offen offset:2048 nt\nMvva 60,232,84\nMvva 60,236,104\nMvva 92,232,88\nMvva 92,236,108\n"
"Mvva 124,232,92\nMvva 124,236,112\nMvva 156,232,96\ns_waitcnt vmcnt(25) lgkmcnt(0)\nMvva 156,236,116\ns_barrier\nMvaa 0,184,120\n"
"s_add_u32 s76, s90, 0x80280\nTa 248,202,32768\nv_add_u32 v196, 0x10000, v196\nMvaa 0,188,140\ns_and_b32 s76, s76, 0x3fffff\nTa 250,202,33280\n"
"v_add_u32 v240, 0x10000, v240\nMvaa 32,184,124\ns_add_u32 m0, s63, 0x0\nTa 252,202,34816\nv_add_u32 v198, 0x10000, v198\nMvaa 32,188,144\n"
"DL 242,40,76,0\nMvaa 64,184,128\nTa 254,202,35328\nv_add_u32 v197, 0x10000, v197\nMvaa 64,188,148\nTv 216,202,32776\nv_add_u32 v241, 0x10000, v241\n"
"Mvaa 96,184,132\nTv 218,202,33288\nv_add_u32 v199, 0x10000, v199\nMvaa 96,188,152\nDL 243,40,76,1024\nMvaa 128,184,136\nTv 220,202,34824\n"
"Mvaa 128,188,156\nTv 222,202,35336\nMvaa 4,192,120\nTv 224,202,33024\nMvaa 4,196,140\nDL 244,40,76,2048\nMvaa 36,192,124\nTv 226,202,33536\n"
"Mvaa 36,196,144\nTv 228,202,35072\nMvaa 68,192,128\nTv 230,202,35584\nMvaa 68,196,148\nDL 245,40,76,3072\nMvaa 100,192,132\n"
"s_add_u32 s77, s91, 0x100\nTv 232,202,33032\nMvaa 100,196,152\ns_and_b32 s77, s77, 0x3ff\nTv 234,202,33544\nMvaa 132,192,136\ns_add_u32 m0, s74, 0x0\n"
"Tv 236,202,35080\nMvaa 132,196,156\nDL 249,36,77,0\nMvaa 8,200,120\nTv 238,202,35592\nMvaa 8,204,140\nMvaa 40,200,124\nv_add_u32 v202, 0x10000, v202\n"
"Mvaa 40,204,144\nDL 250,36,77,1024\nMvaa 72,200,128\nMvaa 72,204,148\nMvaa 104,200,132\nv_mul_f32 v0, s80, v0\nv_mul_f32 v1, s80, v1\n"
"Mvaa 104,204,152\nbuffer_load_dwordx4 a[172:175], v213, s[44:47], s78 offen offset:3072 nt\nMvaa 136,200,136\nv_mul_f32 v2, s80, v2\n"
"v_mul_f32 v3, s80, v3\nMvaa 136,204,156\nv_mul_f32 v4, s80, v4\nv_mul_f32 v5, s80, v5\nMvaa 12,208,120\nv_mul_f32 v6, s80, v6\nv_mul_f32 v7, s80, v7\n"
"Mvaa 12,212,140\nCV 0,0\nCV 1,2\nMvaa 44,208,124\nCV 2,4\nCV 3,6\nMvaa 44,212,144\nv_mul_f32 v32, s80, v32\nv_mul_f32 v33, s80, v33\nMvaa 76,208,128\n"
"v_mul_f32 v34, s80, v34\nv_mul_f32 v35, s80, v35\nMvaa 76,212,148\nv_mul_f32 v36, s80, v36\nv_mul_f32 v37, s80, v37\nMvaa 108,208,132\n"
"v_mul_f32 v38, s80, v38\nv_mul_f32 v39, s80, v39\nMvaa 108,212,152\nCV 32,32\nCV 33,34\nMvaa 140,208,136\ns_waitcnt vmcnt(22) lgkmcnt(0)\nCV 34,36\n"
"CV 35,38\nMvaa 140,212,156\ns_barrier\nMvaa 16,248,120\ns_add_u32 s76, s90, 0x100200\nRa 0,196,2048\nv_mul_f32 v64, s80, v64\nMvaa 16,252,140\n"
"s_and_b32 s76, s76, 0x3fffff\nRa 4,240,4096\nv_mul_f32 v65, s80, v65\nMvaa 48,248,124\ns_add_u32 m0, s63, 0x8000\nRa 8,198,2048\n"
"v_mul_f32 v66, s80, v66\nMvaa 48,252,144\nDL 242,40,76,0\nMvaa 80,248,128\nRa 20,197,2048\nv_mul_f32 v67, s80, v67\nMvaa 80,252,148\nRa 24,241,4096\n"
"v_mul_f32 v68, s80, v68\nMvaa 112,248,132\nRa 28,199,2048\nv_mul_f32 v69, s80, v69\nMvaa 112,252,152\nDL 243,40,76,1024\nMvaa 144,248,136\n"
"Ra 12,196,32768\nv_mul_f32 v70, s80, v70\nMvaa 144,252,156\nRa 16,198,32768\nv_mul_f32 v71, s80, v71\nMvva 20,216,120\nRa 32,197,32768\nCV 64,64\n"
"Mvva 20,220,140\nDL 244,40,76,2048\nMvva 52,216,124\nRa 36,199,32768\nCV 65,66\nMvva 52,220,144\nTa 184,202,0\nCV 66,68\nMvva 84,216,128\n"
"Ta 186,202,512\nCV 67,70\nMvva 84,220,148\nDL 245,40,76,3072\nMvva 116,216,132\ns_add_u32 s77, s91, 0x100\nTa 188,202,2048\nv_mul_f32 v96, s80, v96\n"
"Mvva 116,220,152\ns_and_b32 s77, s77, 0x3ff\nTa 190,202,2560\nv_mul_f32 v97, s80, v97\nMvva 148,216,136\ns_add_u32 m0, s73, 0x8000\nTa 192,202,8\n"
"v_mul_f32 v98, s80, v98\nMvva 148,220,156\nDL 246,36,77,0\nMvva 24,224,120\nTa 194,202,520\nv_mul_f32 v99, s80, v99\nMvva 24,228,140\n"
"Ta 196,202,2056\nv_mul_f32 v100, s80, v100\nMvva 56,224,124\nTa 198,202,2568\nv_mul_f32 v101, s80, v101\nMvva 56,228,144\nDL 247,36,77,1024\n"
"Mvva 88,224,128\nTa 200,202,256\nv_mul_f32 v102, s80, v102\nMvva 88,228,148\nTa 202,202,768\nv_mul_f32 v103, s80, v103\nMvva 120,224,132\n"
"Ta 204,202,2304\nCV 96,96\nMvva 120,228,152\nDL 248,36,77,2048\nMvva 152,224,136\nTa 206,202,2816\nCV 97,98\nMvva 152,228,156\nTa 208,202,264\n"
"CV 98,100\nMvva 28,232,120\nTa 210,202,776\nCV 99,102\nMvva 28,236,140\nbuffer_load_dwordx4 a[176:179], v214, s[48:51], s78 offen offset:0 nt\n"
"Mvva 60,232,124\nTa 212,202,2312\nv_mul_f32 v128, s80, v128\nMvva 60,236,144\nTa 214,202,2824\nv_mul_f32 v129, s80, v129\nMvva 92,232,128\n"
"v_mul_f32 v130, s80, v130\nv_mul_f32 v131, s80, v131\nMvva 92,236,148\nv_mul_f32 v132, s80, v132\nv_mul_f32 v133, s80, v133\nMvva 124,232,132\n"
"v_mul_f32 v134, s80, v134\nv_mul_f32 v135, s80, v135\nMvva 124,236,152\nCV 128,128\nCV 129,130\nMvva 156,232,136\nCV 130,132\nCV 131,134\n"
"Mvva 156,236,156\ns_nop 7\nPS 8,80\nPS 10,80\nPS 12,80\nPS 14,80\nPS 16,80\nPS 18,80\nPS 20,80\nPS 22,80\nPS 24,80\nPS 26,80\nPS 28,80\nPS 30,80\n"
"PS 40,80\nPS 42,80\nPS 44,80\nPS 46,80\nPS 48,80\nPS 50,80\nPS 52,80\nPS 54,80\nPS 56,80\nPS 58,80\nPS 60,80\nPS 62,80\nPS 72,80\nPS 74,80\nPS 76,80\n"
"PS 78,80\nPS 80,80\nPS 82,80\nPS 84,80\nPS 86,80\nPS 88,80\nPS 90,80\nPS 92,80\nPS 94,80\nPS 104,80\nPS 106,80\nPS 108,80\nPS 110,80\nPS 112,80\n"
"PS 114,80\nPS 116,80\nPS 118,80\nPS 120,80\nPS 122,80\nPS 124,80\nPS 126,80\nPS 136,80\nPS 138,80\nPS 140,80\nPS 142,80\nPS 144,80\nPS 146,80\n"
"PS 148,80\nPS 150,80\nPS 152,80\nPS 154,80\nPS 156,80\nPS 158,80\nCV 8,8\nCV 9,10\nCV 10,12\nCV 11,14\nCV 16,16\nCV 17,18\nCV 18,20\nCV 19,22\n"
"CV 24,24\nCV 25,26\nCV 26,28\nCV 27,30\nAW 80,0\nAW 81,1\nAW 82,2\nAW 83,3\nAW 84,8\nAW 85,9\nAW 86,10\nAW 87,11\nAW 88,16\nAW 89,17\nAW 90,18\n"
"AW 91,19\nAW 92,24\nAW 93,25\nAW 94,26\nAW 95,27\nCV 40,40\nCV 41,42\nCV 42,44\nCV 43,46\nCV 48,48\nCV 49,50\nCV 50,52\nCV 51,54\nCV 56,56\nCV 57,58\n"
"CV 58,60\nCV 59,62\nAW 96,32\nAW 97,33\nAW 98,34\nAW 99,35\nAW 100,40\nAW 101,41\nAW 102,42\nAW 103,43\nAW 104,48\nAW 105,49\nAW 106,50\nAW 107,51\n"
"AW 108,56\nAW 109,57\nAW 110,58\nAW 111,59\nCV 72,72\nCV 73,74\nCV 74,76\nCV 75,78\nCV 80,80\nCV 81,82\nCV 82,84\nCV 83,86\nCV 88,88\nCV 89,90\n"
"CV 90,92\nCV 91,94\nAW 112,64\nAW 113,65\nAW 114,66\n.Lfa_pd1839:\ns_cbranch_execz .Lfa_wr0\nAW 115,67\nAW 116,72\nAW 117,73\nAW 118,74\nAW 119,75\n"
"AW 120,80\nAW 121,81\nAW 122,82\nAW 123,83\nAW 124,88\nAW 125,89\nAW 126,90\nAW 127,91\nCV 104,104\nCV 105,106\nCV 106,108\nCV 107,110\nCV 112,112\n"
"CV 113,114\nCV 114,116\nCV 115,118\nCV 120,120\nCV 121,122\nCV 122,124\nCV 123,126\nAW 128,96\nAW 129,97\nAW 130,98\nAW 131,99\nAW 132,104\n"
"AW 133,105\nAW 134,106\nAW 135,107\nAW 136,112\nAW 137,113\nAW 138,114\nAW 139,115\nAW 140,120\nAW 141,121\nAW 142,122\nAW 143,123\nCV 136,136\n"
"CV 137,138\nCV 138,140\nCV 139,142\nCV 144,144\nCV 145,146\nCV 146,148\nCV 147,150\nCV 152,152\nCV 153,154\nCV 154,156\nCV 155,158\nAW 144,128\n"
"AW 145,129\nAW 146,130\nAW 147,131\nAW 148,136\nAW 149,137\nAW 150,138\nAW 151,139\nAW 152,144\nAW 153,145\nAW 154,146\nAW 155,147\nAW 156,152\n"
"AW 157,153\nAW 158,154\nAW 159,155\ns_nop 1\ns_memrealtime s[98:99]\ns_waitcnt vmcnt(24) lgkmcnt(0)\ns_barrier\ns_mov_b32 s93, s98\nZvaa 0,184,0\n"
"s_add_u32 s76, s90, 0x100280\nRa 52,196,34816\nv_add_u32 v240, 0x10000, v240\nMvaa 0,188,20\ns_and_b32 s76, s76, 0x3fffff\nRa 56,198,34816\n"
"v_add_u32 v241, 0x10000, v241\nZvaa 32,184,4\ns_add_u32 m0, s63, 0x10000\nRa 72,197,34816\nv_add_u32 v196, 0x10000, v196\nMvaa 32,188,24\n"
"DL 242,40,76,0\nZvaa 64,184,8\nRa 76,199,34816\nv_add_u32 v198, 0x10000, v198\nMvaa 64,188,28\nTa 248,203,32768\nv_add_u32 v197, 0x10000, v197\n"
"Zvaa 96,184,12\nTa 250,203,33280\nv_add_u32 v199, 0x10000, v199\nMvaa 96,188,32\nDL 243,40,76,1024\nZvaa 128,184,16\nTa 252,203,34816\n"
"v_add_u32 v202, 0x10000, v202\nMvaa 128,188,36\nTa 254,203,35328\nZvaa 4,192,0\nTv 216,203,32776\nMvaa 4,196,20\nDL 244,40,76,2048\nZvaa 36,192,4\n"
"Tv 218,203,33288\nMvaa 36,196,24\nTv 220,203,34824\nZvaa 68,192,8\nTv 222,203,35336\nMvaa 68,196,28\nDL 245,40,76,3072\nZvaa 100,192,12\n"
"s_add_u32 s77, s91, 0x180\nTv 224,203,33024\nMvaa 100,196,32\ns_and_b32 s77, s77, 0x3ff\nTv 226,203,33536\nZvaa 132,192,16\n"
"s_add_u32 m0, s74, 0x10000\nTv 228,203,35072\nMvaa 132,196,36\nDL 249,36,77,0\nZvaa 8,200,0\nTv 230,203,35584\nMvaa 8,204,20\nTv 232,203,33032\n"
"Zvaa 40,200,4\nTv 234,203,33544\nMvaa 40,204,24\nDL 250,36,77,1024\nZvaa 72,200,8\nTv 236,203,35080\nMvaa 72,204,28\nTv 238,203,35592\n"
"Zvaa 104,200,12\nMvaa 104,204,32\nbuffer_load_dwordx4 v[168:171], v215, s[48:51], s78 offen offset:1024 nt\nZvaa 136,200,16\nMvaa 136,204,36\n"
"Zvaa 12,208,0\nMvaa 12,212,20\nZvaa 44,208,4\nMvaa 44,212,24\ns_memrealtime s[98:99]\nZvaa 76,208,8\nMvaa 76,212,28\nZvaa 108,208,12\n"
"Mvaa 108,212,32\nZvaa 140,208,16\ns_waitcnt vmcnt(23) lgkmcnt(0)\nMvaa 140,212,36\ns_barrier\n"
"s_sub_u32 s95, s98, s93\ns_mov_b32 s93, s98\ns_sub_u32 s95, 43, s95\ns_cbranch_scc1 .Lfa_t2b\ns_add_u32 s93, s93, s95\n.Lfa_t2a:\ns_sub_u32 s95, s95, 4\ns_cbranch_scc1 .Lfa_t2b\ns_sleep 1\ns_branch .Lfa_t2a\n.Lfa_t2b:\n"
"Zvaa 16,248,0\ns_add_u32 s76, s90, 0x180200\nRa 40,196,2048\nMvaa 16,252,20\ns_and_b32 s76, s76, 0x3fffff\nRa 44,240,4096\nZvaa 48,248,4\n"
"s_add_u32 m0, s63, 0x18000\nRa 48,198,2048\nv_add_u32 v196, 0xfffe0000, v196\nMvaa 48,252,24\nDL 242,40,76,0\nZvaa 80,248,8\nRa 60,197,2048\n"
"v_add_u32 v198, 0xfffe0000, v198\nMvaa 80,252,28\nRa 64,241,4096\nZvaa 112,248,12\nRa 68,199,2048\nv_add_u32 v197, 0xfffe0000, v197\nMvaa 112,252,32\n"
"DL 243,40,76,1024\nZvaa 144,248,16\nTa 184,202,0\nv_add_u32 v199, 0xfffe0000, v199\nMvaa 144,252,36\nTa 186,202,512\nZvva 20,216,0\nTa 188,202,2048\n"
"Mvva 20,220,20\nDL 244,40,76,2048\nZvva 52,216,4\nTa 190,202,2560\nMvva 52,220,24\nTa 192,202,8\nZvva 84,216,8\nTa 194,202,520\nMvva 84,220,28\n"
"DL 245,40,76,3072\nZvva 116,216,12\ns_add_u32 s77, s91, 0x180\nTa 196,202,2056\nMvva 116,220,32\ns_and_b32 s77, s77, 0x3ff\nTa 198,202,2568\n"
"Zvva 148,216,16\ns_add_u32 m0, s73, 0x18000\nTa 200,202,256\nMvva 148,220,36\nDL 246,36,77,0\nZvva 24,224,0\nTa 202,202,768\nMvva 24,228,20\n"
"Ta 204,202,2304\nZvva 56,224,4\nTa 206,202,2816\nMvva 56,228,24\nDL 247,36,77,1024\nZvva 88,224,8\nTa 208,202,264\nMvva 88,228,28\nTa 210,202,776\n"
"Zvva 120,224,12\nTa 212,202,2312\nMvva 120,228,32\nDL 248,36,77,2048\nZvva 152,224,16\nTa 214,202,2824\nMvva 152,228,36\nZvva 28,232,0\n"
"v_add_u32 v202, 0xfffe0000, v202\nMvva 28,236,20\nbuffer_load_dwordx4 v[172:175], v214, s[48:51], s78 offen offset:2048 nt\nZvva 60,232,4\n"
"Mvva 60,236,24\ns_memrealtime s[98:99]\nZvva 92,232,8\nMvva 92,236,28\nZvva 124,232,12\nMvva 124,236,32\nZvva 156,232,16\n"
"s_waitcnt vmcnt(24) lgkmcnt(0)\nMvva 156,236,36\ns_barrier\n"
"s_sub_u32 s95, s98, s93\ns_mov_b32 s93, s98\ns_sub_u32 s95, 43, s95\ns_cbranch_scc1 .Lfa_t3b\ns_add_u32 s93, s93, s95\n.Lfa_t3a:\ns_sub_u32 s95, s95, 4\ns_cbranch_scc1 .Lfa_t3b\ns_sleep 1\ns_branch .Lfa_t3a\n.Lfa_t3b:\n"
"Mvaa 0,184,40\ns_add_u32 s76, s90, 0x180280\nRa 12,196,2048\nv_add_u32 v240, 0xfffe0000, v240\nMvaa 0,188,60\ns_and_b32 s76, s76, 0x3fffff\n"
"Ra 16,198,2048\nv_add_u32 v241, 0xfffe0000, v241\nMvaa 32,184,44\ns_add_u32 m0, s63, 0x20000\nRa 32,197,2048\nMvaa 32,188,64\nDL 242,40,76,0\n"
"Mvaa 64,184,48\nRa 36,199,2048\nMvaa 64,188,68\nTa 248,202,0\nMvaa 96,184,52\nTa 250,202,512\nMvaa 96,188,72\nDL 243,40,76,1024\nMvaa 128,184,56\n"
"Ta 252,202,2048\nMvaa 128,188,76\nTa 254,202,2560\nMvaa 4,192,40\nTv 216,202,8\nMvaa 4,196,60\nDL 244,40,76,2048\nMvaa 36,192,44\nTv 218,202,520\n"
"Mvaa 36,196,64\nTv 220,202,2056\nMvaa 68,192,48\nTv 222,202,2568\nMvaa 68,196,68\nDL 245,40,76,3072\nMvaa 100,192,52\ns_add_u32 s77, s91, 0x200\n"
"Tv 224,202,256\nMvaa 100,196,72\ns_and_b32 s77, s77, 0x3ff\nTv 226,202,768\nMvaa 132,192,56\ns_add_u32 m0, s74, 0x20000\nTv 228,202,2304\n"
"Mvaa 132,196,76\nDL 249,36,77,0\nMvaa 8,200,40\nTv 230,202,2816\nMvaa 8,204,60\nTv 232,202,264\nMvaa 40,200,44\nTv 234,202,776\nMvaa 40,204,64\n"
"DL 250,36,77,1024\nMvaa 72,200,48\nTv 236,202,2312\nMvaa 72,204,68\nTv 238,202,2824\nMvaa 104,200,52\nMvaa 104,204,72\n"
"buffer_load_dwordx4 v[176:179], v215, s[48:51], s78 offen offset:3072 nt\nMvaa 136,200,56\nMvaa 136,204,76\nMvaa 12,208,40\nMvaa 12,212,60\n"
"Mvaa 44,208,44\nMvaa 44,212,64\ns_memrealtime s[98:99]\nMvaa 76,208,48\nMvaa 76,212,68\nMvaa 108,208,52\nMvaa 108,212,72\nMvaa 140,208,56\n"
"s_waitcnt vmcnt(23) lgkmcnt(0)\nMvaa 140,212,76\ns_barrier\n"
"s_sub_u32 s95, s98, s93\ns_mov_b32 s93, s98\ns_sub_u32 s95, 43, s95\ns_cbranch_scc1 .Lfa_t4b\ns_add_u32 s93, s93, s95\n.Lfa_t4a:\ns_sub_u32 s95, s95, 4\ns_cbranch_scc1 .Lfa_t4b\ns_sleep 1\ns_branch .Lfa_t4a\n.Lfa_t4b:\n"
"Mvaa 16,248,40\ns_add_u32 s76, s90, 0x200200\nRa 0,196,34816\nMvaa 16,252,60\ns_and_b32 s76, s76, 0x3fffff\nRa 4,240,36864\nMvaa 48,248,44\n"
"s_add_u32 m0, s63, 0x0\nRa 8,198,34816\nv_add_u32 v196, 0x10000, v196\nMvaa 48,252,64\nDL 242,40,76,0\nMvaa 80,248,48\nRa 20,197,34816\n"
"v_add_u32 v198, 0x10000, v198\nMvaa 80,252,68\nRa 24,241,36864\nMvaa 112,248,52\nRa 28,199,34816\nv_add_u32 v197, 0x10000, v197\nMvaa 112,252,72\n"
"DL 243,40,76,1024\nMvaa 144,248,56\nTa 184,202,32768\nv_add_u32 v199, 0x10000, v199\nMvaa 144,252,76\nTa 186,202,33280\nMvva 20,216,40\n"
"Ta 188,202,34816\nMvva 20,220,60\nDL 244,40,76,2048\nMvva 52,216,44\nTa 190,202,35328\nMvva 52,220,64\nTa 192,202,32776\nMvva 84,216,48\n"
"Ta 194,202,33288\nMvva 84,220,68\nDL 245,40,76,3072\nMvva 116,216,52\ns_add_u32 s77, s91, 0x200\nTa 196,202,34824\nMvva 116,220,72\n"
"s_and_b32 s77, s77, 0x3ff\nTa 198,202,35336\nMvva 148,216,56\ns_add_u32 m0, s73, 0x0\nTa 200,202,33024\nMvva 148,220,76\nDL 246,36,77,0\n"
"Mvva 24,224,40\nTa 202,202,33536\nMvva 24,228,60\nTa 204,202,35072\nMvva 56,224,44\nTa 206,202,35584\nMvva 56,228,64\nDL 247,36,77,1024\n"
"Mvva 88,224,48\nTa 208,202,33032\nMvva 88,228,68\nTa 210,202,33544\nMvva 120,224,52\nTa 212,202,35080\nMvva 120,228,72\nDL 248,36,77,2048\n"
"Mvva 152,224,56\nTa 214,202,35592\nMvva 152,228,76\ns_add_u32 s78, s97, 0x2000\nMvva 28,232,40\ns_and_b32 s78, s78, 0x1ffff\n"
"v_add_u32 v202, 0x10000, v202\nMvva 28,236,60\nbuffer_load_dwordx4 a[244:247], v210, s[44:47], s78 offen offset:0 nt\nMvva 60,232,44\nMvva 60,236,64\n"
"s_memrealtime s[98:99]\nMvva 92,232,48\nMvva 92,236,68\nMvva 124,232,52\nMvva 124,236,72\nMvva 156,232,56\ns_waitcnt vmcnt(24) lgkmcnt(0)\n"
"Mvva 156,236,76\ns_barrier\n"
"s_sub_u32 s95, s98, s93\ns_mov_b32 s93, s98\ns_sub_u32 s95, 43, s95\ns_cbranch_scc1 .Lfa_t5b\ns_add_u32 s93, s93, s95\n.Lfa_t5a:\ns_sub_u32 s95, s95, 4\ns_cbranch_scc1 .Lfa_t5b\ns_sleep 1\ns_branch .Lfa_t5a\n.Lfa_t5b:\n"
"Mvaa 0,184,0\ns_add_u32 s76, s90, 0x200280\nRa 52,196,2048\nv_add_u32 v240, 0x10000, v240\nMvaa 0,188,20\ns_and_b32 s76, s76, 0x3fffff\n"
"Ra 56,198,2048\nv_add_u32 v241, 0x10000, v241\nMvaa 32,184,4\ns_add_u32 m0, s63, 0x8000\nRa 72,197,2048\nMvaa 32,188,24\nDL 242,40,76,0\n"
"Mvaa 64,184,8\nRa 76,199,2048\nMvaa 64,188,28\nTa 248,202,0\nMvaa 96,184,12\nTa 250,202,512\nMvaa 96,188,32\nDL 243,40,76,1024\nMvaa 128,184,16\n"
"Ta 252,202,2048\nMvaa 128,188,36\nTa 254,202,2560\nMvaa 4,192,0\nTv 216,202,8\nMvaa 4,196,20\nDL 244,40,76,2048\nMvaa 36,192,4\nTv 218,202,520\n"
"Mvaa 36,196,24\nTv 220,202,2056\nMvaa 68,192,8\nTv 222,202,2568\nMvaa 68,196,28\nDL 245,40,76,3072\nMvaa 100,192,12\ns_add_u32 s77, s91, 0x280\n"
"Tv 224,202,256\nMvaa 100,196,32\ns_and_b32 s77, s77, 0x3ff\nTv 226,202,768\nMvaa 132,192,16\ns_add_u32 m0, s74, 0x8000\nTv 228,202,2304\n"
"Mvaa 132,196,36\nDL 249,36,77,0\nMvaa 8,200,0\nTv 230,202,2816\nMvaa 8,204,20\nTv 232,202,264\nMvaa 40,200,4\nTv 234,202,776\nMvaa 40,204,24\n"
"DL 250,36,77,1024\nMvaa 72,200,8\nTv 236,202,2312\nMvaa 72,204,28\nTv 238,202,2824\nMvaa 104,200,12\nMvaa 104,204,32\n"
"buffer_load_dword v252, v210, s[68:71], s96 offen offset:0\nMvaa 136,200,16\nMvaa 136,204,36\nMvaa 12,208,0\nMvaa 12,212,20\n"
"buffer_load_dword v252, v210, s[68:71], s96 offen offset:1024\nMvaa 44,208,4\nMvaa 44,212,24\ns_memrealtime s[98:99]\nMvaa 76,208,8\nMvaa 76,212,28\n"
"buffer_load_dwordx4 a[240:243], v211, s[44:47], s78 offen offset:1024 nt\nMvaa 108,208,12\nMvaa 108,212,32\nMvaa 140,208,16\n"
"s_waitcnt vmcnt(25) lgkmcnt(0)\nMvaa 140,212,36\ns_barrier\n"
"s_sub_u32 s95, s98, s93\ns_mov_b32 s93, s98\ns_sub_u32 s95, 43, s95\ns_cbranch_scc1 .Lfa_t6b\ns_add_u32 s93, s93, s95\n.Lfa_t6a:\ns_sub_u32 s95, s95, 4\ns_cbranch_scc1 .Lfa_t6b\ns_sleep 1\ns_branch .Lfa_t6a\n.Lfa_t6b:\n"
"Mvaa 16,248,0\ns_add_u32 s76, s90, 0x280200\nRa 40,196,34816\nMvaa 16,252,20\ns_and_b32 s76, s76, 0x3fffff\nRa 44,240,36864\nMvaa 48,248,4\n"
"s_add_u32 m0, s63, 0x10000\nRa 48,198,34816\nv_add_u32 v196, 0x10000, v196\nMvaa 48,252,24\nDL 242,40,76,0\nMvaa 80,248,8\n"
".Lfa_pd2343:\ns_cbranch_execz .Lfa_wr0\nRa 60,197,34816\nv_add_u32 v198, 0x10000, v198\nMvaa 80,252,28\nRa 64,241,36864\nMvaa 112,248,12\n"
"Ra 68,199,34816\nv_add_u32 v197, 0x10000, v197\nMvaa 112,252,32\nDL 243,40,76,1024\nMvaa 144,248,16\nTa 184,202,32768\nv_add_u32 v199, 0x10000, v199\n"
"Mvaa 144,252,36\nTa 186,202,33280\nMvva 20,216,0\nTa 188,202,34816\nMvva 20,220,20\nDL 244,40,76,2048\nMvva 52,216,4\nTa 190,202,35328\n"
"Mvva 52,220,24\nTa 192,202,32776\nMvva 84,216,8\nTa 194,202,33288\nMvva 84,220,28\nDL 245,40,76,3072\nMvva 116,216,12\ns_add_u32 s77, s91, 0x280\n"
"Ta 196,202,34824\nMvva 116,220,32\ns_and_b32 s77, s77, 0x3ff\nTa 198,202,35336\nMvva 148,216,16\ns_add_u32 m0, s73, 0x10000\nTa 200,202,33024\n"
"Mvva 148,220,36\nDL 246,36,77,0\nMvva 24,224,0\nTa 202,202,33536\nMvva 24,228,20\nTa 204,202,35072\nMvva 56,224,4\nTa 206,202,35584\nMvva 56,228,24\n"
"DL 247,36,77,1024\nMvva 88,224,8\nTa 208,202,33032\nMvva 88,228,28\nTa 210,202,33544\nMvva 120,224,12\nTa 212,202,35080\nMvva 120,228,32\n"
"DL 248,36,77,2048\nMvva 152,224,16\nTa 214,202,35592\nMvva 152,228,36\nMvva 28,232,0\nv_add_u32 v202, 0x10000, v202\nMvva 28,236,20\n"
"buffer_load_dwordx4 a[236:239], v212, s[44:47], s78 offen offset:2048 nt\nMvva 60,232,4\nMvva 60,236,24\ns_memrealtime s[98:99]\nMvva 92,232,8\n"
"Mvva 92,236,28\nMvva 124,232,12\nMvva 124,236,32\nMvva 156,232,16\ns_waitcnt vmcnt(26) lgkmcnt(0)\nMvva 156,236,36\ns_barrier\n"
"s_sub_u32 s95, s98, s93\ns_mov_b32 s93, s98\ns_sub_u32 s95, 43, s95\ns_cbranch_scc1 .Lfa_t7b\ns_add_u32 s93, s93, s95\n.Lfa_t7a:\ns_sub_u32 s95, s95, 4\ns_cbranch_scc1 .Lfa_t7b\ns_sleep 1\ns_branch .Lfa_t7a\n.Lfa_t7b:\n"
"Mvaa 0,184,40\ns_add_u32 s76, s90, 0x280280\nRa 12,196,2048\nv_add_u32 v240, 0xffff0000, v240\nMvaa 0,188,60\ns_and_b32 s76, s76, 0x3fffff\n"
"Ra 16,198,2048\nv_add_u32 v241, 0xffff0000, v241\nMvaa 32,184,44\ns_add_u32 m0, s63, 0x18000\nRa 32,197,2048\nv_add_u32 v196, 0xfffe0000, v196\n"
"Mvaa 32,188,64\nDL 242,40,76,0\nMvaa 64,184,48\nRa 36,199,2048\nv_add_u32 v198, 0xfffe0000, v198\nMvaa 64,188,68\nTa 248,202,0\n"
"v_add_u32 v197, 0xfffe0000, v197\nMvaa 96,184,52\nTa 250,202,512\nv_add_u32 v199, 0xfffe0000, v199\nMvaa 96,188,72\nDL 243,40,76,1024\n"
"Mvaa 128,184,56\nTa 252,202,2048\nMvaa 128,188,76\nTa 254,202,2560\nMvaa 4,192,40\nTv 216,202,8\nMvaa 4,196,60\nDL 244,40,76,2048\nMvaa 36,192,44\n"
"Tv 218,202,520\nMvaa 36,196,64\nTv 220,202,2056\nMvaa 68,192,48\nTv 222,202,2568\nMvaa 68,196,68\nDL 245,40,76,3072\nMvaa 100,192,52\n"
"s_add_u32 s77, s91, 0x300\nTv 224,202,256\nMvaa 100,196,72\ns_and_b32 s77, s77, 0x3ff\nTv 226,202,768\nMvaa 132,192,56\ns_add_u32 m0, s74, 0x18000\n"
"Tv 228,202,2304\nMvaa 132,196,76\nDL 249,36,77,0\nMvaa 8,200,40\nTv 230,202,2816\nMvaa 8,204,60\nTv 232,202,264\nMvaa 40,200,44\nTv 234,202,776\n"
"Mvaa 40,204,64\nDL 250,36,77,1024\nMvaa 72,200,48\nTv 236,202,2312\nMvaa 72,204,68\nTv 238,202,2824\nMvaa 104,200,52\nMvaa 104,204,72\n"
"buffer_load_dwordx4 a[232:235], v213, s[44:47], s78 offen offset:3072 nt\nMvaa 136,200,56\nv_add_u32 v202, 0xfffe0000, v202\nMvaa 136,204,76\n"
"Mvaa 12,208,40\nMvaa 12,212,60\nMvaa 44,208,44\nMvaa 44,212,64\ns_memrealtime s[98:99]\nMvaa 76,208,48\nMvaa 76,212,68\nMvaa 108,208,52\n"
"Mvaa 108,212,72\nMvaa 140,208,56\ns_waitcnt vmcnt(25) lgkmcnt(0)\nMvaa 140,212,76\ns_barrier\n"
"s_sub_u32 s95, s98, s93\ns_mov_b32 s93, s98\ns_sub_u32 s95, 43, s95\ns_cbranch_scc1 .Lfa_t8b\ns_add_u32 s93, s93, s95\n.Lfa_t8a:\ns_sub_u32 s95, s95, 4\ns_cbranch_scc1 .Lfa_t8b\ns_sleep 1\ns_branch .Lfa_t8a\n.Lfa_t8b:\n"
"Mvaa 16,248,40\ns_add_u32 s76, s90, 0x300200\nRa 0,196,2048\nMvaa 16,252,60\ns_and_b32 s76, s76, 0x3fffff\nRa 4,240,4096\nMvaa 48,248,44\n"
"s_add_u32 m0, s63, 0x20000\nRa 8,198,2048\nMvaa 48,252,64\nDL 242,40,76,0\nMvaa 80,248,48\nRa 20,197,2048\nMvaa 80,252,68\nRa 24,241,4096\n"
"Mvaa 112,248,52\nRa 28,199,2048\nMvaa 112,252,72\nDL 243,40,76,1024\nMvaa 144,248,56\nTa 184,202,0\nMvaa 144,252,76\nTa 186,202,512\nMvva 20,216,40\n"
"Ta 188,202,2048\nMvva 20,220,60\nDL 244,40,76,2048\nMvva 52,216,44\nTa 190,202,2560\nMvva 52,220,64\nTa 192,202,8\nMvva 84,216,48\nTa 194,202,520\n"
"Mvva 84,220,68\nDL 245,40,76,3072\nMvva 116,216,52\ns_add_u32 s77, s91, 0x300\nTa 196,202,2056\nMvva 116,220,72\ns_and_b32 s77, s77, 0x3ff\n"
"Ta 198,202,2568\nMvva 148,216,56\ns_add_u32 m0, s73, 0x20000\nTa 200,202,256\nMvva 148,220,76\nDL 246,36,77,0\nMvva 24,224,40\nTa 202,202,768\n"
"Mvva 24,228,60\nTa 204,202,2304\nMvva 56,224,44\nTa 206,202,2816\nMvva 56,228,64\nDL 247,36,77,1024\nMvva 88,224,48\nTa 208,202,264\nMvva 88,228,68\n"
"Ta 210,202,776\nMvva 120,224,52\nTa 212,202,2312\nMvva 120,228,72\nDL 248,36,77,2048\nMvva 152,224,56\nTa 214,202,2824\nMvva 152,228,76\n"
"Mvva 28,232,40\nMvva 28,236,60\nbuffer_load_dwordx4 a[228:231], v214, s[48:51], s78 offen offset:0 nt\nMvva 60,232,44\nMvva 60,236,64\n"
"s_memrealtime s[98:99]\nMvva 92,232,48\nMvva 92,236,68\nMvva 124,232,52\nMvva 124,236,72\nMvva 156,232,56\ns_waitcnt vmcnt(26) lgkmcnt(0)\n"
"Mvva 156,236,76\ns_barrier\n"
"s_sub_u32 s95, s98, s93\ns_mov_b32 s93, s98\ns_sub_u32 s95, 43, s95\ns_cbranch_scc1 .Lfa_t9b\ns_add_u32 s93, s93, s95\n.Lfa_t9a:\ns_sub_u32 s95, s95, 4\ns_cbranch_scc1 .Lfa_t9b\ns_sleep 1\ns_branch .Lfa_t9a\n.Lfa_t9b:\n"
"Mvaa 0,184,0\ns_add_u32 s76, s90, 0x300280\nRa 52,196,34816\nv_add_u32 v240, 0x10000, v240\nMvaa 0,188,20\ns_and_b32 s76, s76, 0x3fffff\n"
"Ra 56,198,34816\nv_add_u32 v241, 0x10000, v241\nMvaa 32,184,4\ns_add_u32 m0, s63, 0x0\nRa 72,197,34816\nv_add_u32 v196, 0x10000, v196\n"
"Mvaa 32,188,24\nDL 242,40,76,0\nMvaa 64,184,8\nRa 76,199,34816\nv_add_u32 v198, 0x10000, v198\nMvaa 64,188,28\nTa 248,202,32768\n"
"v_add_u32 v197, 0x10000, v197\nMvaa 96,184,12\nTa 250,202,33280\nv_add_u32 v199, 0x10000, v199\nMvaa 96,188,32\nDL 243,40,76,1024\nMvaa 128,184,16\n"
"Ta 252,202,34816\nMvaa 128,188,36\nTa 254,202,35328\nMvaa 4,192,0\nTv 216,202,32776\nMvaa 4,196,20\nDL 244,40,76,2048\nMvaa 36,192,4\n"
"Tv 218,202,33288\nMvaa 36,196,24\nTv 220,202,34824\nMvaa 68,192,8\nTv 222,202,35336\nMvaa 68,196,28\nDL 245,40,76,3072\nMvaa 100,192,12\n"
"s_add_u32 s77, s91, 0x380\nTv 224,202,33024\nMvaa 100,196,32\ns_and_b32 s77, s77, 0x3ff\nTv 226,202,33536\nMvaa 132,192,16\ns_add_u32 m0, s74, 0x0\n"
"Tv 228,202,35072\nMvaa 132,196,36\nDL 249,36,77,0\nMvaa 8,200,0\nTv 230,202,35584\nMvaa 8,204,20\nTv 232,202,33032\nMvaa 40,200,4\nTv 234,202,33544\n"
"Mvaa 40,204,24\nDL 250,36,77,1024\nMvaa 72,200,8\nTv 236,202,35080\nMvaa 72,204,28\nTv 238,202,35592\nMvaa 104,200,12\nMvaa 104,204,32\n"
"buffer_load_dwordx4 a[224:227], v215, s[48:51], s78 offen offset:1024 nt\nMvaa 136,200,16\nv_add_u32 v202, 0x10000, v202\nMvaa 136,204,36\n"
"Mvaa 12,208,0\nMvaa 12,212,20\nMvaa 44,208,4\nMvaa 44,212,24\ns_memrealtime s[98:99]\nMvaa 76,208,8\nMvaa 76,212,28\nMvaa 108,208,12\n"
"Mvaa 108,212,32\nMvaa 140,208,16\ns_waitcnt vmcnt(23) lgkmcnt(0)\nMvaa 140,212,36\ns_barrier\n"
"s_sub_u32 s95, s98, s93\ns_mov_b32 s93, s98\ns_sub_u32 s95, 43, s95\ns_cbranch_scc1 .Lfa_t10b\ns_add_u32 s93, s93, s95\n.Lfa_t10a:\ns_sub_u32 s95, s95, 4\ns_cbranch_scc1 .Lfa_t10b\ns_sleep 1\ns_branch .Lfa_t10a\n.Lfa_t10b:\n"
"Mvaa 16,248,0\ns_add_u32 s76, s90, 0x380200\nRa 40,196,2048\nMvaa 16,252,20\ns_and_b32 s76, s76, 0x3fffff\nRa 44,240,4096\nMvaa 48,248,4\n"
"s_add_u32 m0, s63, 0x8000\nRa 48,198,2048\nMvaa 48,252,24\nDL 242,40,76,0\nMvaa 80,248,8\nRa 60,197,2048\nMvaa 80,252,28\nRa 64,241,4096\n"
"Mvaa 112,248,12\nRa 68,199,2048\nMvaa 112,252,32\nDL 243,40,76,1024\nMvaa 144,248,16\nTa 184,202,0\nMvaa 144,252,36\nTa 186,202,512\nMvva 20,216,0\n"
"Ta 188,202,2048\nMvva 20,220,20\nDL 244,40,76,2048\nMvva 52,216,4\nTa 190,202,2560\nMvva 52,220,24\nTa 192,202,8\nMvva 84,216,8\nTa 194,202,520\n"
"Mvva 84,220,28\nDL 245,40,76,3072\nMvva 116,216,12\ns_add_u32 s77, s91, 0x380\nTa 196,202,2056\nMvva 116,220,32\ns_and_b32 s77, s77, 0x3ff\n"
"Ta 198,202,2568\nMvva 148,216,16\ns_add_u32 m0, s73, 0x8000\nTa 200,202,256\nMvva 148,220,36\nDL 246,36,77,0\nMvva 24,224,0\nTa 202,202,768\n"
"Mvva 24,228,20\nTa 204,202,2304\nMvva 56,224,4\nTa 206,202,2816\nMvva 56,228,24\nDL 247,36,77,1024\nMvva 88,224,8\nTa 208,202,264\nMvva 88,228,28\n"
"Ta 210,202,776\nMvva 120,224,12\nTa 212,202,2312\nMvva 120,228,32\nDL 248,36,77,2048\nMvva 152,224,16\nTa 214,202,2824\nMvva 152,228,36\n"
"Mvva 28,232,0\nMvva 28,236,20\nbuffer_load_dwordx4 a[220:223], v214, s[48:51], s78 offen offset:2048 nt\nMvva 60,232,4\nMvva 60,236,24\n"
"s_memrealtime s[98:99]\nMvva 92,232,8\nMvva 92,236,28\nMvva 124,232,12\nMvva 124,236,32\nMvva 156,232,16\ns_waitcnt vmcnt(24) lgkmcnt(0)\n"
"Mvva 156,236,36\ns_barrier\n"
"s_sub_u32 s95, s98, s93\ns_mov_b32 s93, s98\ns_sub_u32 s95, 43, s95\ns_cbranch_scc1 .Lfa_t11b\ns_add_u32 s93, s93, s95\n.Lfa_t11a:\ns_sub_u32 s95, s95, 4\ns_cbranch_scc1 .Lfa_t11b\ns_sleep 1\ns_branch .Lfa_t11a\n.Lfa_t11b:\n"
"Mvaa 0,184,40\ns_add_u32 s76, s90, 0x380280\nRa 12,196,34816\nv_add_u32 v240, 0x10000, v240\nMvaa 0,188,60\ns_and_b32 s76, s76, 0x3fffff\n"
"Ra 16,198,34816\nv_add_u32 v241, 0x10000, v241\nMvaa 32,184,44\ns_add_u32 m0, s63, 0x10000\nRa 32,197,34816\nv_add_u32 v196, 0x10000, v196\n"
"Mvaa 32,188,64\nDL 242,40,76,0\nMvaa 64,184,48\nRa 36,199,34816\nv_add_u32 v198, 0x10000, v198\nMvaa 64,188,68\nTa 248,202,32768\n"
"v_add_u32 v197, 0x10000, v197\nMvaa 96,184,52\nTa 250,202,33280\nv_add_u32 v199, 0x10000, v199\nMvaa 96,188,72\nDL 243,40,76,1024\nMvaa 128,184,56\n"
"Ta 252,202,34816\nv_mbcnt_lo_u32_b32 v253, -1, 0\nMvaa 128,188,76\nTa 254,202,35328\nv_mbcnt_hi_u32_b32 v253, -1, v253\nMvaa 4,192,40\n"
"s_lshl_b32 s88, s83, 14\nTv 216,202,32776\nMvaa 4,196,60\ns_add_u32 s88, s88, 0x18000\nDL 244,40,76,2048\nMvaa 36,192,44\nTv 218,202,33288\n"
"v_and_b32 v253, 48, v253\nMvaa 36,196,64\nTv 220,202,34824\nv_add_u32 v253, s88, v253\nMvaa 68,192,48\nTv 222,202,35336\nMvaa 68,196,68\n"
"DL 245,40,76,3072\nMvaa 100,192,52\nTv 224,202,33024\nMvaa 100,196,72\nTv 226,202,33536\nMvaa 132,192,56\nTv 228,202,35072\nMvaa 132,196,76\n"
"buffer_load_dwordx4 a[216:219], v215, s[48:51], s78 offen offset:3072 nt\nMvaa 8,200,40\nTv 230,202,35584\nMvaa 8,204,60\nTv 232,202,33032\n"
"Mvaa 40,200,44\nTv 234,202,33544\nMvaa 40,204,64\nTv 236,202,35080\nMvaa 72,200,48\nTv 238,202,35592\nMvaa 72,204,68\nMvaa 104,200,52\n"
"v_add_u32 v202, 0x10000, v202\nMvaa 104,204,72\nMvaa 136,200,56\nMvaa 136,204,76\nMvaa 12,208,40\nMvaa 12,212,60\nMvaa 44,208,44\nMvaa 44,212,64\n"
"s_memrealtime s[98:99]\nMvaa 76,208,48\nMvaa 76,212,68\nMvaa 108,208,52\nMvaa 108,212,72\nMvaa 140,208,56\ns_waitcnt vmcnt(21) lgkmcnt(0)\n"
"Mvaa 140,212,76\ns_barrier\n"
"s_sub_u32 s95, s98, s93\ns_mov_b32 s93, s98\ns_sub_u32 s95, 43, s95\ns_cbranch_scc1 .Lfa_t12b\ns_add_u32 s93, s93, s95\n.Lfa_t12a:\ns_sub_u32 s95, s95, 4\ns_cbranch_scc1 .Lfa_t12b\ns_sleep 1\ns_branch .Lfa_t12a\n.Lfa_t12b:\n"
"Mvaa 16,248,40\ns_add_u32 s88, s72, 0x18000\nRa 0,196,2048\nRa 4,240,4096\nMvaa 16,252,60\nRa 8,198,2048\nv_add_u32 v201, s88, v200\nMvaa 48,248,44\n"
"s_waitcnt vmcnt(60)\nMvaa 48,252,64\nRa 20,197,2048\nRa 24,241,4096\nMvaa 80,248,48\nRa 28,199,2048\nv_add_u32 v196, 0xfffe0000, v196\n"
"Mvaa 80,252,68\n.Lfa_pd2845:\ns_cbranch_execz .Lfa_wr1\nds_write_b128 v201, a[160:163] offset:0\nMvaa 112,248,52\nv_add_u32 v198, 0xfffe0000, v198\n"
"v_add_u32 v197, 0xfffe0000, v197\nMvaa 112,252,72\nTa 184,202,0\nTa 186,202,512\nMvaa 144,248,56\nTa 188,202,2048\nv_add_u32 v199, 0xfffe0000, v199\n"
"Mvaa 144,252,76\nds_write_b128 v201, a[164:167] offset:1024\nMvva 20,216,40\nMvva 20,220,60\nTa 190,202,2560\nTa 192,202,8\nMvva 52,216,44\n"
"Ta 194,202,520\nMvva 52,220,64\nds_write_b128 v201, a[168:171] offset:2048\nMvva 84,216,48\nMvva 84,220,68\nTa 196,202,2056\nTa 198,202,2568\n"
"Mvva 116,216,52\nTa 200,202,256\nMvva 116,220,72\nds_write_b128 v201, a[172:175] offset:3072\nMvva 148,216,56\nMvva 148,220,76\nTa 202,202,768\n"
"Mvva 24,224,40\nTa 204,202,2304\nMvva 24,228,60\nds_write_b128 v201, a[176:179] offset:8192\nMvva 56,224,44\nMvva 56,228,64\nTa 206,202,2816\n"
"Mvva 88,224,48\nTa 208,202,264\nMvva 88,228,68\nds_write_b128 v201, v[168:171] offset:9216\nMvva 120,224,52\nMvva 120,228,72\nTa 210,202,776\n"
"Mvva 152,224,56\nTa 212,202,2312\nMvva 152,228,76\nds_write_b128 v201, v[172:175] offset:10240\nMvva 28,232,40\nMvva 28,236,60\nTa 214,202,2824\n"
"Mvva 60,232,44\nMvva 60,236,64\ns_memrealtime s[98:99]\nds_write_b128 v201, v[176:179] offset:11264\nMvva 92,232,48\n"
"v_add_u32 v202, 0xfffe0000, v202\nMvva 92,236,68\nMvva 124,232,52\nMvva 124,236,72\nMvva 156,232,56\ns_waitcnt vmcnt(14) lgkmcnt(0)\nMvva 156,236,76\n"
"s_barrier\n"
"s_sub_u32 s95, s98, s93\ns_mov_b32 s93, s98\ns_sub_u32 s95, 43, s95\ns_cbranch_scc1 .Lfa_t13b\ns_add_u32 s93, s93, s95\n.Lfa_t13a:\ns_sub_u32 s95, s95, 4\ns_cbranch_scc1 .Lfa_t13b\ns_sleep 1\ns_branch .Lfa_t13a\n.Lfa_t13b:\n"
"Mvaa 0,184,0\ns_add_u32 s88, s72, 0x20000\nRa 52,196,2048\nRa 56,198,2048\nMvaa 0,188,20\nRa 72,197,2048\nv_add_u32 v240, 0xfffe0000, v240\n"
"Mvaa 32,184,4\nRa 76,199,2048\nTa 248,202,0\nMvaa 32,188,24\nTa 250,202,512\nv_add_u32 v241, 0xfffe0000, v241\nMvaa 64,184,8\nTa 252,202,2048\n"
"Ta 254,202,2560\nMvaa 64,188,28\nTv 216,202,8\nv_add_u32 v201, s88, v200\nMvaa 96,184,12\ns_waitcnt vmcnt(0)\nMvaa 96,188,32\n"
"ds_write_b128 v201, a[244:247] offset:0\nMvaa 128,184,16\nMvaa 128,188,36\nds_write_b128 v201, a[240:243] offset:1024\nMvaa 4,192,0\nMvaa 4,196,20\n"
"Tv 218,202,520\nTv 220,202,2056\nMvaa 36,192,4\nTv 222,202,2568\nMvaa 36,196,24\nds_write_b128 v201, a[236:239] offset:2048\nMvaa 68,192,8\n"
"Mvaa 68,196,28\nTv 224,202,256\nMvaa 100,192,12\nTv 226,202,768\nMvaa 100,196,32\nds_write_b128 v201, a[232:235] offset:3072\nMvaa 132,192,16\n"
"Mvaa 132,196,36\nTv 228,202,2304\nMvaa 8,200,0\nTv 230,202,2816\nMvaa 8,204,20\nds_write_b128 v201, a[228:231] offset:8192\nMvaa 40,200,4\n"
"Mvaa 40,204,24\nTv 232,202,264\nMvaa 72,200,8\nTv 234,202,776\nMvaa 72,204,28\nds_write_b128 v201, a[224:227] offset:9216\nMvaa 104,200,12\n"
"Mvaa 104,204,32\nTv 236,202,2312\nMvaa 136,200,16\nTv 238,202,2824\nMvaa 136,204,36\nds_write_b128 v201, a[220:223] offset:10240\nMvaa 12,208,0\n"
"Mvaa 12,212,20\nMvaa 44,208,4\nMvaa 44,212,24\ns_memrealtime s[98:99]\nds_write_b128 v201, a[216:219] offset:11264\nMvaa 76,208,8\nMvaa 76,212,28\n"
"Mvaa 108,208,12\nMvaa 108,212,32\nMvaa 140,208,16\ns_waitcnt vmcnt(6) lgkmcnt(0)\nMvaa 140,212,36\ns_barrier\n"
"s_sub_u32 s95, s98, s93\ns_mov_b32 s93, s98\ns_sub_u32 s95, 43, s95\ns_cbranch_scc1 .Lfa_t14b\ns_add_u32 s93, s93, s95\n.Lfa_t14a:\ns_sub_u32 s95, s95, 4\ns_cbranch_scc1 .Lfa_t14b\ns_sleep 1\ns_branch .Lfa_t14a\n.Lfa_t14b:\n"
"Mvaa 16,248,0\ns_add_u32 s75, s97, 0x4000\nRa 40,196,34816\nMvaa 16,252,20\ns_and_b32 s75, s75, 0x1ffff\nRa 44,240,36864\nMvaa 48,248,4\n"
"s_add_u32 m0, s72, 0x0\nRa 48,198,34816\nMvaa 48,252,24\nDtL 210,44,75,0\nMvaa 80,248,8\nRa 60,197,34816\nMvaa 80,252,28\nRa 64,241,36864\n"
"Mvaa 112,248,12\nRa 68,199,34816\nMvaa 112,252,32\nDtL 211,44,75,1024\nMvaa 144,248,16\nTa 184,202,32768\nMvaa 144,252,36\nTa 186,202,33280\n"
"Mvva 20,216,0\nTa 188,202,34816\nMvva 20,220,20\nDtL 212,44,75,2048\nMvva 52,216,4\nTa 190,202,35328\nMvva 52,220,24\nTa 192,202,32776\n"
"Mvva 84,216,8\nTa 194,202,33288\nMvva 84,220,28\nDtL 213,44,75,3072\nMvva 116,216,12\nTa 196,202,34824\nMvva 116,220,32\nTa 198,202,35336\n"
"Mvva 148,216,16\nTa 200,202,33024\nMvva 148,220,36\nTa 202,202,33536\nMvva 24,224,0\nTa 204,202,35072\nMvva 24,228,20\nTa 206,202,35584\n"
"Mvva 56,224,4\nTa 208,202,33032\nMvva 56,228,24\nTa 210,202,33544\nMvva 88,224,8\nTa 212,202,35080\nMvva 88,228,28\nTa 214,202,35592\n"
"Mvva 120,224,12\nMvva 120,228,32\nv_add_u32 v202, 0x10000, v202\nMvva 152,224,16\nMvva 152,228,36\nMvva 28,232,0\nMvva 28,236,20\nMvva 60,232,4\n"
"Mvva 60,236,24\ns_memrealtime s[98:99]\nMvva 92,232,8\nMvva 92,236,28\nMvva 124,232,12\nMvva 124,236,32\nMvva 156,232,16\n"
"s_waitcnt vmcnt(5) lgkmcnt(0)\nMvva 156,236,36\ns_barrier\n"
"s_sub_u32 s95, s98, s93\ns_mov_b32 s93, s98\ns_sub_u32 s95, 43, s95\ns_cbranch_scc1 .Lfa_t15b\ns_add_u32 s93, s93, s95\n.Lfa_t15a:\ns_sub_u32 s95, s95, 4\ns_cbranch_scc1 .Lfa_t15b\ns_sleep 1\ns_branch .Lfa_t15a\n.Lfa_t15b:\n"
"Mvaa 0,184,40\nTa 244,202,0\nv_add_u32 v204, 0x10000, v204\nMvaa 0,188,60\ns_add_u32 m0, s72, 0x2000\nTa 246,202,512\nv_add_u32 v205, 0x10000, v205\n"
"Mvaa 32,184,44\nDtL 214,48,75,0\nMvaa 32,188,64\nTa 240,202,2048\nv_add_u32 v206, 0x10000, v206\nMvaa 64,184,48\nTa 242,202,2560\n"
"v_add_u32 v207, 0x10000, v207\nMvaa 64,188,68\nTa 236,202,8\nv_add_u32 v208, 0x10000, v208\nMvaa 96,184,52\nDtL 215,48,75,1024\nMvaa 96,188,72\n"
"Ta 238,202,520\nv_add_u32 v209, 0x10000, v209\nMvaa 128,184,56\nTa 232,202,2056\nv_accvgpr_write_b32 a160, 0\nMvaa 128,188,76\nTa 234,202,2568\n"
"v_accvgpr_write_b32 a161, 0\nMvaa 4,192,40\nDtL 214,48,75,2048\nMvaa 4,196,60\nTa 228,202,256\nv_accvgpr_write_b32 a162, 0\nMvaa 36,192,44\n"
"Ta 230,202,768\nv_accvgpr_write_b32 a163, 0\nMvaa 36,196,64\nTa 224,202,2304\nv_accvgpr_write_b32 a164, 0\nMvaa 68,192,48\nDtL 215,48,75,3072\n"
"Mvaa 68,196,68\nTa 226,202,2816\nv_accvgpr_write_b32 a165, 0\nMvaa 100,192,52\nTa 220,202,264\nv_accvgpr_write_b32 a166, 0\nMvaa 100,196,72\n"
"Ta 222,202,776\nv_accvgpr_write_b32 a167, 0\nMvaa 132,192,56\nTa 216,202,2312\nv_accvgpr_write_b32 a168, 0\nMvaa 132,196,76\nTa 218,202,2824\n"
"v_accvgpr_write_b32 a169, 0\nMvaa 8,200,40\nv_accvgpr_write_b32 a170, 0\nv_accvgpr_write_b32 a171, 0\nMvaa 8,204,60\nv_accvgpr_write_b32 a172, 0\n"
"v_accvgpr_write_b32 a173, 0\nMvaa 40,200,44\nv_accvgpr_write_b32 a174, 0\nv_accvgpr_write_b32 a175, 0\nMvaa 40,204,64\nv_accvgpr_write_b32 a176, 0\n"
"v_accvgpr_write_b32 a177, 0\nMvaa 72,200,48\nv_accvgpr_write_b32 a178, 0\nv_accvgpr_write_b32 a179, 0\nMvaa 72,204,68\nMvaa 104,200,52\n"
"v_mul_f32 v0, s80, v0\nv_mul_f32 v1, s80, v1\nMvaa 104,204,72\nv_mul_f32 v2, s80, v2\nv_mul_f32 v3, s80, v3\nMvaa 136,200,56\nv_mul_f32 v4, s80, v4\n"
"v_mul_f32 v5, s80, v5\nMvaa 136,204,76\nv_mul_f32 v6, s80, v6\nv_mul_f32 v7, s80, v7\nMvaa 12,208,40\nCV 0,0\nCV 1,2\nMvaa 12,212,60\nCV 2,4\nCV 3,6\n"
"Mvaa 44,208,44\nv_mul_f32 v32, s80, v32\nv_mul_f32 v33, s80, v33\nMvaa 44,212,64\ns_memrealtime s[98:99]\nv_mul_f32 v34, s80, v34\n"
"v_mul_f32 v35, s80, v35\nMvaa 76,208,48\nv_mul_f32 v36, s80, v36\nv_mul_f32 v37, s80, v37\nMvaa 76,212,68\nv_mul_f32 v38, s80, v38\n"
"v_mul_f32 v39, s80, v39\nMvaa 108,208,52\nCV 32,32\nCV 33,34\nMvaa 108,212,72\nCV 34,36\nCV 35,38\nMvaa 140,208,56\ns_waitcnt lgkmcnt(0)\n"
"v_mul_f32 v64, s80, v64\nv_mul_f32 v65, s80, v65\nMvaa 140,212,76\ns_barrier\n"
"s_sub_u32 s95, s98, s93\ns_mov_b32 s93, s98\ns_sub_u32 s95, 43, s95\ns_cbranch_scc1 .Lfa_t16b\ns_add_u32 s93, s93, s95\n.Lfa_t16a:\ns_sub_u32 s95, s95, 4\ns_cbranch_scc1 .Lfa_t16b\ns_sleep 1\ns_branch .Lfa_t16a\n.Lfa_t16b:\n"
"Mvaa 16,244,40\ns_add_u32 s75, s97, 0x6000\nRv 180,253,0\nMvaa 16,240,60\ns_and_b32 s75, s75, 0x1ffff\nRv 184,253,64\nMvaa 48,244,44\n"
"s_add_u32 m0, s72, 0x8000\nRv 188,253,128\nMvaa 48,240,64\nDtL 210,44,75,0\nMvaa 80,244,48\nRv 192,253,192\nv_mul_f32 v66, s80, v66\nMvaa 80,240,68\n"
"Ra 248,204,32768\nv_mul_f32 v67, s80, v67\nMvaa 112,244,52\nRa 252,205,32768\nv_mul_f32 v68, s80, v68\nMvaa 112,240,72\nDtL 211,44,75,1024\n"
"Mvaa 144,244,56\nRv 216,206,32768\nv_mul_f32 v69, s80, v69\nMvaa 144,240,76\nRv 220,207,32768\nv_mul_f32 v70, s80, v70\nMvaa 20,236,40\n"
"Rv 224,204,36864\nv_mul_f32 v71, s80, v71\nMvaa 20,232,60\nDtL 212,44,75,2048\nMvaa 52,236,44\nRv 228,205,36864\nv_add_u32 v204, 0x10000, v204\n"
"Mvaa 52,232,64\nRv 232,206,36864\nCV 64,64\nMvaa 84,236,48\nRv 236,207,36864\nv_add_u32 v205, 0x10000, v205\nMvaa 84,232,68\nDtL 213,44,75,3072\n"
"Mvaa 116,236,52\nTa 184,208,40960\nv_add_u32 v206, 0x10000, v206\nMvaa 116,232,72\nTa 186,208,45056\nv_add_u32 v207, 0x10000, v207\nMvaa 148,236,56\n"
"Ta 188,209,40960\nCV 65,66\nMvaa 148,232,76\nTa 190,209,45056\nCV 66,68\nMvaa 24,228,40\nTa 192,208,41216\nCV 67,70\nMvaa 24,224,60\n"
"Ta 194,208,45312\nv_mul_f32 v96, s80, v96\nMvaa 56,228,44\nTa 196,209,41216\nv_mul_f32 v97, s80, v97\nMvaa 56,224,64\nTa 198,209,45312\n"
"v_mul_f32 v98, s80, v98\nMvaa 88,228,48\nTa 200,208,41472\nv_mul_f32 v99, s80, v99\nMvaa 88,224,68\nTa 202,208,45568\nv_mul_f32 v100, s80, v100\n"
"Mvaa 120,228,52\nTa 204,209,41472\nv_mul_f32 v101, s80, v101\nMvaa 120,224,72\nTa 206,209,45568\nv_mul_f32 v102, s80, v102\nMvaa 152,228,56\n"
"Ta 208,208,41728\nv_mul_f32 v103, s80, v103\nMvaa 152,224,76\nTa 210,208,45824\nCV 96,96\nMvaa 28,220,40\nTa 212,209,41728\nCV 97,98\nMvaa 28,216,60\n"
"Ta 214,209,45824\nv_add_u32 v208, 0x10000, v208\nMvaa 60,220,44\nCV 98,100\nCV 99,102\nMvaa 60,216,64\nv_add_u32 v209, 0x10000, v209\n"
"v_mul_f32 v128, s80, v128\nMvaa 92,220,48\nv_mul_f32 v129, s80, v129\nv_mul_f32 v130, s80, v130\nMvaa 92,216,68\nv_mul_f32 v131, s80, v131\n"
"v_mul_f32 v132, s80, v132\nMvaa 124,220,52\nv_mul_f32 v133, s80, v133\nv_mul_f32 v134, s80, v134\nMvaa 124,216,72\nv_mul_f32 v135, s80, v135\n"
"CV 128,128\nMvaa 156,220,56\nCV 129,130\nCV 130,132\nMvaa 156,216,76\ns_waitcnt lgkmcnt(0)\nCV 131,134\nv_xor_b32 v180, s81, v180\n"
"v_xor_b32 v181, s81, v181\nv_xor_b32 v182, s81, v182\nv_xor_b32 v183, s81, v183\nv_xor_b32 v184, s81, v184\nv_xor_b32 v185, s81, v185\n"
"v_xor_b32 v186, s81, v186\nv_xor_b32 v187, s81, v187\nv_xor_b32 v188, s81, v188\nv_xor_b32 v189, s81, v189\nv_xor_b32 v190, s81, v190\n"
"v_xor_b32 v191, s81, v191\nv_xor_b32 v192, s81, v192\nv_xor_b32 v193, s81, v193\nv_xor_b32 v194, s81, v194\nv_xor_b32 v195, s81, v195\ns_nop 7\n"
"PS 8,80\nPS 10,80\nPS 12,80\nPS 14,80\nPS 16,80\nPS 18,80\nPS 20,80\nPS 22,80\nPS 24,80\nPS 26,80\nPS 28,80\nPS 30,80\nPS 40,80\nPS 42,80\nPS 44,80\n"
"PS 46,80\nPS 48,80\nPS 50,80\nPS 52,80\nPS 54,80\nPS 56,80\nPS 58,80\nPS 60,80\nPS 62,80\nPS 72,80\nPS 74,80\nPS 76,80\nPS 78,80\nPS 80,80\nPS 82,80\n"
"PS 84,80\nPS 86,80\nPS 88,80\nPS 90,80\nPS 92,80\nPS 94,80\nPS 104,80\nPS 106,80\nPS 108,80\nPS 110,80\nPS 112,80\nPS 114,80\nPS 116,80\nPS 118,80\n"
"PS 120,80\nPS 122,80\nPS 124,80\nPS 126,80\nPS 136,80\nPS 138,80\nPS 140,80\nPS 142,80\nPS 144,80\nPS 146,80\nPS 148,80\nPS 150,80\nPS 152,80\n"
"PS 154,80\nPS 156,80\nPS 158,80\nCV 8,8\nCV 9,10\nZvva 160,180,80\n.Lfa_pd3376:\ns_cbranch_execz .Lfa_wr1\nCV 10,12\nCV 11,14\nCV 16,16\nCV 17,18\n"
"CV 18,20\nCV 19,22\nCV 24,24\nCV 25,26\nCV 26,28\nCV 27,30\nAW 0,0\nAW 1,1\nAW 2,2\nAW 3,3\nMvva 160,184,84\nAW 4,8\nAW 5,9\nAW 6,10\nAW 7,11\n"
"AW 8,16\nAW 9,17\nAW 10,18\nAW 11,19\nAW 12,24\nAW 13,25\nAW 14,26\nAW 15,27\nCV 40,40\nCV 41,42\nMvva 160,188,88\nCV 42,44\nCV 43,46\nCV 48,48\n"
"CV 49,50\nCV 50,52\nCV 51,54\nCV 56,56\nCV 57,58\nCV 58,60\nCV 59,62\nAW 16,32\nAW 17,33\nAW 18,34\nAW 19,35\nMvva 160,192,92\nAW 20,40\nAW 21,41\n"
"AW 22,42\nAW 23,43\nAW 24,48\nAW 25,49\nAW 26,50\nAW 27,51\nAW 28,56\nAW 29,57\nAW 30,58\nAW 31,59\nCV 72,72\nCV 73,74\nZvva 164,180,96\nCV 74,76\n"
"CV 75,78\nCV 80,80\nCV 81,82\nCV 82,84\nCV 83,86\nCV 88,88\nMvva 164,184,100\nCV 89,90\nCV 90,92\nCV 91,94\nAW 32,64\nAW 33,65\nAW 34,66\nAW 35,67\n"
"Mvva 164,188,104\nAW 36,72\nAW 37,73\nAW 38,74\nAW 39,75\nAW 40,80\nAW 41,81\nAW 42,82\nMvva 164,192,108\nAW 43,83\nAW 44,88\nAW 45,89\nAW 46,90\n"
"AW 47,91\nCV 104,104\nCV 105,106\nZvva 168,180,112\nCV 106,108\nCV 107,110\nCV 112,112\nCV 113,114\nCV 114,116\nCV 115,118\nCV 120,120\n"
"Mvva 168,184,116\nCV 121,122\nCV 122,124\nCV 123,126\nAW 48,96\nAW 49,97\nAW 50,98\nAW 51,99\nMvva 168,188,120\nAW 52,104\nAW 53,105\nAW 54,106\n"
"AW 55,107\nAW 56,112\nAW 57,113\nAW 58,114\nMvva 168,192,124\nAW 59,115\nAW 60,120\nAW 61,121\nAW 62,122\nAW 63,123\nCV 136,136\nCV 137,138\n"
"Zvva 172,180,128\nCV 138,140\nCV 139,142\nCV 144,144\nCV 145,146\nCV 146,148\nCV 147,150\nCV 152,152\nMvva 172,184,132\nCV 153,154\nCV 154,156\n"
"CV 155,158\nAW 64,128\nAW 65,129\nAW 66,130\nAW 67,131\nMvva 172,188,136\nAW 68,136\nAW 69,137\nAW 70,138\nAW 71,139\nAW 72,144\nAW 73,145\n"
"AW 74,146\nMvva 172,192,140\nAW 75,147\nAW 76,152\nAW 77,153\nAW 78,154\nAW 79,155\ns_nop 1\nZvva 176,180,144\nMvva 176,184,148\nMvva 176,188,152\n"
"Mvva 176,192,156\nCvaa 180,248,80,160\nMvaa 180,252,84\nMvva 180,216,88\nMvva 180,220,92\nCvva 184,224,80,160\nMvva 184,228,84\nMvva 184,232,88\n"
"Mvva 184,236,92\ns_nop 3\ns_memrealtime s[98:99]\ns_waitcnt lgkmcnt(0)\ns_barrier\ns_mov_b32 s93, s98\nCvaa 188,248,96,164\n"
"s_add_u32 m0, s72, 0xa000\nEX 180\nMvaa 188,252,100\ns_waitcnt vmcnt(7)\nEX 181\nMvva 188,216,104\nEX 182\nCV 196,180\nMvva 188,220,108\nEX 183\n"
"Cvva 192,224,96,164\nEX 184\nCV 197,182\nMvva 192,228,100\nEX 185\nMvva 192,232,104\nEX 186\nCV 198,184\nMvva 192,236,108\nEX 187\ns_nop 0\n"
"CV 199,186\nCvaa 180,248,112,168\nEX 188\nMvaa 180,252,116\nDtL 214,48,75,0\nMvva 180,216,120\nEX 189\nMvva 180,220,124\nRa 244,204,0\nCV 200,188\n"
"Cvva 184,224,112,168\nEX 190\nMvva 184,228,116\nRa 240,205,0\nMvva 184,232,120\nEX 191\nMvva 184,236,124\nRa 236,206,0\nCV 201,190\nZvav 0,184,196\n"
"EX 192\nZvav 4,188,196\nDtL 215,48,75,1024\nZvav 8,192,196\nEX 193\nZvav 12,196,196\nRa 232,207,0\nCV 202,192\nZvav 16,200,196\nEX 194\n"
"Zvav 20,204,196\nRa 228,204,4096\nZvav 24,208,196\nEX 195\nZvav 28,212,196\nRa 224,205,4096\nCV 203,194\nCvaa 188,248,128,172\ns_waitcnt vmcnt(7)\n"
"Ra 220,206,4096\nv_add_u32 v204, 0xfffe0000, v204\nMvaa 188,252,132\nEX 180\nMvva 188,216,136\nDtL 214,48,75,2048\nMvva 188,220,140\nEX 181\n"
"v_smfmac_f32_16x16x64_bf16 a[160:163], a[180:183], v[196:203], v251\nRa 216,207,4096\nCV 196,180\nCvva 192,224,128,172\nEX 182\nMvva 192,228,132\n"
"v_add_u32 v205, 0xfffe0000, v205\nv_add_u32 v206, 0xfffe0000, v206\nMvva 192,232,136\nEX 183\nMvva 192,236,140\nCV 197,182\n"
"v_add_u32 v207, 0xfffe0000, v207\nZvav 32,184,200\nEX 184\nZvav 36,188,200\nDtL 215,48,75,3072\nZvav 40,192,200\nEX 185\nZvav 44,196,200\nCV 198,184\n"
"Zvav 48,200,200\nEX 186\nZvav 52,204,200\ns_add_u32 s75, s97, 0x8000\nZvav 56,208,200\ns_and_b32 s75, s75, 0x1ffff\nEX 187\nZvav 60,212,200\n"
"s_add_u32 m0, s72, 0x10000\nCV 199,186\nCvaa 180,248,144,176\ns_waitcnt vmcnt(7)\nEX 188\nMvaa 180,252,148\nDtL 210,44,75,0\nMvva 180,216,152\n"
"EX 189\nMvva 180,220,156\nTa 248,208,8192\nCV 200,188\nCvva 184,224,144,176\nEX 190\nMvva 184,228,148\nTa 250,208,12288\nMvva 184,232,152\nEX 191\n"
"Mvva 184,236,156\nTa 252,209,8192\nCV 201,190\nZvav 64,184,196\nEX 192\nZvav 68,188,196\nDtL 211,44,75,1024\nZvav 72,192,196\nEX 193\n"
"Zvav 76,196,196\nTa 254,209,12288\nCV 202,192\nZvav 80,200,196\nEX 194\nZvav 84,204,196\nTv 216,208,8448\nZvav 88,208,196\nEX 195\nZvav 92,212,196\n"
"s_waitcnt lgkmcnt(2)\nTv 218,208,12544\nCV 203,194\nCvaa 188,244,80,160\ns_waitcnt vmcnt(7)\nTv 220,209,8448\nMvaa 188,240,84\nEX 180\n"
"Mvaa 188,236,88\nDtL 212,44,75,2048\nMvaa 188,232,92\nEX 181\nv_smfmac_f32_16x16x64_bf16 a[164:167], a[180:183], v[196:203], v251\nTv 222,209,12544\n"
"CV 196,180\nCvaa 192,228,80,160\nEX 182\nMvaa 192,224,84\nTv 224,208,8704\nMvaa 192,220,88\nEX 183\nMvaa 192,216,92\nTv 226,208,12800\nCV 197,182\n"
"Zvav 96,184,200\nEX 184\nZvav 100,188,200\nDtL 213,44,75,3072\nZvav 104,192,200\nEX 185\nZvav 108,196,200\nTv 228,209,8704\nCV 198,184\n"
"Zvav 112,200,200\nEX 186\nZvav 116,204,200\nTv 230,209,12800\nZvav 120,208,200\nEX 187\nZvav 124,212,200\nTv 232,208,8960\nCV 199,186\n"
"Cvaa 180,244,96,164\nEX 188\nMvaa 180,240,100\nTv 234,208,13056\nMvaa 180,236,104\nEX 189\nMvaa 180,232,108\ns_memrealtime s[98:99]\nTv 236,209,8960\n"
"CV 200,188\nCvaa 184,228,96,164\nEX 190\nMvaa 184,224,100\nTv 238,209,13056\nv_add_u32 v208, 0xfffe0000, v208\nMvaa 184,220,104\nEX 191\n"
"Mvaa 184,216,108\nCV 201,190\nv_add_u32 v209, 0xfffe0000, v209\nZvav 128,184,196\ns_waitcnt vmcnt(12) lgkmcnt(0)\nEX 192\nZvav 132,188,196\n"
"s_barrier\n"
"s_mov_b64 exec, 0\ns_cmp_eq_u32 s60, 0\ns_cbranch_scc1 .Lfa_w2_0\ns_cmp_eq_u32 s60, 1\ns_cbranch_scc1 .Lfa_w2_1\ns_cmp_eq_u32 s60, 2\ns_cbranch_scc1 .Lfa_w2_2\ns_branch .Lfa_w2_3\n.Lfa_wr2:\ns_mov_b64 exec, -1\ns_nop 4\n"
"s_sub_u32 s95, s98, s93\ns_mov_b32 s93, s98\ns_sub_u32 s95, 102, s95\ns_cbranch_scc1 .Lfa_t17b\ns_add_u32 s93, s93, s95\n.Lfa_t17a:\ns_sub_u32 s95, s95, 4\ns_cbranch_scc1 .Lfa_t17b\ns_sleep 1\ns_branch .Lfa_t17a\n.Lfa_t17b:\n"
"Zvav 136,192,196\ns_add_u32 m0, s72, 0x12000\nEX 193\nZvav 140,196,196\ns_waitcnt vmcnt(7)\nRa 184,204,0\nCV 202,192\nZvav 144,200,196\nEX 194\n"
"Zvav 148,204,196\nDtL 214,48,75,0\nZvav 152,208,196\nEX 195\nZvav 156,212,196\nRa 188,205,0\nCV 203,194\nCvaa 188,244,112,168\nRa 192,206,0\n"
"Mvaa 188,240,116\nEX 180\nMvaa 188,236,120\nRa 196,207,0\nMvaa 188,232,124\nEX 181\n"
"v_smfmac_f32_16x16x64_bf16 a[168:171], a[180:183], v[196:203], v251\nRa 200,204,4096\nCV 196,180\nCvaa 192,228,112,168\nEX 182\nMvaa 192,224,116\n"
"DtL 215,48,75,1024\nMvaa 192,220,120\nEX 183\nMvaa 192,216,124\nRa 204,205,4096\nCV 197,182\nMvav 0,248,200\nEX 184\nMvav 4,252,200\nRa 208,206,4096\n"
"Mvvv 8,216,200\nEX 185\nMvvv 12,220,200\nRa 212,207,4096\nCV 198,184\nMvvv 16,224,200\ns_waitcnt vmcnt(7)\nEX 186\nMvvv 20,228,200\n"
"DtL 214,48,75,2048\nMvvv 24,232,200\nEX 187\nMvvv 28,236,200\nCV 199,186\nCvaa 180,244,128,172\nEX 188\nMvaa 180,240,132\nMvaa 180,236,136\nEX 189\n"
"Mvaa 180,232,140\nCV 200,188\nCvaa 184,228,128,172\nEX 190\nMvaa 184,224,132\nDtL 215,48,75,3072\nMvaa 184,220,136\nEX 191\nMvaa 184,216,140\n"
"CV 201,190\nMvav 32,248,196\nEX 192\nMvav 36,252,196\ns_add_u32 s75, s97, 0xa000\nMvvv 40,216,196\ns_and_b32 s75, s75, 0x1ffff\nEX 193\n"
"Mvvv 44,220,196\ns_add_u32 m0, s72, 0x18000\nCV 202,192\nMvvv 48,224,196\ns_waitcnt vmcnt(7)\nEX 194\nMvvv 52,228,196\nDtL 210,44,75,0\n"
"Mvvv 56,232,196\nEX 195\nMvvv 60,236,196\nCV 203,194\nCvaa 188,244,144,176\nTa 244,208,8192\nMvaa 188,240,148\nEX 180\nMvaa 188,236,152\n"
"Ta 246,208,12288\nMvaa 188,232,156\nEX 181\nv_smfmac_f32_16x16x64_bf16 a[172:175], a[180:183], v[196:203], v251\nTa 240,209,8192\nCV 196,180\n"
"Cvaa 192,228,144,176\nEX 182\nMvaa 192,224,148\nDtL 211,44,75,1024\nMvaa 192,220,152\nEX 183\nMvaa 192,216,156\nTa 242,209,12288\nCV 197,182\n"
"Mvav 64,248,200\nEX 184\nMvav 68,252,200\nTa 236,208,8448\nMvvv 72,216,200\nEX 185\nMvvv 76,220,200\nTa 238,208,12544\nCV 198,184\nMvvv 80,224,200\n"
"s_waitcnt vmcnt(7)\nEX 186\nMvvv 84,228,200\nDtL 212,44,75,2048\nMvvv 88,232,200\nEX 187\nMvvv 92,236,200\ns_waitcnt lgkmcnt(2)\nTa 232,209,8448\n"
"CV 199,186\nCvaa 180,184,80,160\nEX 188\nMvaa 180,188,84\nTa 234,209,12544\nMvaa 180,192,88\nEX 189\nMvaa 180,196,92\nTa 228,208,8704\nCV 200,188\n"
"Cvaa 184,200,80,160\nEX 190\nMvaa 184,204,84\nDtL 213,44,75,3072\nMvaa 184,208,88\nEX 191\nMvaa 184,212,92\nTa 230,208,12800\nCV 201,190\n"
"Mvav 96,248,196\nEX 192\nMvav 100,252,196\nTa 224,209,8704\nMvvv 104,216,196\nEX 193\nMvvv 108,220,196\nTa 226,209,12800\nCV 202,192\n"
"Mvvv 112,224,196\nEX 194\nMvvv 116,228,196\nTa 220,208,8960\nMvvv 120,232,196\nEX 195\nMvvv 124,236,196\nTa 222,208,13056\nCV 203,194\n"
"Cvaa 188,184,96,164\nTa 216,209,8960\nMvaa 188,188,100\nEX 180\nMvaa 188,192,104\nTa 218,209,13056\nMvaa 188,196,108\nEX 181\n"
"v_smfmac_f32_16x16x64_bf16 a[176:179], a[180:183], v[196:203], v251\ns_memrealtime s[98:99]\nCV 196,180\nCvaa 192,200,96,164\nEX 182\n"
"Mvaa 192,204,100\nMvaa 192,208,104\n.Lfa_pd3931:\ns_cbranch_execz .Lfa_wr1\nEX 183\nMvaa 192,212,108\nCV 197,182\nMvav 128,248,200\n"
"s_waitcnt vmcnt(12) lgkmcnt(0)\nEX 184\nMvav 132,252,200\ns_barrier\n"
"s_sub_u32 s95, s98, s93\ns_mov_b32 s93, s98\ns_sub_u32 s95, 102, s95\ns_cbranch_scc1 .Lfa_t18b\ns_add_u32 s93, s93, s95\n.Lfa_t18a:\ns_sub_u32 s95, s95, 4\ns_cbranch_scc1 .Lfa_t18b\ns_sleep 1\ns_branch .Lfa_t18a\n.Lfa_t18b:\n"
"Mvvv 136,216,200\ns_add_u32 m0, s72, 0x1a000\nEX 185\nMvvv 140,220,200\ns_waitcnt vmcnt(7)\nRa 248,204,32768\nCV 198,184\nMvvv 144,224,200\nEX 186\n"
"Mvvv 148,228,200\nDtL 214,48,75,0\nMvvv 152,232,200\nEX 187\nMvvv 156,236,200\nRa 252,205,32768\nCV 199,186\nCvaa 180,184,112,168\nEX 188\n"
"Mvaa 180,188,116\nDtL 215,48,75,1024\nMvaa 180,192,120\nEX 189\nMvaa 180,196,124\nRv 216,206,32768\nCV 200,188\nCvaa 184,200,112,168\n"
"s_waitcnt vmcnt(7)\nEX 190\nMvaa 184,204,116\nDtL 214,48,75,2048\nMvaa 184,208,120\nEX 191\nMvaa 184,212,124\nRv 220,207,32768\nCV 201,190\n"
"Mvav 0,244,196\nEX 192\nMvav 4,240,196\nDtL 215,48,75,3072\nMvav 8,236,196\ns_add_u32 s75, s97, 0xc000\nEX 193\nMvav 12,232,196\n"
"s_and_b32 s75, s75, 0x1ffff\nRv 224,204,36864\nCV 202,192\nMvav 16,228,196\ns_add_u32 m0, s72, 0x20000\nEX 194\nMvav 20,224,196\ns_waitcnt vmcnt(7)\n"
"Rv 228,205,36864\nv_add_u32 v204, 0x10000, v204\nMvav 24,220,196\nEX 195\nMvav 28,216,196\nRv 232,206,36864\nCV 203,194\nCvaa 188,184,128,172\n"
"DtL 210,44,75,0\nMvaa 188,188,132\nEX 180\nMvaa 188,192,136\nRv 236,207,36864\nv_add_u32 v205, 0x10000, v205\nMvaa 188,196,140\nEX 181\n"
"v_smfmac_f32_16x16x64_bf16 a[160:163], a[180:183], v[196:203], v251\nCV 196,180\nv_add_u32 v206, 0x10000, v206\nCvaa 192,200,128,172\nEX 182\n"
"Mvaa 192,204,132\nDtL 211,44,75,1024\nMvaa 192,208,136\nEX 183\nMvaa 192,212,140\nCV 197,182\nv_add_u32 v207, 0x10000, v207\nMvav 32,244,200\n"
"s_waitcnt vmcnt(7)\nEX 184\nMvav 36,240,200\nDtL 212,44,75,2048\nMvav 40,236,200\nEX 185\nMvav 44,232,200\nCV 198,184\nMvav 48,228,200\nEX 186\n"
"Mvav 52,224,200\nDtL 213,44,75,3072\nMvav 56,220,200\nEX 187\nMvav 60,216,200\nCV 199,186\nCvaa 180,184,144,176\nEX 188\nMvaa 180,188,148\n"
"buffer_load_dword v252, v210, s[68:71], s96 offen offset:0\nMvaa 180,192,152\nEX 189\nMvaa 180,196,156\nTa 184,208,40960\nCV 200,188\n"
"Cvaa 184,200,144,176\nEX 190\nMvaa 184,204,148\nbuffer_load_dword v252, v210, s[68:71], s96 offen offset:1024\nMvaa 184,208,152\nEX 191\n"
"Mvaa 184,212,156\nTa 186,208,45056\nCV 201,190\nMvav 64,244,196\nEX 192\nMvav 68,240,196\nTa 188,209,40960\nMvav 72,236,196\nEX 193\nMvav 76,232,196\n"
"Ta 190,209,45056\nCV 202,192\nMvav 80,228,196\nEX 194\nMvav 84,224,196\nTa 192,208,41216\nMvav 88,220,196\nEX 195\nMvav 92,216,196\n"
"s_waitcnt lgkmcnt(3)\nTa 194,208,45312\nCV 203,194\nCvaa 188,248,80,160\nTa 196,209,41216\nMvaa 188,252,84\nEX 180\nMvva 188,216,88\n"
"Ta 198,209,45312\nMvva 188,220,92\nEX 181\nv_smfmac_f32_16x16x64_bf16 a[164:167], a[180:183], v[196:203], v251\nTa 200,208,41472\nCV 196,180\n"
"Cvva 192,224,80,160\nEX 182\nMvva 192,228,84\nTa 202,208,45568\nMvva 192,232,88\nEX 183\nMvva 192,236,92\nTa 204,209,41472\nCV 197,182\n"
"Mvav 96,244,200\nEX 184\nMvav 100,240,200\nTa 206,209,45568\nMvav 104,236,200\nEX 185\nMvav 108,232,200\nTa 208,208,41728\nCV 198,184\n"
"Mvav 112,228,200\nEX 186\nMvav 116,224,200\nTa 210,208,45824\nMvav 120,220,200\nEX 187\nMvav 124,216,200\nTa 212,209,41728\nCV 199,186\n"
"Cvaa 180,248,96,164\nEX 188\nMvaa 180,252,100\nTa 214,209,45824\nv_add_u32 v208, 0x10000, v208\nMvva 180,216,104\nEX 189\nMvva 180,220,108\n"
"s_memrealtime s[98:99]\nCV 200,188\nv_add_u32 v209, 0x10000, v209\nCvva 184,224,96,164\nEX 190\nMvva 184,228,100\nMvva 184,232,104\nEX 191\n"
"Mvva 184,236,108\nCV 201,190\nMvav 128,244,196\ns_waitcnt vmcnt(14) lgkmcnt(0)\nEX 192\nMvav 132,240,196\ns_barrier\n"
"s_sub_u32 s95, s98, s93\ns_mov_b32 s93, s98\ns_sub_u32 s95, 102, s95\ns_cbranch_scc1 .Lfa_t19b\ns_add_u32 s93, s93, s95\n.Lfa_t19a:\ns_sub_u32 s95, s95, 4\ns_cbranch_scc1 .Lfa_t19b\ns_sleep 1\ns_branch .Lfa_t19a\n.Lfa_t19b:\n"
"Mvav 136,236,196\ns_add_u32 m0, s72, 0x22000\nEX 193\nMvav 140,232,196\ns_waitcnt vmcnt(7)\nRa 244,204,0\nCV 202,192\nMvav 144,228,196\nEX 194\n"
"Mvav 148,224,196\nDtL 214,48,75,0\nMvav 152,220,196\nEX 195\nMvav 156,216,196\nRa 240,205,0\nCV 203,194\nCvaa 188,248,112,168\nRa 236,206,0\n"
"Mvaa 188,252,116\nEX 180\nMvva 188,216,120\nRa 232,207,0\nMvva 188,220,124\nEX 181\n"
"v_smfmac_f32_16x16x64_bf16 a[168:171], a[180:183], v[196:203], v251\nRa 228,204,4096\nCV 196,180\nCvva 192,224,112,168\nEX 182\nMvva 192,228,116\n"
"DtL 215,48,75,1024\nMvva 192,232,120\nEX 183\nMvva 192,236,124\nRa 224,205,4096\nCV 197,182\nMvav 0,184,200\nEX 184\nMvav 4,188,200\nRa 220,206,4096\n"
"Mvav 8,192,200\nEX 185\nMvav 12,196,200\nRa 216,207,4096\nCV 198,184\nMvav 16,200,200\ns_waitcnt vmcnt(7)\nEX 186\nMvav 20,204,200\n"
"DtL 214,48,75,2048\nMvav 24,208,200\nEX 187\nMvav 28,212,200\nCV 199,186\nCvaa 180,248,128,172\nEX 188\nMvaa 180,252,132\nMvva 180,216,136\nEX 189\n"
"Mvva 180,220,140\nCV 200,188\nCvva 184,224,128,172\nEX 190\nMvva 184,228,132\nDtL 215,48,75,3072\nMvva 184,232,136\nEX 191\nMvva 184,236,140\n"
"CV 201,190\nMvav 32,184,196\nEX 192\nMvav 36,188,196\ns_add_u32 s75, s97, 0xe000\nMvav 40,192,196\ns_and_b32 s75, s75, 0x1ffff\nEX 193\n"
"Mvav 44,196,196\ns_add_u32 m0, s72, 0x0\nCV 202,192\nMvav 48,200,196\ns_waitcnt vmcnt(7)\nEX 194\nMvav 52,204,196\nDtL 210,44,75,0\nMvav 56,208,196\n"
"EX 195\nMvav 60,212,196\nCV 203,194\nCvaa 188,248,144,176\nTa 248,208,8192\nMvaa 188,252,148\nEX 180\nMvva 188,216,152\nTa 250,208,12288\n"
"Mvva 188,220,156\nEX 181\nv_smfmac_f32_16x16x64_bf16 a[172:175], a[180:183], v[196:203], v251\nTa 252,209,8192\nCV 196,180\nCvva 192,224,144,176\n"
"EX 182\nMvva 192,228,148\nDtL 211,44,75,1024\nMvva 192,232,152\nEX 183\nMvva 192,236,156\nTa 254,209,12288\nCV 197,182\nMvav 64,184,200\nEX 184\n"
"Mvav 68,188,200\nTv 216,208,8448\nMvav 72,192,200\nEX 185\nMvav 76,196,200\nTv 218,208,12544\nCV 198,184\nMvav 80,200,200\ns_waitcnt vmcnt(7)\n"
"EX 186\nMvav 84,204,200\nDtL 212,44,75,2048\nMvav 88,208,200\nEX 187\nMvav 92,212,200\ns_waitcnt lgkmcnt(2)\nTv 220,209,8448\nCV 199,186\n"
"Cvaa 180,244,80,160\nEX 188\nMvaa 180,240,84\nTv 222,209,12544\nMvaa 180,236,88\nEX 189\nMvaa 180,232,92\nTv 224,208,8704\nCV 200,188\n"
"Cvaa 184,228,80,160\nEX 190\nMvaa 184,224,84\nDtL 213,44,75,3072\nMvaa 184,220,88\nEX 191\nMvaa 184,216,92\nTv 226,208,12800\nCV 201,190\n"
"Mvav 96,184,196\nEX 192\nMvav 100,188,196\nTv 228,209,8704\nMvav 104,192,196\nEX 193\nMvav 108,196,196\nTv 230,209,12800\nCV 202,192\n"
"Mvav 112,200,196\nEX 194\nMvav 116,204,196\nTv 232,208,8960\nMvav 120,208,196\nEX 195\nMvav 124,212,196\nTv 234,208,13056\nCV 203,194\n"
"Cvaa 188,244,96,164\nTv 236,209,8960\nMvaa 188,240,100\nEX 180\nMvaa 188,236,104\nTv 238,209,13056\nMvaa 188,232,108\nEX 181\n"
"v_smfmac_f32_16x16x64_bf16 a[176:179], a[180:183], v[196:203], v251\ns_memrealtime s[98:99]\nCV 196,180\nCvaa 192,228,96,164\nEX 182\n"
"Mvaa 192,224,100\nMvaa 192,220,104\nEX 183\nMvaa 192,216,108\nCV 197,182\nMvav 128,184,200\ns_waitcnt vmcnt(14) lgkmcnt(0)\nEX 184\nMvav 132,188,200\n"
"s_barrier\n"
"s_sub_u32 s95, s98, s93\ns_mov_b32 s93, s98\ns_sub_u32 s95, 102, s95\ns_cbranch_scc1 .Lfa_t20b\ns_add_u32 s93, s93, s95\n.Lfa_t20a:\ns_sub_u32 s95, s95, 4\ns_cbranch_scc1 .Lfa_t20b\ns_sleep 1\ns_branch .Lfa_t20a\n.Lfa_t20b:\n"
"Mvav 136,192,200\ns_add_u32 m0, s72, 0x2000\nEX 185\nMvav 140,196,200\ns_waitcnt vmcnt(7)\nRa 184,204,32768\nCV 198,184\nMvav 144,200,200\nEX 186\n"
"Mvav 148,204,200\nDtL 214,48,75,0\nMvav 152,208,200\nEX 187\nMvav 156,212,200\nRa 188,205,32768\nCV 199,186\nCvaa 180,244,112,168\nEX 188\n"
"Mvaa 180,240,116\nRa 192,206,32768\nMvaa 180,236,120\nEX 189\nMvaa 180,232,124\nRa 196,207,32768\nCV 200,188\nCvaa 184,228,112,168\nEX 190\n"
"Mvaa 184,224,116\nDtL 215,48,75,1024\nMvaa 184,220,120\nEX 191\nMvaa 184,216,124\nRa 200,204,36864\nCV 201,190\nMvav 0,248,196\nEX 192\n"
"Mvav 4,252,196\nRa 204,205,36864\nv_add_u32 v204, 0x10000, v204\nMvvv 8,216,196\nEX 193\nMvvv 12,220,196\nRa 208,206,36864\nCV 202,192\n"
"Mvvv 16,224,196\ns_waitcnt vmcnt(7)\nEX 194\nMvvv 20,228,196\nDtL 214,48,75,2048\nMvvv 24,232,196\nEX 195\nMvvv 28,236,196\nRa 212,207,36864\n"
"CV 203,194\nCvaa 188,244,128,172\nv_add_u32 v205, 0x10000, v205\nv_add_u32 v206, 0x10000, v206\nMvaa 188,240,132\nEX 180\nMvaa 188,236,136\n"
"v_add_u32 v207, 0x10000, v207\nMvaa 188,232,140\nEX 181\nv_smfmac_f32_16x16x64_bf16 a[160:163], a[180:183], v[196:203], v251\nCV 196,180\n"
"Cvaa 192,228,128,172\nEX 182\nMvaa 192,224,132\nDtL 215,48,75,3072\nMvaa 192,220,136\nEX 183\nMvaa 192,216,140\nCV 197,182\nMvav 32,248,200\nEX 184\n"
"Mvav 36,252,200\ns_add_u32 s75, s97, 0x10000\nMvvv 40,216,200\ns_and_b32 s75, s75, 0x1ffff\nEX 185\nMvvv 44,220,200\ns_add_u32 m0, s72, 0x8000\n"
"CV 198,184\nMvvv 48,224,200\ns_waitcnt vmcnt(7)\nEX 186\nMvvv 52,228,200\nDtL 210,44,75,0\nMvvv 56,232,200\nEX 187\nMvvv 60,236,200\nCV 199,186\n"
"Cvaa 180,244,144,176\nEX 188\nMvaa 180,240,148\nTa 244,208,40960\nMvaa 180,236,152\nEX 189\nMvaa 180,232,156\nTa 246,208,45056\nCV 200,188\n"
"Cvaa 184,228,144,176\nEX 190\nMvaa 184,224,148\nDtL 211,44,75,1024\nMvaa 184,220,152\nEX 191\nMvaa 184,216,156\nTa 240,209,40960\nCV 201,190\n"
"Mvav 64,248,196\nEX 192\nMvav 68,252,196\nTa 242,209,45056\nMvvv 72,216,196\nEX 193\nMvvv 76,220,196\nTa 236,208,41216\nCV 202,192\nMvvv 80,224,196\n"
"s_waitcnt vmcnt(7)\nEX 194\nMvvv 84,228,196\nDtL 212,44,75,2048\nMvvv 88,232,196\nEX 195\nMvvv 92,236,196\ns_waitcnt lgkmcnt(2)\nTa 238,208,45312\n"
"CV 203,194\nCvaa 188,184,80,160\nTa 232,209,41216\nMvaa 188,188,84\nEX 180\nMvaa 188,192,88\nTa 234,209,45312\nMvaa 188,196,92\nEX 181\n"
"v_smfmac_f32_16x16x64_bf16 a[164:167], a[180:183], v[196:203], v251\nTa 228,208,41472\nCV 196,180\nCvaa 192,200,80,160\nEX 182\nMvaa 192,204,84\n"
"DtL 213,44,75,3072\nMvaa 192,208,88\nEX 183\nMvaa 192,212,92\nTa 230,208,45568\nCV 197,182\nMvav 96,248,200\nEX 184\nMvav 100,252,200\n"
"Ta 224,209,41472\nMvvv 104,216,200\nEX 185\nMvvv 108,220,200\nTa 226,209,45568\nCV 198,184\nMvvv 112,224,200\nEX 186\nMvvv 116,228,200\n"
"Ta 220,208,41728\nMvvv 120,232,200\nEX 187\nMvvv 124,236,200\nTa 222,208,45824\nCV 199,186\nCvaa 180,184,96,164\nEX 188\nMvaa 180,188,100\n"
"Ta 216,209,41728\nv_add_u32 v208, 0x10000, v208\nMvaa 180,192,104\nEX 189\nMvaa 180,196,108\ns_memrealtime s[98:99]\nTa 218,209,45824\nCV 200,188\n"
"Cvaa 184,200,96,164\n.Lfa_pd4504:\ns_cbranch_execz .Lfa_wr1\nEX 190\nMvaa 184,204,100\nv_add_u32 v209, 0x10000, v209\nMvaa 184,208,104\nEX 191\n"
"Mvaa 184,212,108\nCV 201,190\nMvav 128,248,196\ns_waitcnt vmcnt(12) lgkmcnt(0)\nEX 192\nMvav 132,252,196\ns_barrier\n"
"s_sub_u32 s95, s98, s93\ns_mov_b32 s93, s98\ns_sub_u32 s95, 102, s95\ns_cbranch_scc1 .Lfa_t21b\ns_add_u32 s93, s93, s95\n.Lfa_t21a:\ns_sub_u32 s95, s95, 4\ns_cbranch_scc1 .Lfa_t21b\ns_sleep 1\ns_branch .Lfa_t21a\n.Lfa_t21b:\n"
"Mvvv 136,216,196\ns_add_u32 m0, s72, 0xa000\nEX 193\nMvvv 140,220,196\ns_waitcnt vmcnt(7)\nRa 248,204,0\nCV 202,192\nMvvv 144,224,196\nEX 194\n"
"Mvvv 148,228,196\nDtL 214,48,75,0\nMvvv 152,232,196\nEX 195\nMvvv 156,236,196\nRa 252,205,0\nCV 203,194\nCvaa 188,184,112,168\nRv 216,206,0\n"
"Mvaa 188,188,116\nEX 180\nMvaa 188,192,120\nRv 220,207,0\nMvaa 188,196,124\nEX 181\n"
"v_smfmac_f32_16x16x64_bf16 a[168:171], a[180:183], v[196:203], v251\nRv 224,204,4096\nCV 196,180\nCvaa 192,200,112,168\nEX 182\nMvaa 192,204,116\n"
"DtL 215,48,75,1024\nMvaa 192,208,120\nEX 183\nMvaa 192,212,124\nRv 228,205,4096\nCV 197,182\nMvav 0,244,200\nEX 184\nMvav 4,240,200\nRv 232,206,4096\n"
"v_add_u32 v204, 0xfffe0000, v204\nMvav 8,236,200\nEX 185\nMvav 12,232,200\nRv 236,207,4096\nCV 198,184\nMvav 16,228,200\ns_waitcnt vmcnt(7)\nEX 186\n"
"Mvav 20,224,200\nDtL 214,48,75,2048\nMvav 24,220,200\nEX 187\nMvav 28,216,200\nCV 199,186\nv_add_u32 v205, 0xfffe0000, v205\nCvaa 180,184,128,172\n"
"EX 188\nMvaa 180,188,132\nv_add_u32 v206, 0xfffe0000, v206\nv_add_u32 v207, 0xfffe0000, v207\nMvaa 180,192,136\nEX 189\nMvaa 180,196,140\nCV 200,188\n"
"Cvaa 184,200,128,172\nEX 190\nMvaa 184,204,132\nDtL 215,48,75,3072\nMvaa 184,208,136\nEX 191\nMvaa 184,212,140\nCV 201,190\nMvav 32,244,196\nEX 192\n"
"Mvav 36,240,196\ns_add_u32 s75, s97, 0x12000\nMvav 40,236,196\ns_and_b32 s75, s75, 0x1ffff\nEX 193\nMvav 44,232,196\ns_add_u32 m0, s72, 0x10000\n"
"CV 202,192\nMvav 48,228,196\ns_waitcnt vmcnt(7)\nEX 194\nMvav 52,224,196\nDtL 210,44,75,0\nMvav 56,220,196\nEX 195\nMvav 60,216,196\nCV 203,194\n"
"Cvaa 188,184,144,176\nTa 184,208,8192\nMvaa 188,188,148\nEX 180\nMvaa 188,192,152\nTa 186,208,12288\nMvaa 188,196,156\nEX 181\n"
"v_smfmac_f32_16x16x64_bf16 a[172:175], a[180:183], v[196:203], v251\nTa 188,209,8192\nCV 196,180\nCvaa 192,200,144,176\nEX 182\nMvaa 192,204,148\n"
"DtL 211,44,75,1024\nMvaa 192,208,152\nEX 183\nMvaa 192,212,156\nTa 190,209,12288\nCV 197,182\nMvav 64,244,200\nEX 184\nMvav 68,240,200\n"
"Ta 192,208,8448\nMvav 72,236,200\nEX 185\nMvav 76,232,200\nTa 194,208,12544\nCV 198,184\nMvav 80,228,200\ns_waitcnt vmcnt(7)\nEX 186\n"
"Mvav 84,224,200\nDtL 212,44,75,2048\nMvav 88,220,200\nEX 187\nMvav 92,216,200\ns_waitcnt lgkmcnt(2)\nTa 196,209,8448\nCV 199,186\n"
"Cvaa 180,248,80,160\nEX 188\nMvaa 180,252,84\nTa 198,209,12544\nMvva 180,216,88\nEX 189\nMvva 180,220,92\nTa 200,208,8704\nCV 200,188\n"
"Cvva 184,224,80,160\nEX 190\nMvva 184,228,84\nDtL 213,44,75,3072\nMvva 184,232,88\nEX 191\nMvva 184,236,92\nTa 202,208,12800\nCV 201,190\n"
"Mvav 96,244,196\nEX 192\nMvav 100,240,196\nTa 204,209,8704\nMvav 104,236,196\nEX 193\nMvav 108,232,196\nTa 206,209,12800\nCV 202,192\n"
"Mvav 112,228,196\nEX 194\nMvav 116,224,196\nTa 208,208,8960\nMvav 120,220,196\nEX 195\nMvav 124,216,196\nTa 210,208,13056\nCV 203,194\n"
"Cvaa 188,248,96,164\nTa 212,209,8960\nMvaa 188,252,100\nEX 180\nMvva 188,216,104\nTa 214,209,13056\nv_add_u32 v208, 0xfffe0000, v208\n"
"Mvva 188,220,108\nEX 181\nv_smfmac_f32_16x16x64_bf16 a[176:179], a[180:183], v[196:203], v251\ns_memrealtime s[98:99]\nCV 196,180\n"
"v_add_u32 v209, 0xfffe0000, v209\nCvva 192,224,96,164\nEX 182\nMvva 192,228,100\nMvva 192,232,104\nEX 183\nMvva 192,236,108\nCV 197,182\n"
"Mvav 128,244,200\ns_waitcnt vmcnt(12) lgkmcnt(0)\nEX 184\nMvav 132,240,200\ns_barrier\n"
"s_sub_u32 s95, s98, s93\ns_mov_b32 s93, s98\ns_sub_u32 s95, 102, s95\ns_cbranch_scc1 .Lfa_t22b\ns_add_u32 s93, s93, s95\n.Lfa_t22a:\ns_sub_u32 s95, s95, 4\ns_cbranch_scc1 .Lfa_t22b\ns_sleep 1\ns_branch .Lfa_t22a\n.Lfa_t22b:\n"
"Mvav 136,236,200\ns_add_u32 m0, s72, 0x12000\nEX 185\nMvav 140,232,200\ns_waitcnt vmcnt(7)\nRa 244,204,0\nCV 198,184\nMvav 144,228,200\nEX 186\n"
"Mvav 148,224,200\nDtL 214,48,75,0\nMvav 152,220,200\nEX 187\nMvav 156,216,200\nRa 240,205,0\nCV 199,186\nCvaa 180,248,112,168\nEX 188\n"
"Mvaa 180,252,116\nRa 236,206,0\nMvva 180,216,120\nEX 189\nMvva 180,220,124\nRa 232,207,0\nCV 200,188\nCvva 184,224,112,168\nEX 190\nMvva 184,228,116\n"
"DtL 215,48,75,1024\nMvva 184,232,120\nEX 191\nMvva 184,236,124\nRa 228,204,4096\nCV 201,190\nMvav 0,184,196\nEX 192\nMvav 4,188,196\nRa 224,205,4096\n"
"Mvav 8,192,196\nEX 193\nMvav 12,196,196\nRa 220,206,4096\nCV 202,192\nMvav 16,200,196\ns_waitcnt vmcnt(7)\nEX 194\nMvav 20,204,196\n"
"DtL 214,48,75,2048\nMvav 24,208,196\nEX 195\nMvav 28,212,196\nRa 216,207,4096\nCV 203,194\nCvaa 188,248,128,172\nMvaa 188,252,132\nEX 180\n"
"Mvva 188,216,136\nMvva 188,220,140\nEX 181\nv_smfmac_f32_16x16x64_bf16 a[160:163], a[180:183], v[196:203], v251\nCV 196,180\nCvva 192,224,128,172\n"
"EX 182\nMvva 192,228,132\nDtL 215,48,75,3072\nMvva 192,232,136\nEX 183\nMvva 192,236,140\nCV 197,182\nMvav 32,184,200\nEX 184\nMvav 36,188,200\n"
"s_add_u32 s75, s97, 0x14000\nMvav 40,192,200\ns_and_b32 s75, s75, 0x1ffff\nEX 185\nMvav 44,196,200\ns_add_u32 m0, s72, 0x18000\nCV 198,184\n"
"Mvav 48,200,200\ns_waitcnt vmcnt(7)\nEX 186\nMvav 52,204,200\nDtL 210,44,75,0\nMvav 56,208,200\nEX 187\nMvav 60,212,200\nCV 199,186\n"
"Cvaa 180,248,144,176\nEX 188\nMvaa 180,252,148\nTa 248,208,8192\nMvva 180,216,152\nEX 189\nMvva 180,220,156\nTa 250,208,12288\nCV 200,188\n"
"Cvva 184,224,144,176\nEX 190\nMvva 184,228,148\nDtL 211,44,75,1024\nMvva 184,232,152\nEX 191\nMvva 184,236,156\nTa 252,209,8192\nCV 201,190\n"
"Mvav 64,184,196\nEX 192\nMvav 68,188,196\nTa 254,209,12288\nMvav 72,192,196\nEX 193\nMvav 76,196,196\nTv 216,208,8448\nCV 202,192\nMvav 80,200,196\n"
"s_waitcnt vmcnt(7)\nEX 194\nMvav 84,204,196\nDtL 212,44,75,2048\nMvav 88,208,196\nEX 195\nMvav 92,212,196\ns_waitcnt lgkmcnt(2)\nTv 218,208,12544\n"
"CV 203,194\nCvaa 188,244,80,160\nTv 220,209,8448\nMvaa 188,240,84\nEX 180\nMvaa 188,236,88\nTv 222,209,12544\nMvaa 188,232,92\nEX 181\n"
"v_smfmac_f32_16x16x64_bf16 a[164:167], a[180:183], v[196:203], v251\nTv 224,208,8704\nCV 196,180\nCvaa 192,228,80,160\nEX 182\nMvaa 192,224,84\n"
"DtL 213,44,75,3072\nMvaa 192,220,88\nEX 183\nMvaa 192,216,92\nTv 226,208,12800\nCV 197,182\nMvav 96,184,200\nEX 184\nMvav 100,188,200\n"
"Tv 228,209,8704\nMvav 104,192,200\nEX 185\nMvav 108,196,200\nTv 230,209,12800\nCV 198,184\nMvav 112,200,200\nEX 186\nMvav 116,204,200\n"
"Tv 232,208,8960\nMvav 120,208,200\nEX 187\nMvav 124,212,200\nTv 234,208,13056\nCV 199,186\nCvaa 180,244,96,164\nEX 188\nMvaa 180,240,100\n"
"Tv 236,209,8960\nMvaa 180,236,104\nEX 189\nMvaa 180,232,108\ns_memrealtime s[98:99]\nTv 238,209,13056\nCV 200,188\nCvaa 184,228,96,164\nEX 190\n"
"Mvaa 184,224,100\nMvaa 184,220,104\nEX 191\nMvaa 184,216,108\nCV 201,190\nMvav 128,184,196\ns_waitcnt vmcnt(12) lgkmcnt(0)\nEX 192\nMvav 132,188,196\n"
"s_barrier\n"
"s_sub_u32 s95, s98, s93\ns_mov_b32 s93, s98\ns_sub_u32 s95, 102, s95\ns_cbranch_scc1 .Lfa_t23b\ns_add_u32 s93, s93, s95\n.Lfa_t23a:\ns_sub_u32 s95, s95, 4\ns_cbranch_scc1 .Lfa_t23b\ns_sleep 1\ns_branch .Lfa_t23a\n.Lfa_t23b:\n"
"Mvav 136,192,196\ns_add_u32 m0, s72, 0x1a000\nEX 193\nMvav 140,196,196\ns_waitcnt vmcnt(7)\nRa 184,204,32768\nCV 202,192\nMvav 144,200,196\nEX 194\n"
"Mvav 148,204,196\nDtL 214,48,75,0\nMvav 152,208,196\nEX 195\nMvav 156,212,196\nRa 188,205,32768\nCV 203,194\nCvaa 188,244,112,168\nRa 192,206,32768\n"
"Mvaa 188,240,116\nEX 180\nMvaa 188,236,120\nRa 196,207,32768\nMvaa 188,232,124\nEX 181\n"
"v_smfmac_f32_16x16x64_bf16 a[168:171], a[180:183], v[196:203], v251\nRa 200,204,36864\nCV 196,180\nCvaa 192,228,112,168\nEX 182\nMvaa 192,224,116\n"
"DtL 215,48,75,1024\nMvaa 192,220,120\nEX 183\nMvaa 192,216,124\nRa 204,205,36864\nCV 197,182\nMvav 0,248,200\nEX 184\nMvav 4,252,200\n"
"Ra 208,206,36864\nv_add_u32 v204, 0x10000, v204\nMvvv 8,216,200\nEX 185\nMvvv 12,220,200\nRa 212,207,36864\nCV 198,184\nMvvv 16,224,200\n"
"s_waitcnt vmcnt(7)\nEX 186\nMvvv 20,228,200\nDtL 214,48,75,2048\nMvvv 24,232,200\nEX 187\nMvvv 28,236,200\nCV 199,186\nv_add_u32 v205, 0x10000, v205\n"
"Cvaa 180,244,128,172\nEX 188\nMvaa 180,240,132\nv_add_u32 v206, 0x10000, v206\nv_add_u32 v207, 0x10000, v207\nMvaa 180,236,136\nEX 189\n"
"Mvaa 180,232,140\nCV 200,188\nCvaa 184,228,128,172\nEX 190\nMvaa 184,224,132\nDtL 215,48,75,3072\nMvaa 184,220,136\nEX 191\nMvaa 184,216,140\n"
"CV 201,190\nMvav 32,248,196\nEX 192\nMvav 36,252,196\ns_add_u32 s75, s97, 0x16000\nMvvv 40,216,196\ns_and_b32 s75, s75, 0x1ffff\nEX 193\n"
"Mvvv 44,220,196\ns_add_u32 m0, s72, 0x20000\nCV 202,192\nMvvv 48,224,196\ns_waitcnt vmcnt(7)\nEX 194\nMvvv 52,228,196\nDtL 210,44,75,0\n"
"Mvvv 56,232,196\nEX 195\nMvvv 60,236,196\nCV 203,194\nCvaa 188,244,144,176\nTa 244,208,40960\nMvaa 188,240,148\nEX 180\nMvaa 188,236,152\n"
"Ta 246,208,45056\nMvaa 188,232,156\nEX 181\nv_smfmac_f32_16x16x64_bf16 a[172:175], a[180:183], v[196:203], v251\nTa 240,209,40960\nCV 196,180\n"
"Cvaa 192,228,144,176\nEX 182\nMvaa 192,224,148\nDtL 211,44,75,1024\nMvaa 192,220,152\nEX 183\nMvaa 192,216,156\nTa 242,209,45056\nCV 197,182\n"
"Mvav 64,248,200\nEX 184\nMvav 68,252,200\nTa 236,208,41216\nMvvv 72,216,200\nEX 185\nMvvv 76,220,200\nTa 238,208,45312\nCV 198,184\nMvvv 80,224,200\n"
"s_waitcnt vmcnt(7)\nEX 186\nMvvv 84,228,200\nDtL 212,44,75,2048\nMvvv 88,232,200\nEX 187\nMvvv 92,236,200\ns_waitcnt lgkmcnt(2)\nTa 232,209,41216\n"
"CV 199,186\nCvaa 180,184,80,160\nEX 188\nMvaa 180,188,84\nTa 234,209,45312\nMvaa 180,192,88\nEX 189\nMvaa 180,196,92\nTa 228,208,41472\nCV 200,188\n"
"Cvaa 184,200,80,160\nEX 190\nMvaa 184,204,84\nDtL 213,44,75,3072\nMvaa 184,208,88\nEX 191\nMvaa 184,212,92\nTa 230,208,45568\nCV 201,190\n"
"Mvav 96,248,196\nEX 192\nMvav 100,252,196\nTa 224,209,41472\nMvvv 104,216,196\nEX 193\nMvvv 108,220,196\nTa 226,209,45568\nCV 202,192\n"
"Mvvv 112,224,196\nEX 194\nMvvv 116,228,196\nTa 220,208,41728\nMvvv 120,232,196\nEX 195\nMvvv 124,236,196\nTa 222,208,45824\nCV 203,194\n"
"Cvaa 188,184,96,164\nTa 216,209,41728\nMvaa 188,188,100\nEX 180\nMvaa 188,192,104\n.Lfa_pd5072:\ns_cbranch_execz .Lfa_wr2\nTa 218,209,45824\n"
"v_add_u32 v208, 0x10000, v208\nMvaa 188,196,108\nEX 181\nv_smfmac_f32_16x16x64_bf16 a[176:179], a[180:183], v[196:203], v251\ns_memrealtime s[98:99]\n"
"CV 196,180\nv_add_u32 v209, 0x10000, v209\nCvaa 192,200,96,164\nEX 182\nMvaa 192,204,100\nMvaa 192,208,104\nEX 183\nMvaa 192,212,108\nCV 197,182\n"
"Mvav 128,248,200\ns_waitcnt vmcnt(12) lgkmcnt(0)\nEX 184\nMvav 132,252,200\ns_barrier\n"
"s_mov_b64 exec, 0\ns_cmp_eq_u32 s60, 0\ns_cbranch_scc1 .Lfa_w3_0\ns_cmp_eq_u32 s60, 1\ns_cbranch_scc1 .Lfa_w3_1\ns_cmp_eq_u32 s60, 2\ns_cbranch_scc1 .Lfa_w3_2\ns_branch .Lfa_w3_3\n.Lfa_wr3:\ns_mov_b64 exec, -1\ns_nop 4\n"
"s_sub_u32 s95, s98, s93\ns_mov_b32 s93, s98\ns_sub_u32 s95, 102, s95\ns_cbranch_scc1 .Lfa_t24b\ns_add_u32 s93, s93, s95\n.Lfa_t24a:\ns_sub_u32 s95, s95, 4\ns_cbranch_scc1 .Lfa_t24b\ns_sleep 1\ns_branch .Lfa_t24a\n.Lfa_t24b:\n"
"Mvvv 136,216,200\ns_add_u32 m0, s72, 0x22000\nEX 185\nMvvv 140,220,200\ns_waitcnt vmcnt(7)\nRa 248,204,0\nCV 198,184\nMvvv 144,224,200\nEX 186\n"
"Mvvv 148,228,200\nDtL 214,48,75,0\nMvvv 152,232,200\nEX 187\nMvvv 156,236,200\nRa 252,205,0\nCV 199,186\nCvaa 180,184,112,168\nEX 188\n"
"Mvaa 180,188,116\nRv 216,206,0\nMvaa 180,192,120\nEX 189\nMvaa 180,196,124\nRv 220,207,0\nCV 200,188\nCvaa 184,200,112,168\nEX 190\nMvaa 184,204,116\n"
"DtL 215,48,75,1024\nMvaa 184,208,120\nEX 191\nMvaa 184,212,124\nRv 224,204,4096\nCV 201,190\nMvav 0,244,196\nEX 192\nMvav 4,240,196\nRv 228,205,4096\n"
"Mvav 8,236,196\nEX 193\nMvav 12,232,196\nRv 232,206,4096\nCV 202,192\nMvav 16,228,196\ns_waitcnt vmcnt(7)\nEX 194\nMvav 20,224,196\n"
"DtL 214,48,75,2048\nMvav 24,220,196\nEX 195\nMvav 28,216,196\nRv 236,207,4096\nCV 203,194\nCvaa 188,184,128,172\nMvaa 188,188,132\nEX 180\n"
"Mvaa 188,192,136\nMvaa 188,196,140\nEX 181\nv_smfmac_f32_16x16x64_bf16 a[160:163], a[180:183], v[196:203], v251\nCV 196,180\nCvaa 192,200,128,172\n"
"EX 182\nMvaa 192,204,132\nDtL 215,48,75,3072\nMvaa 192,208,136\nEX 183\nMvaa 192,212,140\nCV 197,182\nMvav 32,244,200\nEX 184\nMvav 36,240,200\n"
"s_add_u32 s75, s97, 0x18000\nMvav 40,236,200\ns_and_b32 s75, s75, 0x1ffff\nEX 185\nMvav 44,232,200\ns_add_u32 m0, s72, 0x0\nCV 198,184\n"
"Mvav 48,228,200\ns_waitcnt vmcnt(7)\nEX 186\nMvav 52,224,200\nDtL 210,44,75,0\nMvav 56,220,200\nEX 187\nMvav 60,216,200\nCV 199,186\n"
"Cvaa 180,184,144,176\nEX 188\nMvaa 180,188,148\nTa 184,208,8192\nMvaa 180,192,152\nEX 189\nMvaa 180,196,156\nTa 186,208,12288\nCV 200,188\n"
"Cvaa 184,200,144,176\nEX 190\nMvaa 184,204,148\nDtL 211,44,75,1024\nMvaa 184,208,152\nEX 191\nMvaa 184,212,156\nTa 188,209,8192\nCV 201,190\n"
"Mvav 64,244,196\nEX 192\nMvav 68,240,196\nTa 190,209,12288\nMvav 72,236,196\nEX 193\nMvav 76,232,196\nTa 192,208,8448\nCV 202,192\nMvav 80,228,196\n"
"s_waitcnt vmcnt(7)\nEX 194\nMvav 84,224,196\nDtL 212,44,75,2048\nMvav 88,220,196\nEX 195\nMvav 92,216,196\ns_waitcnt lgkmcnt(2)\nTa 194,208,12544\n"
"CV 203,194\nCvaa 188,248,80,160\nTa 196,209,8448\nMvaa 188,252,84\nEX 180\nMvva 188,216,88\nTa 198,209,12544\nMvva 188,220,92\nEX 181\n"
"v_smfmac_f32_16x16x64_bf16 a[164:167], a[180:183], v[196:203], v251\nTa 200,208,8704\nCV 196,180\nCvva 192,224,80,160\nEX 182\nMvva 192,228,84\n"
"DtL 213,44,75,3072\nMvva 192,232,88\nEX 183\nMvva 192,236,92\nTa 202,208,12800\nCV 197,182\nMvav 96,244,200\nEX 184\nMvav 100,240,200\n"
"Ta 204,209,8704\nMvav 104,236,200\nEX 185\nMvav 108,232,200\nTa 206,209,12800\nCV 198,184\nMvav 112,228,200\nEX 186\nMvav 116,224,200\n"
"Ta 208,208,8960\nMvav 120,220,200\nEX 187\nMvav 124,216,200\nTa 210,208,13056\nCV 199,186\nCvaa 180,248,96,164\nEX 188\nMvaa 180,252,100\n"
"Ta 212,209,8960\nMvva 180,216,104\nEX 189\nMvva 180,220,108\ns_memrealtime s[98:99]\nTa 214,209,13056\nCV 200,188\nCvva 184,224,96,164\nEX 190\n"
"Mvva 184,228,100\nMvva 184,232,104\nEX 191\nMvva 184,236,108\nCV 201,190\nMvav 128,244,196\ns_waitcnt vmcnt(12) lgkmcnt(0)\nEX 192\nMvav 132,240,196\n"
"s_barrier\n"
"s_sub_u32 s95, s98, s93\ns_mov_b32 s93, s98\ns_sub_u32 s95, 95, s95\ns_cbranch_scc1 .Lfa_t25b\ns_add_u32 s93, s93, s95\n.Lfa_t25a:\ns_sub_u32 s95, s95, 4\ns_cbranch_scc1 .Lfa_t25b\ns_sleep 1\ns_branch .Lfa_t25a\n.Lfa_t25b:\n"
"Mvav 136,236,196\ns_add_u32 m0, s72, 0x2000\nEX 193\nMvav 140,232,196\ns_waitcnt vmcnt(7)\nRa 244,204,32768\nCV 202,192\nMvav 144,228,196\nEX 194\n"
"Mvav 148,224,196\nDtL 214,48,75,0\nMvav 152,220,196\nEX 195\nMvav 156,216,196\nRa 240,205,32768\nCV 203,194\nCvaa 188,248,112,168\nRa 236,206,32768\n"
"Mvaa 188,252,116\nEX 180\nMvva 188,216,120\nDtL 215,48,75,1024\nMvva 188,220,124\nEX 181\n"
"v_smfmac_f32_16x16x64_bf16 a[168:171], a[180:183], v[196:203], v251\nRa 232,207,32768\nCV 196,180\nCvva 192,224,112,168\ns_waitcnt vmcnt(7)\nEX 182\n"
"Mvva 192,228,116\nDtL 214,48,75,2048\nMvva 192,232,120\nEX 183\nMvva 192,236,124\nRa 228,204,36864\nCV 197,182\nMvav 0,184,200\nEX 184\n"
"Mvav 4,188,200\nDtL 215,48,75,3072\nMvav 8,192,200\ns_add_u32 s75, s97, 0x1a000\nEX 185\nMvav 12,196,200\ns_and_b32 s75, s75, 0x1ffff\n"
"Ra 224,205,36864\nCV 198,184\nMvav 16,200,200\ns_add_u32 m0, s72, 0x8000\nEX 186\nMvav 20,204,200\ns_waitcnt vmcnt(7)\nRa 220,206,36864\n"
"v_add_u32 v204, 0x10000, v204\nMvav 24,208,200\nEX 187\nMvav 28,212,200\nRa 216,207,36864\nCV 199,186\nCvaa 180,248,128,172\nEX 188\n"
"Mvaa 180,252,132\nDtL 210,44,75,0\nMvva 180,216,136\nEX 189\nMvva 180,220,140\nCV 200,188\nv_add_u32 v205, 0x10000, v205\nCvva 184,224,128,172\n"
"EX 190\nMvva 184,228,132\nDtL 211,44,75,1024\nMvva 184,232,136\nEX 191\nMvva 184,236,140\nCV 201,190\nv_add_u32 v206, 0x10000, v206\nMvav 32,184,196\n"
"s_waitcnt vmcnt(7)\nEX 192\nMvav 36,188,196\nDtL 212,44,75,2048\nMvav 40,192,196\nEX 193\nMvav 44,196,196\nCV 202,192\nv_add_u32 v207, 0x10000, v207\n"
"Mvav 48,200,196\nEX 194\nMvav 52,204,196\nDtL 213,44,75,3072\nMvav 56,208,196\nEX 195\nMvav 60,212,196\ns_add_u32 m0, s72, 0xa000\nCV 203,194\n"
"Cvaa 188,248,144,176\ns_waitcnt vmcnt(7)\nTa 248,208,40960\nMvaa 188,252,148\nEX 180\nMvva 188,216,152\nDtL 214,48,75,0\nMvva 188,220,156\nEX 181\n"
"v_smfmac_f32_16x16x64_bf16 a[172:175], a[180:183], v[196:203], v251\nTa 250,208,45056\nCV 196,180\nCvva 192,224,144,176\nEX 182\nMvva 192,228,148\n"
"DtL 215,48,75,1024\nMvva 192,232,152\nEX 183\nMvva 192,236,156\nTa 252,209,40960\nCV 197,182\nMvav 64,184,200\ns_waitcnt vmcnt(7)\nEX 184\n"
"Mvav 68,188,200\nDtL 214,48,75,2048\nMvav 72,192,200\nEX 185\nMvav 76,196,200\nTa 254,209,45056\nCV 198,184\nMvav 80,200,200\nEX 186\n"
"Mvav 84,204,200\nDtL 215,48,75,3072\nMvav 88,208,200\nEX 187\nMvav 92,212,200\ns_waitcnt lgkmcnt(1)\nTv 216,208,41216\nCV 199,186\n"
"Cvaa 180,244,80,160\nEX 188\nMvaa 180,240,84\nTv 218,208,45312\nMvaa 180,236,88\nEX 189\nMvaa 180,232,92\nTv 220,209,41216\nCV 200,188\n"
"Cvaa 184,228,80,160\nEX 190\nMvaa 184,224,84\nTv 222,209,45312\nMvaa 184,220,88\nEX 191\nMvaa 184,216,92\nTv 224,208,41472\nCV 201,190\n"
"Mvav 96,184,196\nEX 192\nMvav 100,188,196\nTv 226,208,45568\nMvav 104,192,196\nEX 193\nMvav 108,196,196\nTv 228,209,41472\nCV 202,192\n"
"Mvav 112,200,196\nEX 194\nMvav 116,204,196\nTv 230,209,45568\nMvav 120,208,196\nEX 195\nMvav 124,212,196\nTv 232,208,41728\nCV 203,194\n"
"Cvaa 188,244,96,164\nTv 234,208,45824\nMvaa 188,240,100\nEX 180\nMvaa 188,236,104\nTv 236,209,41728\nv_add_u32 v208, 0x10000, v208\nMvaa 188,232,108\n"
"EX 181\nv_smfmac_f32_16x16x64_bf16 a[176:179], a[180:183], v[196:203], v251\ns_memrealtime s[98:99]\nTv 238,209,45824\nCV 196,180\n"
"Cvaa 192,228,96,164\nEX 182\nMvaa 192,224,100\nv_add_u32 v209, 0x10000, v209\nMvaa 192,220,104\nEX 183\nMvaa 192,216,108\nCV 197,182\n"
"Mvav 128,184,200\ns_waitcnt vmcnt(16) lgkmcnt(0)\nEX 184\nMvav 132,188,200\ns_barrier\n"
"s_sub_u32 s95, s98, s93\ns_mov_b32 s93, s98\ns_sub_u32 s95, 95, s95\ns_cbranch_scc1 .Lfa_t26b\ns_add_u32 s93, s93, s95\n.Lfa_t26a:\ns_sub_u32 s95, s95, 4\ns_cbranch_scc1 .Lfa_t26b\ns_sleep 1\ns_branch .Lfa_t26a\n.Lfa_t26b:\n"
"Mvav 136,192,200\ns_add_u32 s75, s97, 0x1c000\nEX 185\nMvav 140,196,200\ns_and_b32 s75, s75, 0x1ffff\nRa 184,204,0\nCV 198,184\nMvav 144,200,200\n"
"s_add_u32 m0, s72, 0x10000\nEX 186\nMvav 148,204,200\ns_waitcnt vmcnt(7)\nRa 188,205,0\nMvav 152,208,200\nEX 187\nMvav 156,212,200\nRa 192,206,0\n"
"CV 199,186\nCvaa 180,244,112,168\nEX 188\nMvaa 180,240,116\nDtL 210,44,75,0\nMvaa 180,236,120\nEX 189\nMvaa 180,232,124\nRa 196,207,0\nCV 200,188\n"
"Cvaa 184,228,112,168\nEX 190\nMvaa 184,224,116\nRa 200,204,4096\nMvaa 184,220,120\nEX 191\nMvaa 184,216,124\nRa 204,205,4096\nCV 201,190\n"
"Mvav 0,248,196\nEX 192\nMvav 4,252,196\nDtL 211,44,75,1024\nMvvv 8,216,196\nEX 193\nMvvv 12,220,196\nRa 208,206,4096\nCV 202,192\nMvvv 16,224,196\n"
"EX 194\nMvvv 20,228,196\nRa 212,207,4096\nv_add_u32 v204, 0xfffe0000, v204\nMvvv 24,232,196\nEX 195\nMvvv 28,236,196\nCV 203,194\n"
"v_add_u32 v205, 0xfffe0000, v205\nCvaa 188,244,128,172\ns_waitcnt vmcnt(7)\nv_add_u32 v206, 0xfffe0000, v206\nv_add_u32 v207, 0xfffe0000, v207\n"
"Mvaa 188,240,132\nEX 180\nMvaa 188,236,136\nDtL 212,44,75,2048\nMvaa 188,232,140\nEX 181\n"
"v_smfmac_f32_16x16x64_bf16 a[160:163], a[180:183], v[196:203], v251\nCV 196,180\nCvaa 192,228,128,172\nEX 182\nMvaa 192,224,132\nMvaa 192,220,136\n"
"EX 183\nMvaa 192,216,140\nCV 197,182\nMvav 32,248,200\nEX 184\nMvav 36,252,200\nDtL 213,44,75,3072\nMvvv 40,216,200\nEX 185\nMvvv 44,220,200\n"
"CV 198,184\nMvvv 48,224,200\nEX 186\nMvvv 52,228,200\nMvvv 56,232,200\nEX 187\nMvvv 60,236,200\ns_add_u32 m0, s72, 0x12000\nCV 199,186\n"
"Cvaa 180,244,144,176\ns_waitcnt vmcnt(7)\nEX 188\nMvaa 180,240,148\nDtL 214,48,75,0\nMvaa 180,236,152\nEX 189\nMvaa 180,232,156\nTa 244,208,8192\n"
"CV 200,188\nCvaa 184,228,144,176\nEX 190\nMvaa 184,224,148\nTa 246,208,12288\nMvaa 184,220,152\nEX 191\nMvaa 184,216,156\nTa 240,209,8192\n"
"CV 201,190\nMvav 64,248,196\nEX 192\nMvav 68,252,196\nDtL 215,48,75,1024\nMvvv 72,216,196\nEX 193\nMvvv 76,220,196\nTa 242,209,12288\nCV 202,192\n"
"Mvvv 80,224,196\nEX 194\nMvvv 84,228,196\nTa 236,208,8448\nMvvv 88,232,196\nEX 195\nMvvv 92,236,196\ns_waitcnt lgkmcnt(2)\nTa 238,208,12544\n"
"CV 203,194\nCvaa 188,184,80,160\ns_waitcnt vmcnt(7)\nTa 232,209,8448\nMvaa 188,188,84\nEX 180\nMvaa 188,192,88\nDtL 214,48,75,2048\nMvaa 188,196,92\n"
"EX 181\nv_smfmac_f32_16x16x64_bf16 a[164:167], a[180:183], v[196:203], v251\nTa 234,209,12544\nCV 196,180\nCvaa 192,200,80,160\nEX 182\n"
"Mvaa 192,204,84\nTa 228,208,8704\nMvaa 192,208,88\nEX 183\nMvaa 192,212,92\nTa 230,208,12800\nCV 197,182\nMvav 96,248,200\nEX 184\nMvav 100,252,200\n"
"DtL 215,48,75,3072\nMvvv 104,216,200\nEX 185\nMvvv 108,220,200\n.Lfa_pd5639:\ns_cbranch_execz .Lfa_wr2\nTa 224,209,8704\nCV 198,184\n"
"Mvvv 112,224,200\nEX 186\nMvvv 116,228,200\nTa 226,209,12800\nMvvv 120,232,200\nEX 187\nMvvv 124,236,200\nTa 220,208,8960\nCV 199,186\n"
"Cvaa 180,184,96,164\nEX 188\nMvaa 180,188,100\nTa 222,208,13056\nMvaa 180,192,104\nEX 189\nMvaa 180,196,108\ns_memrealtime s[98:99]\nTa 216,209,8960\n"
"CV 200,188\nCvaa 184,200,96,164\nEX 190\nMvaa 184,204,100\nTa 218,209,13056\nv_add_u32 v208, 0xfffe0000, v208\nMvaa 184,208,104\nEX 191\n"
"Mvaa 184,212,108\nCV 201,190\nv_add_u32 v209, 0xfffe0000, v209\nMvav 128,248,196\ns_waitcnt vmcnt(16) lgkmcnt(0)\nEX 192\nMvav 132,252,196\n"
"s_barrier\n"
"s_sub_u32 s95, s98, s93\ns_mov_b32 s93, s98\ns_sub_u32 s95, 95, s95\ns_cbranch_scc1 .Lfa_t27b\ns_add_u32 s93, s93, s95\n.Lfa_t27a:\ns_sub_u32 s95, s95, 4\ns_cbranch_scc1 .Lfa_t27b\ns_sleep 1\ns_branch .Lfa_t27a\n.Lfa_t27b:\n"
"Mvvv 136,216,196\ns_add_u32 s75, s97, 0x1e000\nEX 193\nMvvv 140,220,196\ns_and_b32 s75, s75, 0x1ffff\nRa 248,204,0\nCV 202,192\nMvvv 144,224,196\n"
"s_add_u32 m0, s72, 0x18000\nEX 194\nMvvv 148,228,196\ns_waitcnt vmcnt(7)\nRa 252,205,0\nMvvv 152,232,196\nEX 195\nMvvv 156,236,196\nRv 216,206,0\n"
"CV 203,194\nCvaa 188,184,112,168\nDtL 210,44,75,0\nMvaa 188,188,116\nEX 180\nMvaa 188,192,120\nRv 220,207,0\nMvaa 188,196,124\nEX 181\n"
"v_smfmac_f32_16x16x64_bf16 a[168:171], a[180:183], v[196:203], v251\nRv 224,204,4096\nCV 196,180\nCvaa 192,200,112,168\nEX 182\nMvaa 192,204,116\n"
"DtL 211,44,75,1024\nMvaa 192,208,120\nEX 183\nMvaa 192,212,124\nRv 228,205,4096\nCV 197,182\nMvav 0,244,200\ns_waitcnt vmcnt(7)\nEX 184\n"
"Mvav 4,240,200\nDtL 212,44,75,2048\nMvav 8,236,200\nEX 185\nMvav 12,232,200\nRv 232,206,4096\nCV 198,184\nMvav 16,228,200\nEX 186\nMvav 20,224,200\n"
"DtL 213,44,75,3072\nMvav 24,220,200\nEX 187\nMvav 28,216,200\ns_add_u32 m0, s72, 0x1a000\nRv 236,207,4096\nCV 199,186\nCvaa 180,184,128,172\n"
"s_waitcnt vmcnt(7)\nEX 188\nMvaa 180,188,132\nDtL 214,48,75,0\nMvaa 180,192,136\nEX 189\nMvaa 180,196,140\nCV 200,188\nCvaa 184,200,128,172\nEX 190\n"
"Mvaa 184,204,132\nDtL 215,48,75,1024\nMvaa 184,208,136\nEX 191\nMvaa 184,212,140\nCV 201,190\nMvav 32,244,196\ns_waitcnt vmcnt(7)\nEX 192\n"
"Mvav 36,240,196\nDtL 214,48,75,2048\nMvav 40,236,196\nEX 193\nMvav 44,232,196\nCV 202,192\nMvav 48,228,196\nEX 194\nMvav 52,224,196\n"
"DtL 215,48,75,3072\nMvav 56,220,196\ns_add_u32 s75, s97, 0x0\nEX 195\nMvav 60,216,196\ns_and_b32 s75, s75, 0x1ffff\nCV 203,194\nCvaa 188,184,144,176\n"
"s_add_u32 s75, s75, 0x40000\nTa 184,208,8192\nMvaa 188,188,148\ns_add_u32 m0, s72, 0x20000\nEX 180\nMvaa 188,192,152\ns_waitcnt vmcnt(7)\n"
"Ta 186,208,12288\nMvaa 188,196,156\nEX 181\nv_smfmac_f32_16x16x64_bf16 a[172:175], a[180:183], v[196:203], v251\nTa 188,209,8192\nCV 196,180\n"
"Cvaa 192,200,144,176\nEX 182\nMvaa 192,204,148\nDtL 210,44,75,0\nMvaa 192,208,152\nEX 183\nMvaa 192,212,156\nTa 190,209,12288\nCV 197,182\n"
"Mvav 64,244,200\nEX 184\nMvav 68,240,200\nDtL 211,44,75,1024\nMvav 72,236,200\nEX 185\nMvav 76,232,200\nTa 192,208,8448\nCV 198,184\nMvav 80,228,200\n"
"s_waitcnt vmcnt(7)\nEX 186\nMvav 84,224,200\nDtL 212,44,75,2048\nMvav 88,220,200\nEX 187\nMvav 92,216,200\ns_waitcnt lgkmcnt(1)\nTa 194,208,12544\n"
"CV 199,186\nCvaa 180,248,80,160\nEX 188\nMvaa 180,252,84\nDtL 213,44,75,3072\nMvva 180,216,88\nEX 189\nMvva 180,220,92\nTa 196,209,8448\nCV 200,188\n"
"Cvva 184,224,80,160\nEX 190\nMvva 184,228,84\nTa 198,209,12544\nMvva 184,232,88\nEX 191\nMvva 184,236,92\nTa 200,208,8704\nCV 201,190\n"
"Mvav 96,244,196\nEX 192\nMvav 100,240,196\nTa 202,208,12800\nMvav 104,236,196\nEX 193\nMvav 108,232,196\nTa 204,209,8704\nCV 202,192\n"
"Mvav 112,228,196\nEX 194\nMvav 116,224,196\nTa 206,209,12800\nMvav 120,220,196\nEX 195\nMvav 124,216,196\nTa 208,208,8960\nCV 203,194\n"
"Cvaa 188,248,96,164\nTa 210,208,13056\nMvaa 188,252,100\nEX 180\nMvva 188,216,104\nTa 212,209,8960\nMvva 188,220,108\nEX 181\n"
"v_smfmac_f32_16x16x64_bf16 a[176:179], a[180:183], v[196:203], v251\ns_memrealtime s[98:99]\nTa 214,209,13056\nCV 196,180\nCvva 192,224,96,164\n"
"EX 182\nMvva 192,228,100\nMvva 192,232,104\nEX 183\nMvva 192,236,108\nCV 197,182\nMvav 128,244,200\ns_waitcnt vmcnt(20) lgkmcnt(0)\nEX 184\n"
"Mvav 132,240,200\ns_barrier\n"
"s_sub_u32 s95, s98, s93\ns_mov_b32 s93, s98\ns_sub_u32 s95, 95, s95\ns_cbranch_scc1 .Lfa_t28b\ns_add_u32 s93, s93, s95\n.Lfa_t28a:\ns_sub_u32 s95, s95, 4\ns_cbranch_scc1 .Lfa_t28b\ns_sleep 1\ns_branch .Lfa_t28a\n.Lfa_t28b:\n"
"Mvav 136,236,200\ns_add_u32 m0, s72, 0x22000\nEX 185\nMvav 140,232,200\ns_waitcnt vmcnt(7)\nRa 244,204,32768\nCV 198,184\nMvav 144,228,200\nEX 186\n"
"Mvav 148,224,200\nDL 214,48,75,0\nMvav 152,220,200\nEX 187\nMvav 156,216,200\nRa 240,205,32768\nCV 199,186\nCvaa 180,248,112,168\nEX 188\n"
"Mvaa 180,252,116\nRa 236,206,32768\nMvva 180,216,120\nEX 189\nMvva 180,220,124\nRa 232,207,32768\nCV 200,188\nCvva 184,224,112,168\nEX 190\n"
"Mvva 184,228,116\nDL 215,48,75,1024\nMvva 184,232,120\nEX 191\nMvva 184,236,124\nRa 228,204,36864\nCV 201,190\nMvav 0,184,196\nEX 192\n"
"Mvav 4,188,196\nRa 224,205,36864\nv_add_u32 v204, 0x10000, v204\nMvav 8,192,196\nEX 193\nMvav 12,196,196\nRa 220,206,36864\nCV 202,192\n"
"Mvav 16,200,196\ns_waitcnt vmcnt(7)\nEX 194\nMvav 20,204,196\nDL 214,48,75,2048\nMvav 24,208,196\nEX 195\nMvav 28,212,196\nRa 216,207,36864\n"
"CV 203,194\nCvaa 188,248,128,172\nv_add_u32 v205, 0x10000, v205\nv_add_u32 v206, 0x10000, v206\nMvaa 188,252,132\nEX 180\nMvva 188,216,136\n"
"v_add_u32 v207, 0x10000, v207\nMvva 188,220,140\nEX 181\nv_smfmac_f32_16x16x64_bf16 a[160:163], a[180:183], v[196:203], v251\nCV 196,180\n"
"Cvva 192,224,128,172\nEX 182\nMvva 192,228,132\nDL 215,48,75,3072\nMvva 192,232,136\nEX 183\nMvva 192,236,140\nCV 197,182\nMvav 32,184,200\n"
"s_add_u32 s75, s97, 0x2000\nEX 184\nMvav 36,188,200\ns_and_b32 s75, s75, 0x1ffff\nMvav 40,192,200\ns_add_u32 s75, s75, 0x40000\nEX 185\n"
"Mvav 44,196,200\ns_add_u32 m0, s72, 0x0\nCV 198,184\nMvav 48,200,200\ns_waitcnt vmcnt(7)\nEX 186\nMvav 52,204,200\nDtL 210,44,75,0\nMvav 56,208,200\n"
"EX 187\nMvav 60,212,200\nCV 199,186\nCvaa 180,248,144,176\nEX 188\nMvaa 180,252,148\nTa 248,208,40960\nMvva 180,216,152\nEX 189\nMvva 180,220,156\n"
"Ta 250,208,45056\nCV 200,188\nCvva 184,224,144,176\nEX 190\nMvva 184,228,148\nDtL 211,44,75,1024\nMvva 184,232,152\nEX 191\nMvva 184,236,156\n"
"Ta 252,209,40960\nCV 201,190\nMvav 64,184,196\nEX 192\nMvav 68,188,196\nTa 254,209,45056\nMvav 72,192,196\nEX 193\nMvav 76,196,196\nTv 216,208,41216\n"
"CV 202,192\nMvav 80,200,196\ns_waitcnt vmcnt(7)\nEX 194\nMvav 84,204,196\nDtL 212,44,75,2048\nMvav 88,208,196\nEX 195\nMvav 92,212,196\n"
"s_waitcnt lgkmcnt(2)\nTv 218,208,45312\nCV 203,194\nCvaa 188,244,80,160\nTv 220,209,41216\nMvaa 188,240,84\nEX 180\nMvaa 188,236,88\n"
"Tv 222,209,45312\nMvaa 188,232,92\nEX 181\nv_smfmac_f32_16x16x64_bf16 a[164:167], a[180:183], v[196:203], v251\nTv 224,208,41472\nCV 196,180\n"
"Cvaa 192,228,80,160\nEX 182\nMvaa 192,224,84\nDtL 213,44,75,3072\nMvaa 192,220,88\nEX 183\nMvaa 192,216,92\nTv 226,208,45568\nCV 197,182\n"
"Mvav 96,184,200\nEX 184\nMvav 100,188,200\nTv 228,209,41472\nMvav 104,192,200\nEX 185\nMvav 108,196,200\nTv 230,209,45568\nCV 198,184\n"
"Mvav 112,200,200\nEX 186\nMvav 116,204,200\nTv 232,208,41728\nMvav 120,208,200\nEX 187\nMvav 124,212,200\nTv 234,208,45824\nCV 199,186\n"
"Cvaa 180,244,96,164\nEX 188\nMvaa 180,240,100\nTv 236,209,41728\nv_add_u32 v208, 0x10000, v208\nMvaa 180,236,104\nEX 189\nMvaa 180,232,108\n"
"s_memrealtime s[98:99]\nTv 238,209,45824\nCV 200,188\nCvaa 184,228,96,164\nEX 190\nMvaa 184,224,100\nv_add_u32 v209, 0x10000, v209\nMvaa 184,220,104\n"
"EX 191\nMvaa 184,216,108\nCV 201,190\nMvav 128,184,196\ns_waitcnt vmcnt(20) lgkmcnt(0)\nEX 192\nMvav 132,188,196\ns_barrier\n"
"s_sub_u32 s95, s98, s93\ns_mov_b32 s93, s98\ns_sub_u32 s95, 95, s95\ns_cbranch_scc1 .Lfa_t29b\ns_add_u32 s93, s93, s95\n.Lfa_t29a:\ns_sub_u32 s95, s95, 4\ns_cbranch_scc1 .Lfa_t29b\ns_sleep 1\ns_branch .Lfa_t29a\n.Lfa_t29b:\n"
"Mvav 136,192,196\ns_add_u32 m0, s72, 0x2000\nEX 193\nMvav 140,196,196\ns_waitcnt vmcnt(7)\nRa 184,204,0\nCV 202,192\nMvav 144,200,196\nEX 194\n"
"Mvav 148,204,196\nDL 214,48,75,0\nMvav 152,208,196\nEX 195\nMvav 156,212,196\nRa 188,205,0\nCV 203,194\nCvaa 188,244,112,168\nRa 192,206,0\n"
"v_mbcnt_lo_u32_b32 v253, -1, 0\nMvaa 188,240,116\nEX 180\nMvaa 188,236,120\nDL 215,48,75,1024\nMvaa 188,232,124\nEX 181\n"
"v_smfmac_f32_16x16x64_bf16 a[168:171], a[180:183], v[196:203], v251\nRa 196,207,0\nCV 196,180\nCvaa 192,228,112,168\ns_waitcnt vmcnt(7)\nEX 182\n"
"Mvaa 192,224,116\nDL 214,48,75,2048\nMvaa 192,220,120\nEX 183\nMvaa 192,216,124\nRa 200,204,4096\nCV 197,182\nMvav 0,248,200\nEX 184\nMvav 4,252,200\n"
"DL 215,48,75,3072\nMvvv 8,216,200\ns_add_u32 s75, s97, 0x4000\nEX 185\nMvvv 12,220,200\ns_and_b32 s75, s75, 0x1ffff\nRa 204,205,4096\nCV 198,184\n"
"Mvvv 16,224,200\ns_add_u32 s75, s75, 0x40000\nEX 186\nMvvv 20,228,200\ns_add_u32 m0, s72, 0x8000\nRa 208,206,4096\nv_mbcnt_hi_u32_b32 v253, -1, v253\n"
"Mvvv 24,232,200\ns_waitcnt vmcnt(7)\nEX 187\nMvvv 28,236,200\ns_lshl_b32 s88, s83, 14\nRa 212,207,4096\nCV 199,186\nCvaa 180,244,128,172\n"
"s_add_u32 s88, s88, 0x20000\nEX 188\nMvaa 180,240,132\nDtL 210,44,75,0\nMvaa 180,236,136\nEX 189\nMvaa 180,232,140\nCV 200,188\n"
"v_and_b32 v253, 48, v253\nCvaa 184,228,128,172\nEX 190\nMvaa 184,224,132\nDtL 211,44,75,1024\nMvaa 184,220,136\nEX 191\nMvaa 184,216,140\nCV 201,190\n"
"v_add_u32 v253, s88, v253\nMvav 32,248,196\ns_waitcnt vmcnt(7)\nEX 192\nMvav 36,252,196\nDtL 212,44,75,2048\nMvvv 40,216,196\nEX 193\n"
"Mvvv 44,220,196\nCV 202,192\nMvvv 48,224,196\nEX 194\nMvvv 52,228,196\nDtL 213,44,75,3072\nMvvv 56,232,196\nEX 195\nMvvv 60,236,196\n"
"s_add_u32 m0, s72, 0xa000\nCV 203,194\nCvaa 188,244,144,176\ns_waitcnt vmcnt(7)\nTa 244,208,8192\nMvaa 188,240,148\nEX 180\nMvaa 188,236,152\n"
"DL 214,48,75,0\nMvaa 188,232,156\nEX 181\nv_smfmac_f32_16x16x64_bf16 a[172:175], a[180:183], v[196:203], v251\nTa 246,208,12288\nCV 196,180\n"
"Cvaa 192,228,144,176\nEX 182\nMvaa 192,224,148\nDL 215,48,75,1024\nMvaa 192,220,152\nEX 183\nMvaa 192,216,156\nTa 240,209,8192\nCV 197,182\n"
"Mvav 64,248,200\ns_waitcnt vmcnt(7)\nEX 184\nMvav 68,252,200\nDL 214,48,75,2048\nMvvv 72,216,200\nEX 185\nMvvv 76,220,200\nTa 242,209,12288\n"
"CV 198,184\nMvvv 80,224,200\nEX 186\nMvvv 84,228,200\nDL 215,48,75,3072\nMvvv 88,232,200\nEX 187\nMvvv 92,236,200\ns_waitcnt lgkmcnt(1)\n"
"Ta 236,208,8448\nCV 199,186\nCvaa 180,184,80,160\nEX 188\nMvaa 180,188,84\n.Lfa_pd6213:\ns_cbranch_execz .Lfa_wr2\nTa 238,208,12544\nMvaa 180,192,88\n"
"EX 189\nMvaa 180,196,92\nTa 232,209,8448\nCV 200,188\nCvaa 184,200,80,160\nEX 190\nMvaa 184,204,84\nTa 234,209,12544\nMvaa 184,208,88\nEX 191\n"
"Mvaa 184,212,92\nTa 228,208,8704\nCV 201,190\nMvav 96,248,196\nEX 192\nMvav 100,252,196\nTa 230,208,12800\nMvvv 104,216,196\nEX 193\n"
"Mvvv 108,220,196\nTa 224,209,8704\nCV 202,192\nMvvv 112,224,196\nEX 194\nMvvv 116,228,196\nTa 226,209,12800\nMvvv 120,232,196\nEX 195\n"
"Mvvv 124,236,196\nTa 220,208,8960\nCV 203,194\nCvaa 188,184,96,164\nTa 222,208,13056\nMvaa 188,188,100\nEX 180\nMvaa 188,192,104\nTa 216,209,8960\n"
"Mvaa 188,196,108\nEX 181\nv_smfmac_f32_16x16x64_bf16 a[176:179], a[180:183], v[196:203], v251\ns_memrealtime s[98:99]\nTa 218,209,13056\nCV 196,180\n"
"Cvaa 192,200,96,164\nEX 182\nMvaa 192,204,100\nMvaa 192,208,104\nEX 183\nMvaa 192,212,108\nCV 197,182\nMvav 128,248,200\n"
"s_waitcnt vmcnt(24) lgkmcnt(0)\nEX 184\nMvav 132,252,200\ns_barrier\n"
"s_sub_u32 s95, s98, s93\ns_mov_b32 s93, s98\ns_sub_u32 s95, 95, s95\ns_cbranch_scc1 .Lfa_t30b\ns_add_u32 s93, s93, s95\n.Lfa_t30a:\ns_sub_u32 s95, s95, 4\ns_cbranch_scc1 .Lfa_t30b\ns_sleep 1\ns_branch .Lfa_t30a\n.Lfa_t30b:\n"
"Mvvv 136,216,200\ns_add_u32 s75, s97, 0x6000\nEX 185\nMvvv 140,220,200\ns_and_b32 s75, s75, 0x1ffff\nRa 248,204,32768\nCV 198,184\nMvvv 144,224,200\n"
"s_add_u32 s75, s75, 0x40000\nEX 186\nMvvv 148,228,200\ns_add_u32 m0, s72, 0x10000\nRa 252,205,32768\nMvvv 152,232,200\ns_waitcnt vmcnt(7)\nEX 187\n"
"Mvvv 156,236,200\nRv 216,206,32768\nCV 199,186\nCvaa 180,184,112,168\nEX 188\nMvaa 180,188,116\nDtL 210,44,75,0\nMvaa 180,192,120\nEX 189\n"
"Mvaa 180,196,124\nRv 220,207,32768\nCV 200,188\nCvaa 184,200,112,168\nEX 190\nMvaa 184,204,116\nRv 224,204,36864\nMvaa 184,208,120\nEX 191\n"
"Mvaa 184,212,124\nRv 228,205,36864\nCV 201,190\nMvav 0,244,196\nEX 192\nMvav 4,240,196\nDtL 211,44,75,1024\nMvav 8,236,196\nEX 193\nMvav 12,232,196\n"
"Rv 232,206,36864\nCV 202,192\nMvav 16,228,196\nEX 194\nMvav 20,224,196\nRv 236,207,36864\nv_add_u32 v204, 0x10000, v204\nMvav 24,220,196\nEX 195\n"
"Mvav 28,216,196\nCV 203,194\nv_add_u32 v205, 0x10000, v205\nCvaa 188,184,128,172\ns_waitcnt vmcnt(7)\nv_add_u32 v206, 0x10000, v206\n"
"v_add_u32 v207, 0x10000, v207\nMvaa 188,188,132\nEX 180\nMvaa 188,192,136\nDtL 212,44,75,2048\nMvaa 188,196,140\nEX 181\n"
"v_smfmac_f32_16x16x64_bf16 a[160:163], a[180:183], v[196:203], v251\nCV 196,180\nCvaa 192,200,128,172\nEX 182\nMvaa 192,204,132\nMvaa 192,208,136\n"
"EX 183\nMvaa 192,212,140\nCV 197,182\nMvav 32,244,200\nEX 184\nMvav 36,240,200\nDtL 213,44,75,3072\nMvav 40,236,200\nEX 185\nMvav 44,232,200\n"
"CV 198,184\nMvav 48,228,200\nEX 186\nMvav 52,224,200\nMvav 56,220,200\nEX 187\nMvav 60,216,200\ns_add_u32 m0, s72, 0x12000\nCV 199,186\n"
"Cvaa 180,184,144,176\ns_waitcnt vmcnt(7)\nEX 188\nMvaa 180,188,148\nDL 214,48,75,0\nMvaa 180,192,152\nEX 189\nMvaa 180,196,156\nTa 184,208,40960\n"
"CV 200,188\nCvaa 184,200,144,176\nEX 190\nMvaa 184,204,148\nTa 186,208,45056\nMvaa 184,208,152\nEX 191\nMvaa 184,212,156\nTa 188,209,40960\n"
"CV 201,190\nMvav 64,244,196\nEX 192\nMvav 68,240,196\nDL 215,48,75,1024\nMvav 72,236,196\nEX 193\nMvav 76,232,196\nTa 190,209,45056\nCV 202,192\n"
"Mvav 80,228,196\nEX 194\nMvav 84,224,196\nTa 192,208,41216\nMvav 88,220,196\nEX 195\nMvav 92,216,196\ns_waitcnt lgkmcnt(2)\nTa 194,208,45312\n"
"CV 203,194\nCvaa 188,248,80,160\ns_waitcnt vmcnt(7)\nTa 196,209,41216\nMvaa 188,252,84\nEX 180\nMvva 188,216,88\nDL 214,48,75,2048\nMvva 188,220,92\n"
"EX 181\nv_smfmac_f32_16x16x64_bf16 a[164:167], a[180:183], v[196:203], v251\nTa 198,209,45312\nCV 196,180\nCvva 192,224,80,160\nEX 182\n"
"Mvva 192,228,84\nTa 200,208,41472\nMvva 192,232,88\nEX 183\nMvva 192,236,92\nTa 202,208,45568\nCV 197,182\nMvav 96,244,200\nEX 184\nMvav 100,240,200\n"
"DL 215,48,75,3072\nMvav 104,236,200\nEX 185\nMvav 108,232,200\nTa 204,209,41472\nCV 198,184\nMvav 112,228,200\nEX 186\nMvav 116,224,200\n"
"Ta 206,209,45568\nMvav 120,220,200\nEX 187\nMvav 124,216,200\nTa 208,208,41728\nCV 199,186\nCvaa 180,248,96,164\nEX 188\nMvaa 180,252,100\n"
"Ta 210,208,45824\nMvva 180,216,104\nEX 189\nMvva 180,220,108\ns_memrealtime s[98:99]\nTa 212,209,41728\nCV 200,188\nCvva 184,224,96,164\nEX 190\n"
"Mvva 184,228,100\nTa 214,209,45824\nv_add_u32 v208, 0x10000, v208\nMvva 184,232,104\nEX 191\nMvva 184,236,108\nCV 201,190\n"
"v_add_u32 v209, 0x10000, v209\nMvav 128,244,196\ns_waitcnt vmcnt(24) lgkmcnt(0)\nEX 192\nMvav 132,240,196\ns_barrier\n"
"s_sub_u32 s95, s98, s93\ns_mov_b32 s93, s98\ns_sub_u32 s95, 95, s95\ns_cbranch_scc1 .Lfa_t31b\ns_add_u32 s93, s93, s95\n.Lfa_t31a:\ns_sub_u32 s95, s95, 4\ns_cbranch_scc1 .Lfa_t31b\ns_sleep 1\ns_branch .Lfa_t31a\n.Lfa_t31b:\n"
"Mvav 136,236,196\ns_add_u32 s75, s97, 0x8000\nEX 193\nMvav 140,232,196\ns_and_b32 s75, s75, 0x1ffff\nRa 244,204,0\nCV 202,192\nMvav 144,228,196\n"
"s_add_u32 s75, s75, 0x40000\nEX 194\nMvav 148,224,196\ns_add_u32 m0, s72, 0x18000\nRa 240,205,0\nMvav 152,220,196\ns_waitcnt vmcnt(7)\nEX 195\n"
"Mvav 156,216,196\nRa 236,206,0\nCV 203,194\nCvaa 188,248,112,168\nDtL 210,44,75,0\nMvaa 188,252,116\nEX 180\nMvva 188,216,120\nRa 232,207,0\n"
"Mvva 188,220,124\nEX 181\nv_smfmac_f32_16x16x64_bf16 a[168:171], a[180:183], v[196:203], v251\nRa 228,204,4096\nCV 196,180\nCvva 192,224,112,168\n"
"EX 182\nMvva 192,228,116\nRa 224,205,4096\nv_add_u32 v204, 0xfffe0000, v204\nMvva 192,232,120\nEX 183\nMvva 192,236,124\nRa 220,206,4096\nCV 197,182\n"
"Mvav 0,184,200\nEX 184\nMvav 4,188,200\nDtL 211,44,75,1024\nMvav 8,192,200\nEX 185\nMvav 12,196,200\nRa 216,207,4096\nCV 198,184\nMvav 16,200,200\n"
"EX 186\nMvav 20,204,200\nv_add_u32 v205, 0xfffe0000, v205\nv_add_u32 v206, 0xfffe0000, v206\nMvav 24,208,200\nEX 187\nMvav 28,212,200\nCV 199,186\n"
"v_add_u32 v207, 0xfffe0000, v207\nCvaa 180,248,128,172\ns_waitcnt vmcnt(7)\nEX 188\nMvaa 180,252,132\nDtL 212,44,75,2048\nMvva 180,216,136\nEX 189\n"
"Mvva 180,220,140\nCV 200,188\nCvva 184,224,128,172\nEX 190\nMvva 184,228,132\nMvva 184,232,136\nEX 191\nMvva 184,236,140\nCV 201,190\n"
"Mvav 32,184,196\nEX 192\nMvav 36,188,196\nDtL 213,44,75,3072\nMvav 40,192,196\nEX 193\nMvav 44,196,196\nCV 202,192\nMvav 48,200,196\nEX 194\n"
"Mvav 52,204,196\nMvav 56,208,196\nEX 195\nMvav 60,212,196\ns_add_u32 m0, s72, 0x1a000\nCV 203,194\nCvaa 188,248,144,176\ns_waitcnt vmcnt(7)\n"
"Ta 248,208,8192\nMvaa 188,252,148\nEX 180\nMvva 188,216,152\nDL 214,48,75,0\nMvva 188,220,156\nEX 181\n"
"v_smfmac_f32_16x16x64_bf16 a[172:175], a[180:183], v[196:203], v251\nTa 250,208,12288\nCV 196,180\nCvva 192,224,144,176\nEX 182\nMvva 192,228,148\n"
"Ta 252,209,8192\nMvva 192,232,152\nEX 183\nMvva 192,236,156\nTa 254,209,12288\nCV 197,182\nMvav 64,184,200\nEX 184\nMvav 68,188,200\n"
"DL 215,48,75,1024\nMvav 72,192,200\nEX 185\nMvav 76,196,200\nTv 216,208,8448\nCV 198,184\nMvav 80,200,200\nEX 186\nMvav 84,204,200\nTv 218,208,12544\n"
"Mvav 88,208,200\nEX 187\nMvav 92,212,200\nCV 199,186\ns_nop 1\nMvav 96,184,196\ns_waitcnt vmcnt(7)\nEX 188\nMvav 100,188,196\nEX 189\n"
"Mvav 104,192,196\nEX 190\nCV 200,188\nMvav 108,196,196\nEX 191\nMvav 112,200,196\nEX 192\nCV 201,190\nMvav 116,204,196\nEX 193\nMvav 120,208,196\n"
"EX 194\nCV 202,192\nMvav 124,212,196\nEX 195\ns_nop 0\nCV 203,194\ns_nop 1\nMvav 128,184,200\nDL 214,48,75,2048\nMvav 132,188,200\nTv 220,209,8448\n"
"Mvav 136,192,200\nTv 222,209,12544\nMvav 140,196,200\nTv 224,208,8704\nv_smfmac_f32_16x16x64_bf16 a[176:179], a[180:183], v[196:203], v251\n"
"Tv 226,208,12800\nMvav 144,200,200\nTv 228,209,8704\nMvav 148,204,200\nTv 230,209,12800\nMvav 152,208,200\nTv 232,208,8960\nMvav 156,212,200\n"
"DL 215,48,75,3072\nTv 234,208,13056\nTv 236,209,8960\nTv 238,209,13056\ns_nop 7\nv_accvgpr_read_b32 v160, a160\nv_accvgpr_read_b32 v161, a161\n"
"v_accvgpr_read_b32 v162, a162\nv_accvgpr_read_b32 v163, a163\nv_accvgpr_read_b32 v164, a164\nv_accvgpr_read_b32 v165, a165\n"
"v_accvgpr_read_b32 v166, a166\nv_accvgpr_read_b32 v167, a167\nv_accvgpr_read_b32 v168, a168\nv_accvgpr_read_b32 v169, a169\n"
"v_accvgpr_read_b32 v170, a170\nv_accvgpr_read_b32 v171, a171\nv_accvgpr_read_b32 v172, a172\nv_accvgpr_read_b32 v173, a173\n"
"v_accvgpr_read_b32 v174, a174\nv_accvgpr_read_b32 v175, a175\nv_accvgpr_read_b32 v176, a176\nv_accvgpr_read_b32 v177, a177\n"
"v_accvgpr_read_b32 v178, a178\nv_accvgpr_read_b32 v179, a179\nAD 160,161\nAD 162,163\nAD 164,165\nAD 166,167\nAD 168,169\nAD 170,171\nAD 172,173\n"
"AD 174,175\nAD 176,177\nAD 178,179\nv_add_f32 v180, v160, v170\nv_add_f32 v182, v172, v162\nv_add_f32 v184, v164, v174\nv_add_f32 v186, v176, v166\n"
"v_add_f32 v188, v168, v178\nv_add_f32 v161, v180, v182\nv_add_f32 v162, v184, v186\nAD 161,188\nAD 161,162\nv_rcp_f32 v180, v180\n"
"v_rcp_f32 v182, v182\nv_rcp_f32 v184, v184\nv_rcp_f32 v186, v186\nv_rcp_f32 v188, v188\nv_add_f32 v162, v180, v182\nv_add_f32 v163, v184, v186\n"
"AD 161,162\nAD 163,188\nAD 161,163\nv_cmp_ngt_f32 vcc, s82, v161\ns_nop 1\ns_cmp_lg_u64 vcc, 0\ns_cselect_b32 s79, 1, s79\nPV 0,180\nPV 2,180\n"
"PV 4,180\nPV 6,180\nPV 8,180\nPV 10,180\nPV 12,180\nPV 14,180\nPV 16,180\nPV 18,180\nPV 20,180\nPV 22,180\nPV 24,180\nPV 26,180\nPV 28,180\n"
"PV 30,180\nPV 32,182\nPV 34,182\nPV 36,182\nPV 38,182\nPV 40,182\nPV 42,182\nPV 44,182\nPV 46,182\nPV 48,182\nPV 50,182\nPV 52,182\nPV 54,182\n"
"PV 56,182\nPV 58,182\nPV 60,182\nPV 62,182\nPV 64,184\nPV 66,184\nPV 68,184\nPV 70,184\nPV 72,184\nPV 74,184\nPV 76,184\nPV 78,184\nPV 80,184\n"
"PV 82,184\nPV 84,184\nPV 86,184\nPV 88,184\nPV 90,184\nPV 92,184\nPV 94,184\nPV 96,186\nPV 98,186\nPV 100,186\nPV 102,186\nPV 104,186\nPV 106,186\n"
"PV 108,186\nPV 110,186\nPV 112,186\nPV 114,186\nPV 116,186\nPV 118,186\nPV 120,186\nPV 122,186\nPV 124,186\nPV 126,186\nPV 128,188\nPV 130,188\n"
"PV 132,188\nPV 134,188\nPV 136,188\nPV 138,188\nPV 140,188\nPV 142,188\nPV 144,188\nPV 146,188\nPV 148,188\nPV 150,188\nPV 152,188\nPV 154,188\n"
"PV 156,188\nPV 158,188\nRv 180,253,0\nRv 184,253,64\nRv 188,253,128\nRv 192,253,192\nv_accvgpr_write_b32 a160, 0\nv_accvgpr_write_b32 a161, 0\n"
"v_accvgpr_write_b32 a162, 0\nv_accvgpr_write_b32 a163, 0\nv_accvgpr_write_b32 a164, 0\nv_accvgpr_write_b32 a165, 0\nv_accvgpr_write_b32 a166, 0\n"
".Lfa_pd6785:\ns_cbranch_execz .Lfa_wr2\nv_accvgpr_write_b32 a167, 0\nv_accvgpr_write_b32 a168, 0\nv_accvgpr_write_b32 a169, 0\n"
"v_accvgpr_write_b32 a170, 0\nv_accvgpr_write_b32 a171, 0\nv_accvgpr_write_b32 a172, 0\nv_accvgpr_write_b32 a173, 0\nv_accvgpr_write_b32 a174, 0\n"
"v_accvgpr_write_b32 a175, 0\nv_accvgpr_write_b32 a176, 0\nv_accvgpr_write_b32 a177, 0\nv_accvgpr_write_b32 a178, 0\nv_accvgpr_write_b32 a179, 0\n"
"CV 0,0\nCV 1,2\nCV 2,4\nCV 3,6\nAW 80,0\nAW 81,1\nAW 82,2\nAW 83,3\nCV 8,8\nCV 9,10\nCV 10,12\nCV 11,14\nAW 84,8\nAW 85,9\nAW 86,10\nAW 87,11\n"
"CV 16,16\nCV 17,18\nCV 18,20\nCV 19,22\nAW 88,16\nAW 89,17\nAW 90,18\nAW 91,19\nCV 24,24\nCV 25,26\nCV 26,28\nCV 27,30\nAW 92,24\nAW 93,25\nAW 94,26\n"
"AW 95,27\ns_waitcnt lgkmcnt(3)\nv_xor_b32 v180, s81, v180\nv_xor_b32 v181, s81, v181\nv_xor_b32 v182, s81, v182\nv_xor_b32 v183, s81, v183\n"
"s_waitcnt lgkmcnt(2)\nv_xor_b32 v184, s81, v184\nv_xor_b32 v185, s81, v185\nv_xor_b32 v186, s81, v186\nv_xor_b32 v187, s81, v187\n"
"s_waitcnt lgkmcnt(1)\nv_xor_b32 v188, s81, v188\nv_xor_b32 v189, s81, v189\nv_xor_b32 v190, s81, v190\nv_xor_b32 v191, s81, v191\n"
"s_waitcnt lgkmcnt(0)\nv_xor_b32 v192, s81, v192\nv_xor_b32 v193, s81, v193\nv_xor_b32 v194, s81, v194\nv_xor_b32 v195, s81, v195\ns_nop 1\nCV 32,32\n"
"CV 33,34\nZvva 160,180,0\nCV 34,36\nCV 35,38\nAW 96,32\nAW 97,33\nMvva 160,184,4\nAW 98,34\nAW 99,35\nCV 40,40\nCV 41,42\nMvva 160,188,8\nCV 42,44\n"
"CV 43,46\nAW 100,40\nAW 101,41\nMvva 160,192,12\nAW 102,42\nAW 103,43\nCV 48,48\nCV 49,50\nZvva 164,180,16\nCV 50,52\nCV 51,54\nAW 104,48\nAW 105,49\n"
"Mvva 164,184,20\nAW 106,50\nAW 107,51\nCV 56,56\nCV 57,58\nMvva 164,188,24\nCV 58,60\nCV 59,62\nAW 108,56\nAW 109,57\nMvva 164,192,28\nAW 110,58\n"
"AW 111,59\nCV 64,64\nCV 65,66\nZvva 168,180,32\nCV 66,68\nCV 67,70\nAW 112,64\nAW 113,65\nMvva 168,184,36\nAW 114,66\nAW 115,67\nCV 72,72\nCV 73,74\n"
"Mvva 168,188,40\nCV 74,76\nCV 75,78\nAW 116,72\nAW 117,73\nMvva 168,192,44\nAW 118,74\nAW 119,75\nCV 80,80\nCV 81,82\nZvva 172,180,48\nCV 82,84\n"
"CV 83,86\nAW 120,80\nAW 121,81\nAW 122,82\nAW 123,83\nCV 88,88\nCV 89,90\nCV 90,92\nCV 91,94\nAW 124,88\nAW 125,89\nMvva 172,184,52\nAW 126,90\n"
"AW 127,91\nCV 96,96\nCV 97,98\nCV 98,100\nCV 99,102\nAW 128,96\nAW 129,97\nAW 130,98\nAW 131,99\nCV 104,104\nCV 105,106\nMvva 172,188,56\nCV 106,108\n"
"CV 107,110\nAW 132,104\nAW 133,105\nAW 134,106\nAW 135,107\nCV 112,112\nCV 113,114\nCV 114,116\nCV 115,118\nAW 136,112\nAW 137,113\nMvva 172,192,60\n"
"AW 138,114\nAW 139,115\nCV 120,120\nCV 121,122\nCV 122,124\nCV 123,126\nAW 140,120\nAW 141,121\nAW 142,122\nAW 143,123\ns_nop 1\nZvva 176,180,64\n"
"Mvva 176,184,68\nMvva 176,188,72\nMvva 176,192,76\nCvaa 180,244,0,160\nMvaa 180,240,4\nMvaa 180,236,8\nMvaa 180,232,12\nCvaa 184,228,0,160\n"
"Mvaa 184,224,4\nMvaa 184,220,8\nMvaa 184,216,12\ns_nop 3\ns_memrealtime s[98:99]\ns_waitcnt vmcnt(24) lgkmcnt(0)\ns_barrier\ns_mov_b32 s93, s98\n"
"Cvaa 188,244,16,164\ns_add_u32 s75, s97, 0xa000\nEX 180\nMvaa 188,240,20\ns_and_b32 s75, s75, 0x1ffff\nEX 181\nMvaa 188,236,24\n"
"s_add_u32 s75, s75, 0x40000\nEX 182\nCV 196,180\nMvaa 188,232,28\ns_add_u32 m0, s72, 0x20000\nEX 183\nCvaa 192,228,16,164\ns_waitcnt vmcnt(7)\n"
"EX 184\nCV 197,182\nMvaa 192,224,20\nEX 185\nMvaa 192,220,24\nEX 186\nCV 198,184\nMvaa 192,216,28\nEX 187\ns_nop 0\nCV 199,186\nCvaa 180,244,32,168\n"
"EX 188\nMvaa 180,240,36\nDtL 210,44,75,0\nMvaa 180,236,40\nEX 189\nMvaa 180,232,44\nRa 184,204,0\nCV 200,188\nCvaa 184,228,32,168\nEX 190\n"
"Mvaa 184,224,36\nRa 188,205,0\nv_add_u32 v208, 0xfffe0000, v208\nMvaa 184,220,40\nEX 191\nMvaa 184,216,44\nRa 192,206,0\nCV 201,190\nZvav 0,248,196\n"
"EX 192\nZvav 4,252,196\nDtL 211,44,75,1024\nZvvv 8,216,196\nEX 193\nZvvv 12,220,196\nRa 196,207,0\nCV 202,192\nZvvv 16,224,196\nEX 194\n"
"Zvvv 20,228,196\nRa 200,204,4096\nv_add_u32 v209, 0xfffe0000, v209\nZvvv 24,232,196\nEX 195\nZvvv 28,236,196\nRa 204,205,4096\nCV 203,194\n"
"Cvaa 188,244,48,172\ns_waitcnt vmcnt(7)\nRa 208,206,4096\nCV 128,128\nMvaa 188,240,52\nEX 180\nMvaa 188,236,56\nDtL 212,44,75,2048\nMvaa 188,232,60\n"
"EX 181\nv_smfmac_f32_16x16x64_bf16 a[160:163], a[180:183], v[196:203], v251\nRa 212,207,4096\nCV 196,180\nCvaa 192,228,48,172\nEX 182\n"
"Mvaa 192,224,52\nCV 129,130\nCV 130,132\nMvaa 192,220,56\nEX 183\nMvaa 192,216,60\nCV 197,182\nCV 131,134\nZvav 32,248,200\nEX 184\nZvav 36,252,200\n"
"DtL 213,44,75,3072\nZvvv 40,216,200\nEX 185\nZvvv 44,220,200\nCV 198,184\nAW 144,128\nZvvv 48,224,200\nEX 186\nZvvv 52,228,200\nAW 145,129\n"
"AW 146,130\nZvvv 56,232,200\nEX 187\nZvvv 60,236,200\ns_add_u32 m0, s72, 0x22000\nCV 199,186\nAW 147,131\nCvaa 180,244,64,176\ns_waitcnt vmcnt(7)\n"
"EX 188\nMvaa 180,240,68\nDL 214,48,75,0\nMvaa 180,236,72\nEX 189\nMvaa 180,232,76\nTa 244,208,8192\nCV 200,188\nCvaa 184,228,64,176\nEX 190\n"
"Mvaa 184,224,68\nTa 246,208,12288\nCV 136,136\nMvaa 184,220,72\nEX 191\nMvaa 184,216,76\nTa 240,209,8192\nCV 201,190\nZvav 64,248,196\nEX 192\n"
"Zvav 68,252,196\nDL 215,48,75,1024\nZvvv 72,216,196\nEX 193\nZvvv 76,220,196\nTa 242,209,12288\nCV 202,192\nZvvv 80,224,196\nEX 194\nZvvv 84,228,196\n"
"Ta 236,208,8448\nCV 137,138\nZvvv 88,232,196\nEX 195\nZvvv 92,236,196\ns_waitcnt lgkmcnt(2)\nTa 238,208,12544\nCV 203,194\nCvaa 188,184,0,160\n"
"s_waitcnt vmcnt(7)\nTa 232,209,8448\nCV 138,140\nMvaa 188,188,4\nEX 180\nMvaa 188,192,8\nDL 214,48,75,2048\nMvaa 188,196,12\nEX 181\n"
"v_smfmac_f32_16x16x64_bf16 a[164:167], a[180:183], v[196:203], v251\nTa 234,209,12544\nCV 196,180\nCvaa 192,200,0,160\nEX 182\nMvaa 192,204,4\n"
"Ta 228,208,8704\nCV 139,142\nMvaa 192,208,8\nEX 183\nMvaa 192,212,12\nTa 230,208,12800\nCV 197,182\nZvav 96,248,200\nEX 184\nZvav 100,252,200\n"
"DL 215,48,75,3072\nZvvv 104,216,200\nEX 185\nZvvv 108,220,200\nTa 224,209,8704\nCV 198,184\nZvvv 112,224,200\nEX 186\nZvvv 116,228,200\n"
"Ta 226,209,12800\nAW 148,136\nZvvv 120,232,200\nEX 187\nZvvv 124,236,200\nTa 220,208,8960\nCV 199,186\nCvaa 180,184,16,164\nEX 188\nMvaa 180,188,20\n"
"Ta 222,208,13056\nAW 149,137\nMvaa 180,192,24\nEX 189\nMvaa 180,196,28\ns_memrealtime s[98:99]\nTa 216,209,8960\nCV 200,188\nCvaa 184,200,16,164\n"
"EX 190\nMvaa 184,204,20\nTa 218,209,13056\nAW 150,138\nMvaa 184,208,24\nEX 191\nMvaa 184,212,28\nCV 201,190\nAW 151,139\nZvav 128,248,196\n"
"s_waitcnt vmcnt(24) lgkmcnt(0)\nEX 192\nZvav 132,252,196\ns_barrier\n"
"s_sub_u32 s95, s98, s93\ns_mov_b32 s93, s98\ns_sub_u32 s95, 102, s95\ns_cbranch_scc1 .Lfa_t32b\ns_add_u32 s93, s93, s95\n.Lfa_t32a:\ns_sub_u32 s95, s95, 4\ns_cbranch_scc1 .Lfa_t32b\ns_sleep 1\ns_branch .Lfa_t32a\n.Lfa_t32b:\n"
"Zvvv 136,216,196\ns_add_u32 s75, s97, 0xc000\nEX 193\nZvvv 140,220,196\ns_and_b32 s75, s75, 0x1ffff\nRa 248,204,32768\nCV 202,192\nCV 144,144\n"
"CV 145,146\nCV 146,148\nCV 147,150\nAW 152,144\nAW 153,145\nAW 154,146\nAW 155,147\nZvvv 144,224,196\ns_add_u32 s75, s75, 0x40000\nEX 194\n"
"Zvvv 148,228,196\ns_add_u32 m0, s72, 0x0\nRa 252,205,32768\nCV 152,152\nCV 153,154\nCV 154,156\nCV 155,158\nAW 156,152\nAW 157,153\nAW 158,154\n"
"AW 159,155\nZvvv 152,232,196\ns_waitcnt vmcnt(7)\nEX 195\nZvvv 156,236,196\nRv 216,206,32768\nCV 203,194\nCvaa 188,184,32,168\nDtL 210,44,75,0\n"
"Mvaa 188,188,36\nEX 180\nMvaa 188,192,40\nRv 220,207,32768\nMvaa 188,196,44\nEX 181\n"
"v_smfmac_f32_16x16x64_bf16 a[168:171], a[180:183], v[196:203], v251\nRv 224,204,36864\nCV 196,180\nCvaa 192,200,32,168\nEX 182\nMvaa 192,204,36\n"
"Rv 228,205,36864\nv_add_u32 v204, 0x10000, v204\nMvaa 192,208,40\nEX 183\nMvaa 192,212,44\nRv 232,206,36864\nCV 197,182\nMvav 0,244,200\nEX 184\n"
"Mvav 4,240,200\nDtL 211,44,75,1024\nMvav 8,236,200\nEX 185\nMvav 12,232,200\nRv 236,207,36864\nCV 198,184\nMvav 16,228,200\nEX 186\nMvav 20,224,200\n"
"v_add_u32 v205, 0x10000, v205\nv_add_u32 v206, 0x10000, v206\nMvav 24,220,200\nEX 187\nMvav 28,216,200\nCV 199,186\nv_add_u32 v207, 0x10000, v207\n"
"Cvaa 180,184,48,172\ns_waitcnt vmcnt(7)\nEX 188\nMvaa 180,188,52\nDtL 212,44,75,2048\nMvaa 180,192,56\nEX 189\nMvaa 180,196,60\nCV 200,188\n"
"Cvaa 184,200,48,172\nEX 190\nMvaa 184,204,52\nMvaa 184,208,56\nEX 191\nMvaa 184,212,60\nCV 201,190\nMvav 32,244,196\nEX 192\nMvav 36,240,196\n"
"DtL 213,44,75,3072\nMvav 40,236,196\nEX 193\nMvav 44,232,196\nCV 202,192\nMvav 48,228,196\nEX 194\nMvav 52,224,196\nMvav 56,220,196\nEX 195\n"
"Mvav 60,216,196\nCV 203,194\nCvaa 188,184,64,176\nTa 184,208,40960\nMvaa 188,188,68\nEX 180\nMvaa 188,192,72\nTa 186,208,45056\nMvaa 188,196,76\n"
"EX 181\nv_smfmac_f32_16x16x64_bf16 a[172:175], a[180:183], v[196:203], v251\nTa 188,209,40960\nCV 196,180\nCvaa 192,200,64,176\nEX 182\n"
"Mvaa 192,204,68\nTa 190,209,45056\nMvaa 192,208,72\nEX 183\nMvaa 192,212,76\nTa 192,208,41216\nCV 197,182\nMvav 64,244,200\nEX 184\nMvav 68,240,200\n"
"Ta 194,208,45312\nMvav 72,236,200\nEX 185\nMvav 76,232,200\nTa 196,209,41216\nCV 198,184\nMvav 80,228,200\nEX 186\nMvav 84,224,200\nTa 198,209,45312\n"
"Mvav 88,220,200\nEX 187\nMvav 92,216,200\ns_waitcnt lgkmcnt(3)\nTa 200,208,41472\nCV 199,186\nCvaa 180,248,0,160\nEX 188\nMvaa 180,252,4\n"
"Ta 202,208,45568\nMvva 180,216,8\nEX 189\nMvva 180,220,12\n.Lfa_pd7350:\ns_cbranch_execz .Lfa_wr3\nTa 204,209,41472\nCV 200,188\nCvva 184,224,0,160\n"
"EX 190\nMvva 184,228,4\nTa 206,209,45568\nMvva 184,232,8\nEX 191\nMvva 184,236,12\nTa 208,208,41728\nCV 201,190\nMvav 96,244,196\nEX 192\n"
"Mvav 100,240,196\nTa 210,208,45824\nMvav 104,236,196\nEX 193\nMvav 108,232,196\nTa 212,209,41728\nCV 202,192\nMvav 112,228,196\nEX 194\n"
"Mvav 116,224,196\nTa 214,209,45824\nv_add_u32 v208, 0x10000, v208\nMvav 120,220,196\nEX 195\nMvav 124,216,196\nCV 203,194\n"
"v_add_u32 v209, 0x10000, v209\nCvaa 188,248,16,164\nMvaa 188,252,20\nEX 180\nMvva 188,216,24\nMvva 188,220,28\nEX 181\n"
"v_smfmac_f32_16x16x64_bf16 a[176:179], a[180:183], v[196:203], v251\ns_memrealtime s[98:99]\nCV 196,180\nCvva 192,224,16,164\nEX 182\n"
"Mvva 192,228,20\nMvva 192,232,24\nEX 183\nMvva 192,236,28\nCV 197,182\nMvav 128,244,200\ns_waitcnt vmcnt(20) lgkmcnt(0)\nEX 184\nMvav 132,240,200\n"
"s_barrier\n"
"s_mov_b64 exec, 0\ns_cmp_eq_u32 s60, 0\ns_cbranch_scc1 .Lfa_w4_0\ns_cmp_eq_u32 s60, 1\ns_cbranch_scc1 .Lfa_w4_1\ns_cmp_eq_u32 s60, 2\ns_cbranch_scc1 .Lfa_w4_2\ns_branch .Lfa_w4_3\n.Lfa_wr4:\ns_mov_b64 exec, -1\ns_nop 4\n"
"s_sub_u32 s95, s98, s93\ns_mov_b32 s93, s98\ns_sub_u32 s95, 102, s95\ns_cbranch_scc1 .Lfa_t33b\ns_add_u32 s93, s93, s95\n.Lfa_t33a:\ns_sub_u32 s95, s95, 4\ns_cbranch_scc1 .Lfa_t33b\ns_sleep 1\ns_branch .Lfa_t33a\n.Lfa_t33b:\n"
"Mvav 136,236,200\ns_add_u32 m0, s72, 0x2000\nEX 185\nMvav 140,232,200\ns_waitcnt vmcnt(7)\nRa 244,204,0\nCV 198,184\nMvav 144,228,200\nEX 186\n"
"Mvav 148,224,200\nDL 214,48,75,0\nMvav 152,220,200\nEX 187\nMvav 156,216,200\nRa 240,205,0\nCV 199,186\nCvaa 180,248,32,168\nEX 188\nMvaa 180,252,36\n"
"DL 215,48,75,1024\nMvva 180,216,40\nEX 189\nMvva 180,220,44\nRa 236,206,0\nCV 200,188\nCvva 184,224,32,168\ns_waitcnt vmcnt(7)\nEX 190\n"
"Mvva 184,228,36\nDL 214,48,75,2048\nMvva 184,232,40\nEX 191\nMvva 184,236,44\nRa 232,207,0\nCV 201,190\nMvav 0,184,196\nEX 192\nMvav 4,188,196\n"
"DL 215,48,75,3072\nMvav 8,192,196\ns_add_u32 s75, s97, 0xe000\nEX 193\nMvav 12,196,196\ns_and_b32 s75, s75, 0x1ffff\nRa 228,204,4096\nCV 202,192\n"
"Mvav 16,200,196\ns_add_u32 s75, s75, 0x40000\nEX 194\nMvav 20,204,196\ns_add_u32 m0, s72, 0x8000\nRa 224,205,4096\nMvav 24,208,196\n"
"s_waitcnt vmcnt(7)\nEX 195\nMvav 28,212,196\nRa 220,206,4096\nCV 203,194\nCvaa 188,248,48,172\nDtL 210,44,75,0\nMvaa 188,252,52\nEX 180\n"
"Mvva 188,216,56\nRa 216,207,4096\nMvva 188,220,60\nEX 181\nv_smfmac_f32_16x16x64_bf16 a[160:163], a[180:183], v[196:203], v251\nCV 196,180\n"
"Cvva 192,224,48,172\nEX 182\nMvva 192,228,52\nDtL 211,44,75,1024\nMvva 192,232,56\nEX 183\nMvva 192,236,60\nCV 197,182\nMvav 32,184,200\n"
"s_waitcnt vmcnt(7)\nEX 184\nMvav 36,188,200\nDtL 212,44,75,2048\nMvav 40,192,200\nEX 185\nMvav 44,196,200\nCV 198,184\nMvav 48,200,200\nEX 186\n"
"Mvav 52,204,200\nDtL 213,44,75,3072\nMvav 56,208,200\nEX 187\nMvav 60,212,200\nCV 199,186\nCvaa 180,248,64,176\nEX 188\nMvaa 180,252,68\n"
"buffer_load_dword v252, v210, s[68:71], s96 offen offset:0\nMvva 180,216,72\nEX 189\nMvva 180,220,76\nTa 248,208,8192\nCV 200,188\n"
"Cvva 184,224,64,176\nEX 190\nMvva 184,228,68\nbuffer_load_dword v252, v210, s[68:71], s96 offen offset:1024\nMvva 184,232,72\nEX 191\n"
"Mvva 184,236,76\nTa 250,208,12288\nCV 201,190\nMvav 64,184,196\nEX 192\nMvav 68,188,196\nTa 252,209,8192\nMvav 72,192,196\nEX 193\nMvav 76,196,196\n"
"Ta 254,209,12288\nCV 202,192\nMvav 80,200,196\nEX 194\nMvav 84,204,196\nTv 216,208,8448\nMvav 88,208,196\nEX 195\nMvav 92,212,196\n"
"s_waitcnt lgkmcnt(3)\nTv 218,208,12544\nCV 203,194\nCvaa 188,244,0,160\nTv 220,209,8448\nMvaa 188,240,4\nEX 180\nMvaa 188,236,8\nTv 222,209,12544\n"
"Mvaa 188,232,12\nEX 181\nv_smfmac_f32_16x16x64_bf16 a[164:167], a[180:183], v[196:203], v251\nTv 224,208,8704\nCV 196,180\nCvaa 192,228,0,160\n"
"EX 182\nMvaa 192,224,4\nTv 226,208,12800\nMvaa 192,220,8\nEX 183\nMvaa 192,216,12\nTv 228,209,8704\nCV 197,182\nMvav 96,184,200\nEX 184\n"
"Mvav 100,188,200\nTv 230,209,12800\nMvav 104,192,200\nEX 185\nMvav 108,196,200\nTv 232,208,8960\nCV 198,184\nMvav 112,200,200\nEX 186\n"
"Mvav 116,204,200\nTv 234,208,13056\nMvav 120,208,200\nEX 187\nMvav 124,212,200\nTv 236,209,8960\nCV 199,186\nCvaa 180,244,16,164\nEX 188\n"
"Mvaa 180,240,20\nTv 238,209,13056\nMvaa 180,236,24\nEX 189\nMvaa 180,232,28\ns_memrealtime s[98:99]\nCV 200,188\nCvaa 184,228,16,164\nEX 190\n"
"Mvaa 184,224,20\nMvaa 184,220,24\nEX 191\nMvaa 184,216,28\nCV 201,190\nMvav 128,184,196\ns_waitcnt vmcnt(22) lgkmcnt(0)\nEX 192\nMvav 132,188,196\n"
"s_barrier\n"
"s_sub_u32 s95, s98, s93\ns_mov_b32 s93, s98\ns_sub_u32 s95, 102, s95\ns_cbranch_scc1 .Lfa_t34b\ns_add_u32 s93, s93, s95\n.Lfa_t34a:\ns_sub_u32 s95, s95, 4\ns_cbranch_scc1 .Lfa_t34b\ns_sleep 1\ns_branch .Lfa_t34a\n.Lfa_t34b:\n"
"Mvav 136,192,196\ns_add_u32 m0, s72, 0xa000\nEX 193\nMvav 140,196,196\ns_waitcnt vmcnt(7)\nRa 184,204,32768\nCV 202,192\nMvav 144,200,196\nEX 194\n"
"Mvav 148,204,196\nDL 214,48,75,0\nMvav 152,208,196\nEX 195\nMvav 156,212,196\nRa 188,205,32768\nCV 203,194\nCvaa 188,244,32,168\nRa 192,206,32768\n"
"Mvaa 188,240,36\nEX 180\nMvaa 188,236,40\nRa 196,207,32768\nMvaa 188,232,44\nEX 181\n"
"v_smfmac_f32_16x16x64_bf16 a[168:171], a[180:183], v[196:203], v251\nRa 200,204,36864\nCV 196,180\nCvaa 192,228,32,168\nEX 182\nMvaa 192,224,36\n"
"DL 215,48,75,1024\nMvaa 192,220,40\nEX 183\nMvaa 192,216,44\nRa 204,205,36864\nCV 197,182\nMvav 0,248,200\nEX 184\nMvav 4,252,200\nRa 208,206,36864\n"
"v_add_u32 v204, 0x10000, v204\nMvvv 8,216,200\nEX 185\nMvvv 12,220,200\nRa 212,207,36864\nCV 198,184\nMvvv 16,224,200\ns_waitcnt vmcnt(7)\nEX 186\n"
"Mvvv 20,228,200\nDL 214,48,75,2048\nMvvv 24,232,200\nEX 187\nMvvv 28,236,200\nCV 199,186\nv_add_u32 v205, 0x10000, v205\nCvaa 180,244,48,172\nEX 188\n"
"Mvaa 180,240,52\nv_add_u32 v206, 0x10000, v206\nv_add_u32 v207, 0x10000, v207\nMvaa 180,236,56\nEX 189\nMvaa 180,232,60\nCV 200,188\n"
"Cvaa 184,228,48,172\nEX 190\nMvaa 184,224,52\nDL 215,48,75,3072\nMvaa 184,220,56\nEX 191\nMvaa 184,216,60\nCV 201,190\nMvav 32,248,196\n"
"s_add_u32 s75, s97, 0x10000\nEX 192\nMvav 36,252,196\ns_and_b32 s75, s75, 0x1ffff\nMvvv 40,216,196\ns_add_u32 s75, s75, 0x40000\nEX 193\n"
"Mvvv 44,220,196\ns_add_u32 m0, s72, 0x10000\nCV 202,192\nMvvv 48,224,196\ns_waitcnt vmcnt(7)\nEX 194\nMvvv 52,228,196\nDtL 210,44,75,0\n"
"Mvvv 56,232,196\nEX 195\nMvvv 60,236,196\nCV 203,194\nCvaa 188,244,64,176\nTa 244,208,40960\nMvaa 188,240,68\nEX 180\nMvaa 188,236,72\n"
"Ta 246,208,45056\nMvaa 188,232,76\nEX 181\nv_smfmac_f32_16x16x64_bf16 a[172:175], a[180:183], v[196:203], v251\nTa 240,209,40960\nCV 196,180\n"
"Cvaa 192,228,64,176\nEX 182\nMvaa 192,224,68\nDtL 211,44,75,1024\nMvaa 192,220,72\nEX 183\nMvaa 192,216,76\nTa 242,209,45056\nCV 197,182\n"
"Mvav 64,248,200\nEX 184\nMvav 68,252,200\nTa 236,208,41216\nMvvv 72,216,200\nEX 185\nMvvv 76,220,200\nTa 238,208,45312\nCV 198,184\nMvvv 80,224,200\n"
"s_waitcnt vmcnt(7)\nEX 186\nMvvv 84,228,200\nDtL 212,44,75,2048\nMvvv 88,232,200\nEX 187\nMvvv 92,236,200\ns_waitcnt lgkmcnt(2)\nTa 232,209,41216\n"
"CV 199,186\nCvaa 180,184,0,160\nEX 188\nMvaa 180,188,4\nTa 234,209,45312\nMvaa 180,192,8\nEX 189\nMvaa 180,196,12\nTa 228,208,41472\nCV 200,188\n"
"Cvaa 184,200,0,160\nEX 190\nMvaa 184,204,4\nDtL 213,44,75,3072\nMvaa 184,208,8\nEX 191\nMvaa 184,212,12\nTa 230,208,45568\nCV 201,190\n"
"Mvav 96,248,196\nEX 192\nMvav 100,252,196\nTa 224,209,41472\nMvvv 104,216,196\nEX 193\nMvvv 108,220,196\nTa 226,209,45568\nCV 202,192\n"
"Mvvv 112,224,196\nEX 194\nMvvv 116,228,196\nTa 220,208,41728\nMvvv 120,232,196\nEX 195\nMvvv 124,236,196\nTa 222,208,45824\nCV 203,194\n"
"Cvaa 188,184,16,164\nTa 216,209,41728\nMvaa 188,188,20\nEX 180\nMvaa 188,192,24\nTa 218,209,45824\nv_add_u32 v208, 0x10000, v208\nMvaa 188,196,28\n"
"EX 181\nv_smfmac_f32_16x16x64_bf16 a[176:179], a[180:183], v[196:203], v251\ns_memrealtime s[98:99]\nCV 196,180\nv_add_u32 v209, 0x10000, v209\n"
"Cvaa 192,200,16,164\nEX 182\nMvaa 192,204,20\nMvaa 192,208,24\nEX 183\nMvaa 192,212,28\nCV 197,182\nMvav 128,248,200\ns_waitcnt vmcnt(22) lgkmcnt(0)\n"
"EX 184\nMvav 132,252,200\ns_barrier\n"
"s_sub_u32 s95, s98, s93\ns_mov_b32 s93, s98\ns_sub_u32 s95, 102, s95\ns_cbranch_scc1 .Lfa_t35b\ns_add_u32 s93, s93, s95\n.Lfa_t35a:\ns_sub_u32 s95, s95, 4\ns_cbranch_scc1 .Lfa_t35b\ns_sleep 1\ns_branch .Lfa_t35a\n.Lfa_t35b:\n"
"Mvvv 136,216,200\ns_add_u32 m0, s72, 0x12000\nEX 185\nMvvv 140,220,200\ns_waitcnt vmcnt(7)\nRa 248,204,0\nCV 198,184\nMvvv 144,224,200\nEX 186\n"
"Mvvv 148,228,200\nDL 214,48,75,0\nMvvv 152,232,200\nEX 187\nMvvv 156,236,200\nRa 252,205,0\nCV 199,186\nCvaa 180,184,32,168\nEX 188\nMvaa 180,188,36\n"
"Rv 216,206,0\nv_mbcnt_lo_u32_b32 v253, -1, 0\nMvaa 180,192,40\nEX 189\nMvaa 180,196,44\nRv 220,207,0\nCV 200,188\nCvaa 184,200,32,168\nEX 190\n"
"Mvaa 184,204,36\nDL 215,48,75,1024\nMvaa 184,208,40\nEX 191\nMvaa 184,212,44\nRv 224,204,4096\nCV 201,190\nMvav 0,244,196\nEX 192\nMvav 4,240,196\n"
"Rv 228,205,4096\nv_add_u32 v204, 0xfffe0000, v204\nMvav 8,236,196\nEX 193\nMvav 12,232,196\nRv 232,206,4096\nCV 202,192\nMvav 16,228,196\n"
"s_waitcnt vmcnt(7)\nEX 194\nMvav 20,224,196\nDL 214,48,75,2048\nMvav 24,220,196\nEX 195\nMvav 28,216,196\nRv 236,207,4096\nCV 203,194\n"
"Cvaa 188,184,48,172\nv_add_u32 v205, 0xfffe0000, v205\nv_add_u32 v206, 0xfffe0000, v206\nMvaa 188,188,52\nEX 180\nMvaa 188,192,56\n"
"v_add_u32 v207, 0xfffe0000, v207\nv_mbcnt_hi_u32_b32 v253, -1, v253\nMvaa 188,196,60\nEX 181\n"
"v_smfmac_f32_16x16x64_bf16 a[160:163], a[180:183], v[196:203], v251\nCV 196,180\nv_lshrrev_b32 v249, 5, v253\nCvaa 192,200,48,172\nEX 182\n"
"Mvaa 192,204,52\nDL 215,48,75,3072\nMvaa 192,208,56\nEX 183\nMvaa 192,212,60\nCV 197,182\nv_bfe_u32 v250, v253, 4, 1\nMvav 32,244,200\nEX 184\n"
"Mvav 36,240,200\nv_bfe_u32 v248, v253, 2, 2\nv_and_b32 v243, 3, v253\nMvav 40,236,200\nEX 185\nMvav 44,232,200\nCV 198,184\n"
"v_lshlrev_b32 v240, 1, v249\nMvav 48,228,200\nEX 186\nMvav 52,224,200\nv_xor_b32 v240, v240, v243\nv_lshlrev_b32 v240, 4, v240\nMvav 56,220,200\n"
"EX 187\nMvav 60,216,200\nCV 199,186\nv_lshl_add_u32 v240, v250, 6, v240\nCvaa 180,184,64,176\nEX 188\nMvaa 180,188,68\nTa 184,208,8192\n"
"v_lshl_add_u32 v240, v248, 10, v240\nMvaa 180,192,72\nEX 189\nMvaa 180,196,76\nTa 186,208,12288\nCV 200,188\nCvaa 184,200,64,176\nEX 190\n"
"Mvaa 184,204,68\nTa 188,209,8192\nv_lshl_add_u32 v240, v249, 12, v240\nMvaa 184,208,72\ns_mul_i32 s88, s83, 8448\nEX 191\nMvaa 184,212,76\n"
"Ta 190,209,12288\nCV 201,190\nMvav 64,244,196\nEX 192\nMvav 68,240,196\nTa 192,208,8448\nv_mul_u32_u24 v244, 544, v250\nMvav 72,236,196\nEX 193\n"
"Mvav 76,232,196\nTa 194,208,12544\nCV 202,192\nMvav 80,228,196\n.Lfa_pd7918:\ns_cbranch_execz .Lfa_wr3\nEX 194\nMvav 84,224,196\nTa 196,209,8448\n"
"v_lshl_add_u32 v244, v249, 10, v244\nMvav 88,220,196\nEX 195\nMvav 92,216,196\ns_waitcnt lgkmcnt(3)\nTa 198,209,12544\nCV 203,194\n"
"Cvaa 188,248,0,160\nTa 200,208,8704\nv_lshl_add_u32 v244, v248, 6, v244\nMvaa 188,252,4\nEX 180\nMvva 188,216,8\nTa 202,208,12800\n"
"v_lshl_add_u32 v244, v243, 3, v244\nMvva 188,220,12\nEX 181\nv_smfmac_f32_16x16x64_bf16 a[164:167], a[180:183], v[196:203], v251\nTa 204,209,8704\n"
"CV 196,180\nCvva 192,224,0,160\nEX 182\nMvva 192,228,4\nTa 206,209,12800\nv_add_u32 v244, s88, v244\nMvva 192,232,8\nEX 183\nMvva 192,236,12\n"
"Ta 208,208,8960\nCV 197,182\nMvav 96,244,200\nEX 184\nMvav 100,240,200\nTa 210,208,13056\nv_xor_b32 v245, 32, v244\nMvav 104,236,200\nEX 185\n"
"Mvav 108,232,200\nTa 212,209,8960\nCV 198,184\nMvav 112,228,200\nEX 186\nMvav 116,224,200\nTa 214,209,13056\nv_add_u32 v208, 0xfffe0000, v208\n"
"Mvav 120,220,200\nEX 187\nMvav 124,216,200\nCV 199,186\nv_add_u32 v209, 0xfffe0000, v209\nCvaa 180,248,16,164\nEX 188\nMvaa 180,252,20\n"
"v_xor_b32 v246, 0x100, v244\nv_xor_b32 v247, 0x120, v244\nMvva 180,216,24\nEX 189\nMvva 180,220,28\ns_memrealtime s[98:99]\nCV 200,188\n"
"v_add_u32 v241, 0x1c00, v240\nCvva 184,224,16,164\nEX 190\nMvva 184,228,20\nv_add_u32 v242, 0x3800, v240\nv_add_u32 v243, 0x5400, v240\n"
"Mvva 184,232,24\ns_and_b32 s88, s62, 7\nEX 191\nMvva 184,236,28\ns_lshl_b32 s88, s88, 12\nCV 201,190\nv_mul_u32_u24 v249, 61440, v249\n"
"Mvav 128,244,196\ns_waitcnt vmcnt(18) lgkmcnt(0)\nEX 192\nMvav 132,240,196\ns_barrier\n"
"s_sub_u32 s95, s98, s93\ns_mov_b32 s93, s98\ns_sub_u32 s95, 102, s95\ns_cbranch_scc1 .Lfa_t36b\ns_add_u32 s93, s93, s95\n.Lfa_t36a:\ns_sub_u32 s95, s95, 4\ns_cbranch_scc1 .Lfa_t36b\ns_sleep 1\ns_branch .Lfa_t36a\n.Lfa_t36b:\n"
"Mvav 136,236,196\ns_add_u32 s75, s97, 0x12000\nEX 193\nMvav 140,232,196\ns_and_b32 s75, s75, 0x1ffff\nRa 244,204,0\nCV 202,192\nMvav 144,228,196\n"
"s_add_u32 s75, s75, 0x40000\nEX 194\nMvav 148,224,196\ns_add_u32 m0, s72, 0x18000\nRa 240,205,0\nv_lshl_add_u32 v249, v253, 7, v249\n"
"Mvav 152,220,196\ns_waitcnt vmcnt(7)\nEX 195\nMvav 156,216,196\nRa 236,206,0\nCV 203,194\nCvaa 188,248,32,168\nDtL 210,44,75,0\nMvaa 188,252,36\n"
"EX 180\nMvva 188,216,40\nRa 232,207,0\nv_add_u32 v249, s88, v249\nMvva 188,220,44\nEX 181\n"
"v_smfmac_f32_16x16x64_bf16 a[168:171], a[180:183], v[196:203], v251\nRa 228,204,4096\nCV 196,180\nCvva 192,224,32,168\nEX 182\nMvva 192,228,36\n"
"Ra 224,205,4096\nv_add_u32 v250, 0x40000, v249\nMvva 192,232,40\ns_mul_i32 s88, s60, 12288\nEX 183\nMvva 192,236,44\ns_add_u32 s88, s88, 0x10000\n"
"Ra 220,206,4096\nCV 197,182\nMvav 0,184,200\nEX 184\nMvav 4,188,200\nDtL 211,44,75,1024\nMvav 8,192,200\nEX 185\nMvav 12,196,200\nRa 216,207,4096\n"
"CV 198,184\nMvav 16,200,200\nEX 186\nMvav 20,204,200\nv_lshlrev_b32 v248, 4, v253\nv_add_u32 v248, s88, v248\nMvav 24,208,200\nEX 187\n"
"Mvav 28,212,200\nCV 199,186\nCvaa 180,248,48,172\ns_waitcnt vmcnt(7)\nEX 188\nMvaa 180,252,52\nDtL 212,44,75,2048\nMvva 180,216,56\nEX 189\n"
"Mvva 180,220,60\nCV 200,188\nCvva 184,224,48,172\nEX 190\nMvva 184,228,52\nMvva 184,232,56\nEX 191\nMvva 184,236,60\nCV 201,190\nMvav 32,184,196\n"
"EX 192\nMvav 36,188,196\nDtL 213,44,75,3072\nMvav 40,192,196\nEX 193\nMvav 44,196,196\nCV 202,192\nMvav 48,200,196\nEX 194\nMvav 52,204,196\n"
"Mvav 56,208,196\nEX 195\nMvav 60,212,196\ns_add_u32 m0, s72, 0x1a000\nCV 203,194\nCvaa 188,248,64,176\ns_waitcnt vmcnt(7)\nTa 248,208,8192\n"
"Mvaa 188,252,68\nEX 180\nMvva 188,216,72\nDL 214,48,75,0\nMvva 188,220,76\nEX 181\n"
"v_smfmac_f32_16x16x64_bf16 a[172:175], a[180:183], v[196:203], v251\nTa 250,208,12288\nCV 196,180\nCvva 192,224,64,176\nEX 182\nMvva 192,228,68\n"
"Ta 252,209,8192\nMvva 192,232,72\nEX 183\nMvva 192,236,76\nTa 254,209,12288\nCV 197,182\nMvav 64,184,200\nEX 184\nMvav 68,188,200\nDL 215,48,75,1024\n"
"Mvav 72,192,200\nEX 185\nMvav 76,196,200\nTv 216,208,8448\nCV 198,184\nMvav 80,200,200\nEX 186\nMvav 84,204,200\nTv 218,208,12544\nMvav 88,208,200\n"
"EX 187\nMvav 92,212,200\ns_waitcnt lgkmcnt(2)\nTv 220,209,8448\nCV 199,186\nCvaa 180,244,0,160\ns_waitcnt vmcnt(7)\nEX 188\nMvaa 180,240,4\n"
"DL 214,48,75,2048\nMvaa 180,236,8\nEX 189\nMvaa 180,232,12\nTv 222,209,12544\nCV 200,188\nCvaa 184,228,0,160\nEX 190\nMvaa 184,224,4\n"
"Tv 224,208,8704\nMvaa 184,220,8\nEX 191\nMvaa 184,216,12\nTv 226,208,12800\nCV 201,190\nMvav 96,184,196\nEX 192\nMvav 100,188,196\nDL 215,48,75,3072\n"
"Mvav 104,192,196\nEX 193\nMvav 108,196,196\nTv 228,209,8704\nCV 202,192\nMvav 112,200,196\nEX 194\nMvav 116,204,196\nTv 230,209,12800\n"
"Mvav 120,208,196\nEX 195\nMvav 124,212,196\nTv 232,208,8960\nCV 203,194\nCvaa 188,244,16,164\nTv 234,208,13056\nMvaa 188,240,20\nEX 180\n"
"Mvaa 188,236,24\nTv 236,209,8960\nMvaa 188,232,28\nEX 181\nv_smfmac_f32_16x16x64_bf16 a[176:179], a[180:183], v[196:203], v251\n"
"s_memrealtime s[98:99]\nTv 238,209,13056\nCV 196,180\nCvaa 192,228,16,164\nEX 182\nMvaa 192,224,20\nMvaa 192,220,24\nEX 183\nMvaa 192,216,28\n"
"CV 197,182\nMvav 128,184,200\ns_waitcnt vmcnt(16) lgkmcnt(0)\nEX 184\nMvav 132,188,200\ns_barrier\n"
"s_sub_u32 s95, s98, s93\ns_mov_b32 s93, s98\ns_sub_u32 s95, 102, s95\ns_cbranch_scc1 .Lfa_t37b\ns_add_u32 s93, s93, s95\n.Lfa_t37a:\ns_sub_u32 s95, s95, 4\ns_cbranch_scc1 .Lfa_t37b\ns_sleep 1\ns_branch .Lfa_t37a\n.Lfa_t37b:\n"
"Mvav 136,192,200\ns_add_u32 s75, s97, 0x14000\nEX 185\nMvav 140,196,200\ns_and_b32 s75, s75, 0x1ffff\nRa 184,204,32768\nCV 198,184\nMvav 144,200,200\n"
"s_add_u32 s75, s75, 0x40000\nEX 186\nMvav 148,204,200\ns_add_u32 m0, s72, 0x20000\nRa 188,205,32768\nMvav 152,208,200\ns_waitcnt vmcnt(7)\nEX 187\n"
"Mvav 156,212,200\nRa 192,206,32768\nCV 199,186\nCvaa 180,244,32,168\nEX 188\nMvaa 180,240,36\nDtL 210,44,75,0\nMvaa 180,236,40\nEX 189\n"
"Mvaa 180,232,44\nRa 196,207,32768\nCV 200,188\nCvaa 184,228,32,168\nEX 190\nMvaa 184,224,36\nRa 200,204,36864\nMvaa 184,220,40\nEX 191\n"
"Mvaa 184,216,44\nRa 204,205,36864\nCV 201,190\nMvav 0,248,196\nEX 192\nMvav 4,252,196\nDtL 211,44,75,1024\nMvvv 8,216,196\nEX 193\nMvvv 12,220,196\n"
"Ra 208,206,36864\nCV 202,192\nMvvv 16,224,196\nEX 194\nMvvv 20,228,196\nRa 212,207,36864\nv_add_u32 v204, 0x10000, v204\nMvvv 24,232,196\nEX 195\n"
"Mvvv 28,236,196\nCV 203,194\nv_add_u32 v205, 0x10000, v205\nCvaa 188,244,48,172\ns_waitcnt vmcnt(7)\nv_add_u32 v206, 0x10000, v206\n"
"v_add_u32 v207, 0x10000, v207\nMvaa 188,240,52\nEX 180\nMvaa 188,236,56\nDtL 212,44,75,2048\nMvaa 188,232,60\nEX 181\n"
"v_smfmac_f32_16x16x64_bf16 a[160:163], a[180:183], v[196:203], v251\nCV 196,180\nCvaa 192,228,48,172\nEX 182\nMvaa 192,224,52\nMvaa 192,220,56\n"
"EX 183\nMvaa 192,216,60\nCV 197,182\nMvav 32,248,200\nEX 184\nMvav 36,252,200\nDtL 213,44,75,3072\nMvvv 40,216,200\nEX 185\nMvvv 44,220,200\n"
"CV 198,184\nMvvv 48,224,200\nEX 186\nMvvv 52,228,200\nMvvv 56,232,200\nEX 187\nMvvv 60,236,200\ns_add_u32 m0, s72, 0x22000\nCV 199,186\n"
"Cvaa 180,244,64,176\ns_waitcnt vmcnt(7)\nEX 188\nMvaa 180,240,68\nDL 214,48,75,0\nMvaa 180,236,72\nEX 189\nMvaa 180,232,76\nTa 244,208,40960\n"
"CV 200,188\nCvaa 184,228,64,176\nEX 190\nMvaa 184,224,68\nTa 246,208,45056\nMvaa 184,220,72\nEX 191\nMvaa 184,216,76\nTa 240,209,40960\nCV 201,190\n"
"Mvav 64,248,196\nEX 192\nMvav 68,252,196\nDL 215,48,75,1024\nMvvv 72,216,196\nEX 193\nMvvv 76,220,196\nTa 242,209,45056\nCV 202,192\nMvvv 80,224,196\n"
"EX 194\nMvvv 84,228,196\nTa 236,208,41216\nMvvv 88,232,196\nEX 195\nMvvv 92,236,196\ns_waitcnt lgkmcnt(2)\nTa 238,208,45312\nCV 203,194\n"
"Cvaa 188,184,0,160\ns_waitcnt vmcnt(7)\nTa 232,209,41216\nMvaa 188,188,4\nEX 180\nMvaa 188,192,8\nDL 214,48,75,2048\nMvaa 188,196,12\nEX 181\n"
"v_smfmac_f32_16x16x64_bf16 a[164:167], a[180:183], v[196:203], v251\nTa 234,209,45312\nCV 196,180\nCvaa 192,200,0,160\nEX 182\nMvaa 192,204,4\n"
"Ta 228,208,41472\nMvaa 192,208,8\nEX 183\nMvaa 192,212,12\nTa 230,208,45568\nCV 197,182\nMvav 96,248,200\nEX 184\nMvav 100,252,200\n"
"DL 215,48,75,3072\nMvvv 104,216,200\nEX 185\nMvvv 108,220,200\nTa 224,209,41472\nCV 198,184\nMvvv 112,224,200\nEX 186\nMvvv 116,228,200\n"
"Ta 226,209,45568\nMvvv 120,232,200\nEX 187\nMvvv 124,236,200\nTa 220,208,41728\nCV 199,186\nCvaa 180,184,16,164\nEX 188\nMvaa 180,188,20\n"
"Ta 222,208,45824\nMvaa 180,192,24\nEX 189\nMvaa 180,196,28\ns_memrealtime s[98:99]\nTa 216,209,41728\nCV 200,188\nCvaa 184,200,16,164\nEX 190\n"
"Mvaa 184,204,20\nTa 218,209,45824\nv_add_u32 v208, 0x10000, v208\nMvaa 184,208,24\nEX 191\nMvaa 184,212,28\nCV 201,190\n"
"v_add_u32 v209, 0x10000, v209\nMvav 128,248,196\ns_waitcnt vmcnt(16) lgkmcnt(0)\nEX 192\nMvav 132,252,196\ns_barrier\n"
"s_sub_u32 s95, s98, s93\ns_mov_b32 s93, s98\ns_sub_u32 s95, 102, s95\ns_cbranch_scc1 .Lfa_t38b\ns_add_u32 s93, s93, s95\n.Lfa_t38a:\ns_sub_u32 s95, s95, 4\ns_cbranch_scc1 .Lfa_t38b\ns_sleep 1\ns_branch .Lfa_t38a\n.Lfa_t38b:\n"
"Mvvv 136,216,196\ns_add_u32 s75, s97, 0x16000\nEX 193\nMvvv 140,220,196\ns_and_b32 s75, s75, 0x1ffff\nRa 248,204,0\nCV 202,192\nMvvv 144,224,196\n"
"s_add_u32 s75, s75, 0x40000\nEX 194\nMvvv 148,228,196\ns_add_u32 m0, s72, 0x0\nRa 252,205,0\nMvvv 152,232,196\ns_waitcnt vmcnt(7)\nEX 195\n"
"Mvvv 156,236,196\nRv 216,206,0\nCV 203,194\nCvaa 188,184,32,168\nDtL 210,44,75,0\nMvaa 188,188,36\nEX 180\nMvaa 188,192,40\nRv 220,207,0\n"
"Mvaa 188,196,44\nEX 181\nv_smfmac_f32_16x16x64_bf16 a[168:171], a[180:183], v[196:203], v251\nRv 224,204,4096\nCV 196,180\nCvaa 192,200,32,168\n"
"EX 182\nMvaa 192,204,36\nRv 228,205,4096\nMvaa 192,208,40\nEX 183\nMvaa 192,212,44\nRv 232,206,4096\nCV 197,182\nMvav 0,244,200\nEX 184\n"
"Mvav 4,240,200\nDtL 211,44,75,1024\nMvav 8,236,200\nEX 185\nMvav 12,232,200\nRv 236,207,4096\nCV 198,184\nMvav 16,228,200\nEX 186\nMvav 20,224,200\n"
"Mvav 24,220,200\nEX 187\nMvav 28,216,200\nCV 199,186\nCvaa 180,184,48,172\ns_waitcnt vmcnt(7)\nEX 188\nMvaa 180,188,52\nDtL 212,44,75,2048\n"
"Mvaa 180,192,56\nEX 189\nMvaa 180,196,60\nCV 200,188\nCvaa 184,200,48,172\nEX 190\nMvaa 184,204,52\nMvaa 184,208,56\nEX 191\nMvaa 184,212,60\n"
"CV 201,190\nMvav 32,244,196\nEX 192\nMvav 36,240,196\nDtL 213,44,75,3072\nMvav 40,236,196\nEX 193\nMvav 44,232,196\nCV 202,192\nMvav 48,228,196\n"
"EX 194\nMvav 52,224,196\nMvav 56,220,196\nEX 195\nMvav 60,216,196\nCV 203,194\nCvaa 188,184,64,176\nTa 184,208,8192\nMvaa 188,188,68\nEX 180\n"
"Mvaa 188,192,72\nTa 186,208,12288\nMvaa 188,196,76\nEX 181\nv_smfmac_f32_16x16x64_bf16 a[172:175], a[180:183], v[196:203], v251\nTa 188,209,8192\n"
"CV 196,180\nCvaa 192,200,64,176\n.Lfa_pd8489:\ns_cbranch_execz .Lfa_wr3\nEX 182\nMvaa 192,204,68\nTa 190,209,12288\nMvaa 192,208,72\nEX 183\n"
"Mvaa 192,212,76\nTa 192,208,8448\nCV 197,182\nMvav 64,244,200\nEX 184\nMvav 68,240,200\nTa 194,208,12544\nMvav 72,236,200\nEX 185\nMvav 76,232,200\n"
"Ta 196,209,8448\nCV 198,184\nMvav 80,228,200\nEX 186\nMvav 84,224,200\nTa 198,209,12544\nMvav 88,220,200\nEX 187\nMvav 92,216,200\n"
"s_waitcnt lgkmcnt(3)\nTa 200,208,8704\nCV 199,186\nCvaa 180,248,0,160\nEX 188\nMvaa 180,252,4\nTa 202,208,12800\nMvva 180,216,8\nEX 189\n"
"Mvva 180,220,12\nTa 204,209,8704\nCV 200,188\nCvva 184,224,0,160\nEX 190\nMvva 184,228,4\nTa 206,209,12800\nMvva 184,232,8\nEX 191\nMvva 184,236,12\n"
"Ta 208,208,8960\nCV 201,190\nMvav 96,244,196\nEX 192\nMvav 100,240,196\nTa 210,208,13056\nMvav 104,236,196\nEX 193\nMvav 108,232,196\n"
"Ta 212,209,8960\nCV 202,192\nMvav 112,228,196\nEX 194\nMvav 116,224,196\nTa 214,209,13056\nMvav 120,220,196\nEX 195\nMvav 124,216,196\nCV 203,194\n"
"Cvaa 188,248,16,164\nMvaa 188,252,20\nEX 180\nMvva 188,216,24\nMvva 188,220,28\nEX 181\n"
"v_smfmac_f32_16x16x64_bf16 a[176:179], a[180:183], v[196:203], v251\ns_memrealtime s[98:99]\nCV 196,180\nCvva 192,224,16,164\nEX 182\n"
"Mvva 192,228,20\nMvva 192,232,24\nEX 183\nMvva 192,236,28\nCV 197,182\nMvav 128,244,200\ns_waitcnt vmcnt(12) lgkmcnt(0)\nEX 184\nMvav 132,240,200\n"
"s_barrier\n"
"s_mov_b64 exec, 0\ns_cmp_eq_u32 s60, 0\ns_cbranch_scc1 .Lfa_w5_0\ns_cmp_eq_u32 s60, 1\ns_cbranch_scc1 .Lfa_w5_1\ns_cmp_eq_u32 s60, 2\ns_cbranch_scc1 .Lfa_w5_2\ns_branch .Lfa_w5_3\n.Lfa_wr5:\ns_mov_b64 exec, -1\ns_nop 4\n"
"s_sub_u32 s95, s98, s93\ns_mov_b32 s93, s98\ns_sub_u32 s95, 102, s95\ns_cbranch_scc1 .Lfa_t39b\ns_add_u32 s93, s93, s95\n.Lfa_t39a:\ns_sub_u32 s95, s95, 4\ns_cbranch_scc1 .Lfa_t39b\ns_sleep 1\ns_branch .Lfa_t39a\n.Lfa_t39b:\n"
"Mvav 136,236,200\ns_add_u32 m0, s72, 0x2000\nEX 185\nMvav 140,232,200\ns_waitcnt vmcnt(7)\nRa 244,204,32768\nCV 198,184\nMvav 144,228,200\nEX 186\n"
"Mvav 148,224,200\nDL 214,48,75,0\nMvav 152,220,200\nEX 187\nMvav 156,216,200\nRa 240,205,32768\nCV 199,186\nCvaa 180,248,32,168\nEX 188\n"
"Mvaa 180,252,36\nRa 236,206,32768\nMvva 180,216,40\nEX 189\nMvva 180,220,44\nRa 232,207,32768\nCV 200,188\nCvva 184,224,32,168\nEX 190\n"
"Mvva 184,228,36\nDL 215,48,75,1024\nMvva 184,232,40\nEX 191\nMvva 184,236,44\nRa 228,204,36864\nCV 201,190\nMvav 0,184,196\nEX 192\nMvav 4,188,196\n"
"Ra 224,205,36864\nv_add_u32 v204, 0x10000, v204\nMvav 8,192,196\nEX 193\nMvav 12,196,196\nRa 220,206,36864\nCV 202,192\nMvav 16,200,196\n"
"s_waitcnt vmcnt(7)\nEX 194\nMvav 20,204,196\nDL 214,48,75,2048\nMvav 24,208,196\nEX 195\nMvav 28,212,196\nRa 216,207,36864\nCV 203,194\n"
"Cvaa 188,248,48,172\nv_add_u32 v205, 0x10000, v205\nv_add_u32 v206, 0x10000, v206\nMvaa 188,252,52\nEX 180\nMvva 188,216,56\n"
"v_add_u32 v207, 0x10000, v207\nMvva 188,220,60\nEX 181\nv_smfmac_f32_16x16x64_bf16 a[160:163], a[180:183], v[196:203], v251\nCV 196,180\n"
"Cvva 192,224,48,172\nEX 182\nMvva 192,228,52\nDL 215,48,75,3072\nMvva 192,232,56\nEX 183\nMvva 192,236,60\nCV 197,182\nMvav 32,184,200\n"
"s_add_u32 s75, s97, 0x18000\nEX 184\nMvav 36,188,200\ns_and_b32 s75, s75, 0x1ffff\nMvav 40,192,200\ns_add_u32 s75, s75, 0x40000\nEX 185\n"
"Mvav 44,196,200\ns_add_u32 m0, s72, 0x8000\nCV 198,184\nMvav 48,200,200\ns_waitcnt vmcnt(7)\nEX 186\nMvav 52,204,200\nDtL 210,44,75,0\n"
"Mvav 56,208,200\nEX 187\nMvav 60,212,200\nCV 199,186\nCvaa 180,248,64,176\nEX 188\nMvaa 180,252,68\nTa 248,208,40960\nMvva 180,216,72\nEX 189\n"
"Mvva 180,220,76\nTa 250,208,45056\nCV 200,188\nCvva 184,224,64,176\nEX 190\nMvva 184,228,68\nDtL 211,44,75,1024\nMvva 184,232,72\nEX 191\n"
"Mvva 184,236,76\nTa 252,209,40960\nCV 201,190\nMvav 64,184,196\nEX 192\nMvav 68,188,196\nTa 254,209,45056\nMvav 72,192,196\nEX 193\nMvav 76,196,196\n"
"Tv 216,208,41216\nCV 202,192\nMvav 80,200,196\ns_waitcnt vmcnt(7)\nEX 194\nMvav 84,204,196\nDtL 212,44,75,2048\nMvav 88,208,196\nEX 195\n"
"Mvav 92,212,196\ns_waitcnt lgkmcnt(2)\nTv 218,208,45312\nCV 203,194\nCvaa 188,244,0,160\nTv 220,209,41216\nMvaa 188,240,4\nEX 180\nMvaa 188,236,8\n"
"Tv 222,209,45312\nMvaa 188,232,12\nEX 181\nv_smfmac_f32_16x16x64_bf16 a[164:167], a[180:183], v[196:203], v251\nTv 224,208,41472\nCV 196,180\n"
"Cvaa 192,228,0,160\nEX 182\nMvaa 192,224,4\nDtL 213,44,75,3072\nMvaa 192,220,8\nEX 183\nMvaa 192,216,12\nTv 226,208,45568\nCV 197,182\n"
"Mvav 96,184,200\nEX 184\nMvav 100,188,200\nTv 228,209,41472\nMvav 104,192,200\nEX 185\nMvav 108,196,200\nTv 230,209,45568\nCV 198,184\n"
"Mvav 112,200,200\nEX 186\nMvav 116,204,200\nTv 232,208,41728\nMvav 120,208,200\nEX 187\nMvav 124,212,200\nTv 234,208,45824\nCV 199,186\n"
"Cvaa 180,244,16,164\nEX 188\nMvaa 180,240,20\nTv 236,209,41728\nv_add_u32 v208, 0x10000, v208\nMvaa 180,236,24\nEX 189\nMvaa 180,232,28\n"
"s_memrealtime s[98:99]\nTv 238,209,45824\nCV 200,188\nCvaa 184,228,16,164\nEX 190\nMvaa 184,224,20\nv_add_u32 v209, 0x10000, v209\nMvaa 184,220,24\n"
"EX 191\nMvaa 184,216,28\nCV 201,190\nMvav 128,184,196\ns_waitcnt vmcnt(12) lgkmcnt(0)\nEX 192\nMvav 132,188,196\ns_barrier\n"
"s_sub_u32 s95, s98, s93\ns_mov_b32 s93, s98\ns_sub_u32 s95, 102, s95\ns_cbranch_scc1 .Lfa_t40b\ns_add_u32 s93, s93, s95\n.Lfa_t40a:\ns_sub_u32 s95, s95, 4\ns_cbranch_scc1 .Lfa_t40b\ns_sleep 1\ns_branch .Lfa_t40a\n.Lfa_t40b:\n"
"Mvav 136,192,196\ns_add_u32 m0, s72, 0xa000\nEX 193\nMvav 140,196,196\ns_waitcnt vmcnt(7)\nRa 184,204,0\nCV 202,192\nMvav 144,200,196\nEX 194\n"
"Mvav 148,204,196\nDL 214,48,75,0\nMvav 152,208,196\nEX 195\nMvav 156,212,196\nRa 188,205,0\nCV 203,194\nCvaa 188,244,32,168\nRa 192,206,0\n"
"Mvaa 188,240,36\nEX 180\nMvaa 188,236,40\nRa 196,207,0\nMvaa 188,232,44\nEX 181\nv_smfmac_f32_16x16x64_bf16 a[168:171], a[180:183], v[196:203], v251\n"
"Ra 200,204,4096\nCV 196,180\nCvaa 192,228,32,168\nEX 182\nMvaa 192,224,36\nDL 215,48,75,1024\nMvaa 192,220,40\nEX 183\nMvaa 192,216,44\n"
"Ra 204,205,4096\nCV 197,182\nMvav 0,248,200\nEX 184\nMvav 4,252,200\nRa 208,206,4096\nv_add_u32 v204, 0xfffe0000, v204\nMvvv 8,216,200\nEX 185\n"
"Mvvv 12,220,200\nRa 212,207,4096\nCV 198,184\nMvvv 16,224,200\ns_waitcnt vmcnt(7)\nEX 186\nMvvv 20,228,200\nDL 214,48,75,2048\nMvvv 24,232,200\n"
"EX 187\nMvvv 28,236,200\nCV 199,186\nv_add_u32 v205, 0xfffe0000, v205\nCvaa 180,244,48,172\nEX 188\nMvaa 180,240,52\n"
"v_add_u32 v206, 0xfffe0000, v206\nv_add_u32 v207, 0xfffe0000, v207\nMvaa 180,236,56\nEX 189\nMvaa 180,232,60\nCV 200,188\nCvaa 184,228,48,172\n"
"EX 190\nMvaa 184,224,52\nDL 215,48,75,3072\nMvaa 184,220,56\nEX 191\nMvaa 184,216,60\nCV 201,190\nMvav 32,248,196\ns_add_u32 s75, s97, 0x1a000\n"
"EX 192\nMvav 36,252,196\ns_and_b32 s75, s75, 0x1ffff\nMvvv 40,216,196\ns_add_u32 s75, s75, 0x40000\nEX 193\nMvvv 44,220,196\n"
"s_add_u32 m0, s72, 0x10000\nCV 202,192\nMvvv 48,224,196\ns_waitcnt vmcnt(7)\nEX 194\nMvvv 52,228,196\nDtL 210,44,75,0\nMvvv 56,232,196\nEX 195\n"
"Mvvv 60,236,196\nCV 203,194\nCvaa 188,244,64,176\nTa 244,208,8192\nMvaa 188,240,68\nEX 180\nMvaa 188,236,72\nTa 246,208,12288\nMvaa 188,232,76\n"
"EX 181\nv_smfmac_f32_16x16x64_bf16 a[172:175], a[180:183], v[196:203], v251\nTa 240,209,8192\nCV 196,180\nCvaa 192,228,64,176\nEX 182\n"
"Mvaa 192,224,68\nDtL 211,44,75,1024\nMvaa 192,220,72\nEX 183\nMvaa 192,216,76\nTa 242,209,12288\nCV 197,182\nMvav 64,248,200\nEX 184\n"
"Mvav 68,252,200\nTa 236,208,8448\nMvvv 72,216,200\nEX 185\nMvvv 76,220,200\nTa 238,208,12544\nCV 198,184\nMvvv 80,224,200\ns_waitcnt vmcnt(7)\n"
"EX 186\nMvvv 84,228,200\nDtL 212,44,75,2048\nMvvv 88,232,200\nEX 187\nMvvv 92,236,200\ns_waitcnt lgkmcnt(2)\nTa 232,209,8448\nCV 199,186\n"
"Cvaa 180,184,0,160\nEX 188\nMvaa 180,188,4\nTa 234,209,12544\nMvaa 180,192,8\nEX 189\nMvaa 180,196,12\nTa 228,208,8704\nCV 200,188\n"
"Cvaa 184,200,0,160\nEX 190\nMvaa 184,204,4\nDtL 213,44,75,3072\nMvaa 184,208,8\nEX 191\nMvaa 184,212,12\nTa 230,208,12800\nCV 201,190\n"
"Mvav 96,248,196\nEX 192\nMvav 100,252,196\nTa 224,209,8704\nMvvv 104,216,196\nEX 193\nMvvv 108,220,196\nTa 226,209,12800\nCV 202,192\n"
"Mvvv 112,224,196\nEX 194\nMvvv 116,228,196\nTa 220,208,8960\nMvvv 120,232,196\nEX 195\nMvvv 124,236,196\nTa 222,208,13056\nCV 203,194\n"
"Cvaa 188,184,16,164\nTa 216,209,8960\nMvaa 188,188,20\nEX 180\nMvaa 188,192,24\nTa 218,209,13056\nv_add_u32 v208, 0xfffe0000, v208\nMvaa 188,196,28\n"
"EX 181\nv_smfmac_f32_16x16x64_bf16 a[176:179], a[180:183], v[196:203], v251\ns_memrealtime s[98:99]\nCV 196,180\nv_add_u32 v209, 0xfffe0000, v209\n"
"Cvaa 192,200,16,164\nEX 182\nMvaa 192,204,20\nMvaa 192,208,24\nEX 183\nMvaa 192,212,28\nCV 197,182\nMvav 128,248,200\ns_waitcnt vmcnt(12) lgkmcnt(0)\n"
"EX 184\nMvav 132,252,200\ns_barrier\n"
"s_sub_u32 s95, s98, s93\ns_mov_b32 s93, s98\ns_sub_u32 s95, 102, s95\ns_cbranch_scc1 .Lfa_t41b\ns_add_u32 s93, s93, s95\n.Lfa_t41a:\ns_sub_u32 s95, s95, 4\ns_cbranch_scc1 .Lfa_t41b\ns_sleep 1\ns_branch .Lfa_t41a\n.Lfa_t41b:\n"
"Mvvv 136,216,200\ns_add_u32 m0, s72, 0x12000\nEX 185\nMvvv 140,220,200\ns_waitcnt vmcnt(7)\nRa 248,204,0\nCV 198,184\nMvvv 144,224,200\nEX 186\n"
"Mvvv 148,228,200\nDL 214,48,75,0\nMvvv 152,232,200\nEX 187\nMvvv 156,236,200\nRa 252,205,0\nCV 199,186\nCvaa 180,184,32,168\nEX 188\nMvaa 180,188,36\n"
"Rv 216,206,0\nMvaa 180,192,40\nEX 189\nMvaa 180,196,44\nRv 220,207,0\nCV 200,188\nCvaa 184,200,32,168\nEX 190\nMvaa 184,204,36\nDL 215,48,75,1024\n"
"Mvaa 184,208,40\nEX 191\nMvaa 184,212,44\nRv 224,204,4096\nCV 201,190\nMvav 0,244,196\nEX 192\nMvav 4,240,196\nRv 228,205,4096\nMvav 8,236,196\n"
"EX 193\nMvav 12,232,196\nRv 232,206,4096\nCV 202,192\nMvav 16,228,196\ns_waitcnt vmcnt(7)\nEX 194\nMvav 20,224,196\nDL 214,48,75,2048\n"
"Mvav 24,220,196\nEX 195\nMvav 28,216,196\nRv 236,207,4096\nCV 203,194\nCvaa 188,184,48,172\nMvaa 188,188,52\nEX 180\nMvaa 188,192,56\n"
"Mvaa 188,196,60\nEX 181\nv_smfmac_f32_16x16x64_bf16 a[160:163], a[180:183], v[196:203], v251\nCV 196,180\nCvaa 192,200,48,172\nEX 182\n"
"Mvaa 192,204,52\nDL 215,48,75,3072\nMvaa 192,208,56\nEX 183\nMvaa 192,212,60\nCV 197,182\nMvav 32,244,200\nEX 184\nMvav 36,240,200\nMvav 40,236,200\n"
"EX 185\nMvav 44,232,200\nCV 198,184\nMvav 48,228,200\nEX 186\nMvav 52,224,200\nMvav 56,220,200\nEX 187\nMvav 60,216,200\nCV 199,186\n"
"Cvaa 180,184,64,176\nEX 188\nMvaa 180,188,68\nTa 184,208,8192\nMvaa 180,192,72\nEX 189\nMvaa 180,196,76\n.Lfa_pd9054:\ns_cbranch_execz .Lfa_wr3\n"
"Ta 186,208,12288\nCV 200,188\nCvaa 184,200,64,176\nEX 190\nMvaa 184,204,68\nTa 188,209,8192\nMvaa 184,208,72\nEX 191\nMvaa 184,212,76\n"
"Ta 190,209,12288\nCV 201,190\nMvav 64,244,196\nEX 192\nMvav 68,240,196\nTa 192,208,8448\nMvav 72,236,196\nEX 193\nMvav 76,232,196\nTa 194,208,12544\n"
"CV 202,192\nMvav 80,228,196\nEX 194\nMvav 84,224,196\nTa 196,209,8448\nMvav 88,220,196\nEX 195\nMvav 92,216,196\ns_waitcnt lgkmcnt(3)\n"
"Ta 198,209,12544\nCV 203,194\nCvaa 188,248,0,160\nTa 200,208,8704\nMvaa 188,252,4\nEX 180\nMvva 188,216,8\nTa 202,208,12800\nMvva 188,220,12\nEX 181\n"
"v_smfmac_f32_16x16x64_bf16 a[164:167], a[180:183], v[196:203], v251\nTa 204,209,8704\nCV 196,180\nCvva 192,224,0,160\nEX 182\nMvva 192,228,4\n"
"Ta 206,209,12800\nMvva 192,232,8\nEX 183\nMvva 192,236,12\nTa 208,208,8960\nCV 197,182\nMvav 96,244,200\nEX 184\nMvav 100,240,200\nTa 210,208,13056\n"
"Mvav 104,236,200\nEX 185\nMvav 108,232,200\nTa 212,209,8960\nCV 198,184\nMvav 112,228,200\nEX 186\nMvav 116,224,200\nTa 214,209,13056\n"
"Mvav 120,220,200\nEX 187\nMvav 124,216,200\nCV 199,186\nCvaa 180,248,16,164\nEX 188\nMvaa 180,252,20\nMvva 180,216,24\nEX 189\nMvva 180,220,28\n"
"s_memrealtime s[98:99]\nCV 200,188\nCvva 184,224,16,164\nEX 190\nMvva 184,228,20\nMvva 184,232,24\nEX 191\nMvva 184,236,28\nCV 201,190\n"
"Mvav 128,244,196\ns_waitcnt vmcnt(8) lgkmcnt(0)\nEX 192\nMvav 132,240,196\ns_barrier\n"
"s_sub_u32 s95, s98, s93\ns_mov_b32 s93, s98\ns_sub_u32 s95, 102, s95\ns_cbranch_scc1 .Lfa_t42b\ns_add_u32 s93, s93, s95\n.Lfa_t42a:\ns_sub_u32 s95, s95, 4\ns_cbranch_scc1 .Lfa_t42b\ns_sleep 1\ns_branch .Lfa_t42a\n.Lfa_t42b:\n"
"Mvav 136,236,196\ns_add_u32 s75, s97, 0x1c000\nEX 193\nMvav 140,232,196\ns_and_b32 s75, s75, 0x1ffff\nRa 244,204,32768\nCV 202,192\nMvav 144,228,196\n"
"s_add_u32 s75, s75, 0x40000\nEX 194\nMvav 148,224,196\ns_add_u32 m0, s72, 0x18000\nRa 240,205,32768\nMvav 152,220,196\ns_waitcnt vmcnt(7)\nEX 195\n"
"Mvav 156,216,196\nRa 236,206,32768\nCV 203,194\nCvaa 188,248,32,168\nDtL 210,44,75,0\nMvaa 188,252,36\nEX 180\nMvva 188,216,40\nRa 232,207,32768\n"
"Mvva 188,220,44\nEX 181\nv_smfmac_f32_16x16x64_bf16 a[168:171], a[180:183], v[196:203], v251\nRa 228,204,36864\nCV 196,180\nCvva 192,224,32,168\n"
"EX 182\nMvva 192,228,36\nRa 224,205,36864\nv_add_u32 v204, 0x10000, v204\nMvva 192,232,40\nEX 183\nMvva 192,236,44\nRa 220,206,36864\nCV 197,182\n"
"Mvav 0,184,200\nEX 184\nMvav 4,188,200\nDtL 211,44,75,1024\nMvav 8,192,200\nEX 185\nMvav 12,196,200\nRa 216,207,36864\nCV 198,184\nMvav 16,200,200\n"
"EX 186\nMvav 20,204,200\nv_add_u32 v205, 0x10000, v205\nv_add_u32 v206, 0x10000, v206\nMvav 24,208,200\nEX 187\nMvav 28,212,200\nCV 199,186\n"
"v_add_u32 v207, 0x10000, v207\nCvaa 180,248,48,172\ns_waitcnt vmcnt(7)\nEX 188\nMvaa 180,252,52\nDtL 212,44,75,2048\nMvva 180,216,56\nEX 189\n"
"Mvva 180,220,60\nCV 200,188\nCvva 184,224,48,172\nEX 190\nMvva 184,228,52\nMvva 184,232,56\nEX 191\nMvva 184,236,60\nCV 201,190\nMvav 32,184,196\n"
"EX 192\nMvav 36,188,196\nDtL 213,44,75,3072\nMvav 40,192,196\nEX 193\nMvav 44,196,196\nCV 202,192\nMvav 48,200,196\nEX 194\nMvav 52,204,196\n"
"Mvav 56,208,196\nEX 195\nMvav 60,212,196\ns_add_u32 m0, s72, 0x1a000\nCV 203,194\nCvaa 188,248,64,176\ns_waitcnt vmcnt(7)\nTa 248,208,40960\n"
"Mvaa 188,252,68\nEX 180\nMvva 188,216,72\nDL 214,48,75,0\nMvva 188,220,76\nEX 181\n"
"v_smfmac_f32_16x16x64_bf16 a[172:175], a[180:183], v[196:203], v251\nTa 250,208,45056\nCV 196,180\nCvva 192,224,64,176\nEX 182\nMvva 192,228,68\n"
"Ta 252,209,40960\nMvva 192,232,72\nEX 183\nMvva 192,236,76\nTa 254,209,45056\nCV 197,182\nMvav 64,184,200\nEX 184\nMvav 68,188,200\n"
"DL 215,48,75,1024\nMvav 72,192,200\nEX 185\nMvav 76,196,200\nTv 216,208,41216\nCV 198,184\nMvav 80,200,200\nEX 186\nMvav 84,204,200\n"
"Tv 218,208,45312\nMvav 88,208,200\nEX 187\nMvav 92,212,200\ns_waitcnt lgkmcnt(2)\nTv 220,209,41216\nCV 199,186\nCvaa 180,244,0,160\n"
"s_waitcnt vmcnt(7)\nEX 188\nMvaa 180,240,4\nDL 214,48,75,2048\nMvaa 180,236,8\nEX 189\nMvaa 180,232,12\nTv 222,209,45312\nCV 200,188\n"
"Cvaa 184,228,0,160\nEX 190\nMvaa 184,224,4\nTv 224,208,41472\nMvaa 184,220,8\nEX 191\nMvaa 184,216,12\nTv 226,208,45568\nCV 201,190\nMvav 96,184,196\n"
"EX 192\nMvav 100,188,196\nDL 215,48,75,3072\nMvav 104,192,196\nEX 193\nMvav 108,196,196\nTv 228,209,41472\nCV 202,192\nMvav 112,200,196\nEX 194\n"
"Mvav 116,204,196\nTv 230,209,45568\nMvav 120,208,196\nEX 195\nMvav 124,212,196\nTv 232,208,41728\nCV 203,194\nCvaa 188,244,16,164\nTv 234,208,45824\n"
"Mvaa 188,240,20\nEX 180\nMvaa 188,236,24\nTv 236,209,41728\nv_add_u32 v208, 0x10000, v208\nMvaa 188,232,28\nEX 181\n"
"v_smfmac_f32_16x16x64_bf16 a[176:179], a[180:183], v[196:203], v251\ns_memrealtime s[98:99]\nTv 238,209,45824\nCV 196,180\nCvaa 192,228,16,164\n"
"EX 182\nMvaa 192,224,20\nv_add_u32 v209, 0x10000, v209\nMvaa 192,220,24\nEX 183\nMvaa 192,216,28\nCV 197,182\nMvav 128,184,200\n"
"s_waitcnt vmcnt(8) lgkmcnt(0)\nEX 184\nMvav 132,188,200\ns_barrier\n"
"s_sub_u32 s95, s98, s93\ns_mov_b32 s93, s98\ns_sub_u32 s95, 102, s95\ns_cbranch_scc1 .Lfa_t43b\ns_add_u32 s93, s93, s95\n.Lfa_t43a:\ns_sub_u32 s95, s95, 4\ns_cbranch_scc1 .Lfa_t43b\ns_sleep 1\ns_branch .Lfa_t43a\n.Lfa_t43b:\n"
"Mvav 136,192,200\ns_add_u32 s75, s97, 0x1e000\nEX 185\nMvav 140,196,200\ns_and_b32 s75, s75, 0x1ffff\nRa 184,204,0\nCV 198,184\nMvav 144,200,200\n"
"s_add_u32 s75, s75, 0x40000\nEX 186\nMvav 148,204,200\ns_add_u32 m0, s72, 0x20000\nRa 188,205,0\nMvav 152,208,200\ns_waitcnt vmcnt(7)\nEX 187\n"
"Mvav 156,212,200\nRa 192,206,0\nCV 199,186\nCvaa 180,244,32,168\nEX 188\nMvaa 180,240,36\nDtL 210,44,75,0\nMvaa 180,236,40\nEX 189\nMvaa 180,232,44\n"
"Ra 196,207,0\nCV 200,188\nCvaa 184,228,32,168\nEX 190\nMvaa 184,224,36\nRa 200,204,4096\nMvaa 184,220,40\nEX 191\nMvaa 184,216,44\nRa 204,205,4096\n"
"CV 201,190\nMvav 0,248,196\nEX 192\nMvav 4,252,196\nDtL 211,44,75,1024\nMvvv 8,216,196\nEX 193\nMvvv 12,220,196\nRa 208,206,4096\nCV 202,192\n"
"Mvvv 16,224,196\nEX 194\nMvvv 20,228,196\nRa 212,207,4096\nMvvv 24,232,196\nEX 195\nMvvv 28,236,196\nCV 203,194\nCvaa 188,244,48,172\n"
"s_waitcnt vmcnt(7)\nMvaa 188,240,52\nEX 180\nMvaa 188,236,56\nDtL 212,44,75,2048\nMvaa 188,232,60\nEX 181\n"
"v_smfmac_f32_16x16x64_bf16 a[160:163], a[180:183], v[196:203], v251\nCV 196,180\nCvaa 192,228,48,172\nEX 182\nMvaa 192,224,52\nMvaa 192,220,56\n"
"EX 183\nMvaa 192,216,60\nCV 197,182\nMvav 32,248,200\nEX 184\nMvav 36,252,200\nDtL 213,44,75,3072\nMvvv 40,216,200\nEX 185\nMvvv 44,220,200\n"
"CV 198,184\nMvvv 48,224,200\nEX 186\nMvvv 52,228,200\nMvvv 56,232,200\nEX 187\nMvvv 60,236,200\ns_add_u32 m0, s72, 0x22000\nCV 199,186\n"
"Cvaa 180,244,64,176\ns_waitcnt vmcnt(7)\nEX 188\nMvaa 180,240,68\nDL 214,48,75,0\nMvaa 180,236,72\nEX 189\nMvaa 180,232,76\nTa 244,208,8192\n"
"CV 200,188\nCvaa 184,228,64,176\nEX 190\nMvaa 184,224,68\nTa 246,208,12288\nMvaa 184,220,72\nEX 191\nMvaa 184,216,76\nTa 240,209,8192\nCV 201,190\n"
"Mvav 64,248,196\nEX 192\nMvav 68,252,196\nDL 215,48,75,1024\nMvvv 72,216,196\nEX 193\nMvvv 76,220,196\nTa 242,209,12288\nCV 202,192\nMvvv 80,224,196\n"
"EX 194\nMvvv 84,228,196\nTa 236,208,8448\nMvvv 88,232,196\nEX 195\nMvvv 92,236,196\ns_waitcnt lgkmcnt(2)\nTa 238,208,12544\nCV 203,194\n"
"Cvaa 188,184,0,160\ns_waitcnt vmcnt(7)\nTa 232,209,8448\nMvaa 188,188,4\nEX 180\nMvaa 188,192,8\nDL 214,48,75,2048\nMvaa 188,196,12\nEX 181\n"
"v_smfmac_f32_16x16x64_bf16 a[164:167], a[180:183], v[196:203], v251\nTa 234,209,12544\nCV 196,180\nCvaa 192,200,0,160\nEX 182\nMvaa 192,204,4\n"
"Ta 228,208,8704\nMvaa 192,208,8\nEX 183\nMvaa 192,212,12\nTa 230,208,12800\nCV 197,182\nMvav 96,248,200\nEX 184\nMvav 100,252,200\nDL 215,48,75,3072\n"
"Mvvv 104,216,200\nEX 185\nMvvv 108,220,200\nTa 224,209,8704\nCV 198,184\nMvvv 112,224,200\nEX 186\nMvvv 116,228,200\nTa 226,209,12800\n"
"Mvvv 120,232,200\nEX 187\nMvvv 124,236,200\nTa 220,208,8960\nCV 199,186\nCvaa 180,184,16,164\nEX 188\nMvaa 180,188,20\nTa 222,208,13056\n"
"Mvaa 180,192,24\nEX 189\nMvaa 180,196,28\nTa 216,209,8960\nCV 200,188\nCvaa 184,200,16,164\nEX 190\nMvaa 184,204,20\nTa 218,209,13056\n"
"Mvaa 184,208,24\nEX 191\nMvaa 184,212,28\nCV 201,190\nMvav 128,248,196\ns_waitcnt vmcnt(8) lgkmcnt(0)\nEX 192\nMvav 132,252,196\ns_barrier\n"
"Mvvv 136,216,196\ns_add_u32 s76, s85, 0x80\nEX 193\nMvvv 140,220,196\ns_and_b32 s76, s76, 0x3ff\nRa 248,204,32768\nCV 202,192\nMvvv 144,224,196\n"
"s_add_u32 s76, s76, 0x0\nEX 194\nMvvv 148,228,196\ns_add_u32 m0, s94, 0x0\nRa 252,205,32768\nMvvv 152,232,196\nEX 195\nMvvv 156,236,196\n"
"Rv 216,206,32768\nCV 203,194\nCvaa 188,184,32,168\nDL 240,52,76,0\nMvaa 188,188,36\nEX 180\nMvaa 188,192,40\nRv 220,207,32768\nMvaa 188,196,44\n"
"EX 181\nv_smfmac_f32_16x16x64_bf16 a[168:171], a[180:183], v[196:203], v251\nRv 224,204,36864\nCV 196,180\nCvaa 192,200,32,168\nEX 182\n"
"Mvaa 192,204,36\nRv 228,205,36864\nv_add_u32 v204, 0x10000, v204\nMvaa 192,208,40\nEX 183\nMvaa 192,212,44\nRv 232,206,36864\nCV 197,182\n"
"Mvav 0,244,200\nEX 184\nMvav 4,240,200\nDL 241,52,76,1024\nMvav 8,236,200\nEX 185\nMvav 12,232,200\nRv 236,207,36864\nCV 198,184\nMvav 16,228,200\n"
"EX 186\nMvav 20,224,200\nv_add_u32 v205, 0x10000, v205\nv_add_u32 v206, 0x10000, v206\nMvav 24,220,200\nEX 187\nMvav 28,216,200\nCV 199,186\n"
"v_add_u32 v207, 0x10000, v207\nCvaa 180,184,48,172\nEX 188\nMvaa 180,188,52\nDL 242,52,76,2048\nMvaa 180,192,56\nEX 189\nMvaa 180,196,60\nCV 200,188\n"
"Cvaa 184,200,48,172\nEX 190\nMvaa 184,204,52\nMvaa 184,208,56\nEX 191\nMvaa 184,212,60\nCV 201,190\nMvav 32,244,196\nEX 192\nMvav 36,240,196\n"
"DL 243,52,76,3072\nMvav 40,236,196\nEX 193\nMvav 44,232,196\nCV 202,192\nMvav 48,228,196\nEX 194\nMvav 52,224,196\ns_add_u32 s76, s85, 0x80\n"
"Mvav 56,220,196\ns_and_b32 s76, s76, 0x3ff\nEX 195\nMvav 60,216,196\ns_add_u32 s76, s76, 0x10000\nCV 203,194\nCvaa 188,184,64,176\n"
"s_add_u32 m0, s94, 0x4000\nTa 184,208,40960\nMvaa 188,188,68\nEX 180\nMvaa 188,192,72\nDL 240,52,76,0\nMvaa 188,196,76\nEX 181\n"
"v_smfmac_f32_16x16x64_bf16 a[172:175], a[180:183], v[196:203], v251\nTa 186,208,45056\nCV 196,180\nCvaa 192,200,64,176\n"
".Lfa_pd9628:\ns_cbranch_execz .Lfa_wr4\nEX 182\nMvaa 192,204,68\nTa 188,209,40960\nMvaa 192,208,72\nEX 183\nMvaa 192,212,76\nTa 190,209,45056\n"
"CV 197,182\nMvav 64,244,200\nEX 184\nMvav 68,240,200\nDL 241,52,76,1024\nMvav 72,236,200\nEX 185\nMvav 76,232,200\nTa 192,208,41216\nCV 198,184\n"
"Mvav 80,228,200\nEX 186\nMvav 84,224,200\nTa 194,208,45312\nMvav 88,220,200\nEX 187\nMvav 92,216,200\ns_waitcnt lgkmcnt(2)\nTa 196,209,41216\n"
"CV 199,186\nCvaa 180,248,0,160\nEX 188\nMvaa 180,252,4\nDL 242,52,76,2048\nMvva 180,216,8\nEX 189\nMvva 180,220,12\nTa 198,209,45312\nCV 200,188\n"
"Cvva 184,224,0,160\nEX 190\nMvva 184,228,4\nTa 200,208,41472\nMvva 184,232,8\nEX 191\nMvva 184,236,12\nTa 202,208,45568\nCV 201,190\nMvav 96,244,196\n"
"EX 192\nMvav 100,240,196\nDL 243,52,76,3072\nMvav 104,236,196\nEX 193\nMvav 108,232,196\nTa 204,209,41472\nCV 202,192\nMvav 112,228,196\nEX 194\n"
"Mvav 116,224,196\nTa 206,209,45568\nMvav 120,220,196\nEX 195\nMvav 124,216,196\nTa 208,208,41728\nCV 203,194\nCvaa 188,248,16,164\nTa 210,208,45824\n"
"Mvaa 188,252,20\nEX 180\nMvva 188,216,24\nTa 212,209,41728\nv_add_u32 v208, 0x10000, v208\nMvva 188,220,28\nEX 181\n"
"v_smfmac_f32_16x16x64_bf16 a[176:179], a[180:183], v[196:203], v251\nTa 214,209,45824\nCV 196,180\nCvva 192,224,16,164\nEX 182\nMvva 192,228,20\n"
"v_add_u32 v209, 0x10000, v209\nMvva 192,232,24\nEX 183\nMvva 192,236,28\nCV 197,182\nMvav 128,244,200\ns_waitcnt vmcnt(8) lgkmcnt(0)\nEX 184\n"
"Mvav 132,240,200\ns_barrier\nMvav 136,236,200\ns_add_u32 s76, s85, 0x80\nEX 185\nMvav 140,232,200\ns_and_b32 s76, s76, 0x3ff\nRa 244,204,0\n"
"CV 198,184\nMvav 144,228,200\ns_add_u32 s76, s76, 0x40000\nEX 186\nMvav 148,224,200\ns_add_u32 m0, s94, 0x8000\nRa 240,205,0\nMvav 152,220,200\n"
"EX 187\nMvav 156,216,200\nRa 236,206,0\nCV 199,186\nCvaa 180,248,32,168\nEX 188\nMvaa 180,252,36\nDL 240,52,76,0\nMvva 180,216,40\nEX 189\n"
"Mvva 180,220,44\nRa 232,207,0\nCV 200,188\nCvva 184,224,32,168\nEX 190\nMvva 184,228,36\nRa 228,204,4096\nMvva 184,232,40\nEX 191\nMvva 184,236,44\n"
"Ra 224,205,4096\nCV 201,190\nMvav 0,184,196\nEX 192\nMvav 4,188,196\nDL 241,52,76,1024\nMvav 8,192,196\nEX 193\nMvav 12,196,196\nRa 220,206,4096\n"
"CV 202,192\nMvav 16,200,196\nEX 194\nMvav 20,204,196\nRa 216,207,4096\nMvav 24,208,196\nEX 195\nMvav 28,212,196\nCV 203,194\nCvaa 188,248,48,172\n"
"Mvaa 188,252,52\nEX 180\nMvva 188,216,56\nDL 242,52,76,2048\nMvva 188,220,60\nEX 181\n"
"v_smfmac_f32_16x16x64_bf16 a[160:163], a[180:183], v[196:203], v251\nCV 196,180\nCvva 192,224,48,172\nEX 182\nMvva 192,228,52\nMvva 192,232,56\n"
"EX 183\nMvva 192,236,60\nCV 197,182\nMvav 32,184,200\nEX 184\nMvav 36,188,200\nDL 243,52,76,3072\nMvav 40,192,200\nEX 185\nMvav 44,196,200\n"
"CV 198,184\nMvav 48,200,200\nEX 186\nMvav 52,204,200\ns_add_u32 s76, s85, 0x80\nMvav 56,208,200\ns_and_b32 s76, s76, 0x3ff\nEX 187\nMvav 60,212,200\n"
"s_add_u32 s76, s76, 0x50000\nCV 199,186\nCvaa 180,248,64,176\ns_add_u32 m0, s94, 0xc000\nEX 188\nMvaa 180,252,68\nDL 240,52,76,0\nMvva 180,216,72\n"
"EX 189\nMvva 180,220,76\nTa 248,208,8192\nCV 200,188\nCvva 184,224,64,176\nEX 190\nMvva 184,228,68\nTa 250,208,12288\nMvva 184,232,72\nEX 191\n"
"Mvva 184,236,76\nTa 252,209,8192\nCV 201,190\nMvav 64,184,196\nEX 192\nMvav 68,188,196\nDL 241,52,76,1024\nMvav 72,192,196\nEX 193\nMvav 76,196,196\n"
"Ta 254,209,12288\nCV 202,192\nMvav 80,200,196\nEX 194\nMvav 84,204,196\nTv 216,208,8448\nMvav 88,208,196\nEX 195\nMvav 92,212,196\n"
"s_waitcnt lgkmcnt(2)\nTv 218,208,12544\nCV 203,194\nCvaa 188,244,0,160\nTv 220,209,8448\nMvaa 188,240,4\nEX 180\nMvaa 188,236,8\nDL 242,52,76,2048\n"
"Mvaa 188,232,12\nEX 181\nv_smfmac_f32_16x16x64_bf16 a[164:167], a[180:183], v[196:203], v251\nTv 222,209,12544\nCV 196,180\nCvaa 192,228,0,160\n"
"EX 182\nMvaa 192,224,4\nTv 224,208,8704\nMvaa 192,220,8\nEX 183\nMvaa 192,216,12\nTv 226,208,12800\nCV 197,182\nMvav 96,184,200\nEX 184\n"
"Mvav 100,188,200\nDL 243,52,76,3072\nMvav 104,192,200\nEX 185\nMvav 108,196,200\nTv 228,209,8704\nCV 198,184\nMvav 112,200,200\nEX 186\n"
"Mvav 116,204,200\nTv 230,209,12800\nMvav 120,208,200\nEX 187\nMvav 124,212,200\nTv 232,208,8960\nCV 199,186\nCvaa 180,244,16,164\nEX 188\n"
"Mvaa 180,240,20\nTv 234,208,13056\nMvaa 180,236,24\nEX 189\nMvaa 180,232,28\nTv 236,209,8960\nCV 200,188\nCvaa 184,228,16,164\nEX 190\n"
"Mvaa 184,224,20\nTv 238,209,13056\nMvaa 184,220,24\nEX 191\nMvaa 184,216,28\nCV 201,190\nMvav 128,184,196\ns_waitcnt lgkmcnt(0)\nEX 192\n"
"Mvav 132,188,196\ns_barrier\nMvav 136,192,196\nEX 193\nMvav 140,196,196\nCV 202,192\nMvav 144,200,196\nEX 194\nMvav 148,204,196\n"
"ds_write_b128 v248, a[112:115] offset:0\nMvav 152,208,196\nEX 195\nMvav 156,212,196\nCV 203,194\nCvaa 188,244,32,168\nMvaa 188,240,36\nEX 180\n"
"Mvaa 188,236,40\nds_write_b128 v248, a[116:119] offset:1024\nMvaa 188,232,44\nEX 181\n"
"v_smfmac_f32_16x16x64_bf16 a[168:171], a[180:183], v[196:203], v251\nCV 196,180\nCvaa 192,228,32,168\nEX 182\nMvaa 192,224,36\n"
"buffer_load_dword v253, v249, s[52:55], 0 offen offset:0\nMvaa 192,220,40\nEX 183\nMvaa 192,216,44\nCV 197,182\nMvav 0,248,200\nEX 184\n"
"Mvav 4,252,200\nds_write_b128 v248, a[120:123] offset:2048\nMvvv 8,216,200\nEX 185\nMvvv 12,220,200\nCV 198,184\nMvvv 16,224,200\nEX 186\n"
"Mvvv 20,228,200\nds_write_b128 v248, a[124:127] offset:3072\nMvvv 24,232,200\nEX 187\nMvvv 28,236,200\nCV 199,186\nCvaa 180,244,48,172\nEX 188\n"
"Mvaa 180,240,52\nds_write_b128 v248, a[96:99] offset:4096\nMvaa 180,236,56\nEX 189\nMvaa 180,232,60\nCV 200,188\nCvaa 184,228,48,172\nEX 190\n"
"Mvaa 184,224,52\nds_write_b128 v248, a[100:103] offset:5120\nMvaa 184,220,56\nEX 191\nMvaa 184,216,60\nCV 201,190\nMvav 32,248,196\nEX 192\n"
"Mvav 36,252,196\nbuffer_load_dword v253, v250, s[52:55], 0 offen offset:0\nMvvv 40,216,196\nEX 193\nMvvv 44,220,196\nCV 202,192\nMvvv 48,224,196\n"
"EX 194\nMvvv 52,228,196\nds_write_b128 v248, a[104:107] offset:6144\nMvvv 56,232,196\nEX 195\nMvvv 60,236,196\nCV 203,194\nCvaa 188,244,64,176\n"
"Mvaa 188,240,68\nEX 180\nMvaa 188,236,72\nds_write_b128 v248, a[108:111] offset:7168\nMvaa 188,232,76\nEX 181\n"
"v_smfmac_f32_16x16x64_bf16 a[172:175], a[180:183], v[196:203], v251\nCV 196,180\nCvaa 192,228,64,176\nEX 182\nMvaa 192,224,68\n"
"ds_write_b128 v248, a[144:147] offset:8192\nMvaa 192,220,72\nEX 183\nMvaa 192,216,76\nCV 197,182\nMvav 64,248,200\nEX 184\nMvav 68,252,200\n"
"ds_write_b128 v248, a[148:151] offset:9216\nMvvv 72,216,200\nEX 185\nMvvv 76,220,200\nCV 198,184\nMvvv 80,224,200\nEX 186\nMvvv 84,228,200\n"
"ds_write_b128 v248, a[152:155] offset:10240\nMvvv 88,232,200\nEX 187\nMvvv 92,236,200\nCV 199,186\ns_nop 1\nMvav 96,248,196\nEX 188\n"
"Mvav 100,252,196\nEX 189\nMvvv 104,216,196\nEX 190\nCV 200,188\nMvvv 108,220,196\nEX 191\nMvvv 112,224,196\nEX 192\nCV 201,190\nMvvv 116,228,196\n"
"EX 193\nMvvv 120,232,196\nEX 194\nCV 202,192\nMvvv 124,236,196\nEX 195\ns_nop 0\nCV 203,194\ns_nop 1\nMvav 128,248,200\n"
"ds_write_b128 v248, a[156:159] offset:11264\nMvav 132,252,200\nMvvv 136,216,200\nMvvv 140,220,200\n"
"v_smfmac_f32_16x16x64_bf16 a[176:179], a[180:183], v[196:203], v251\nMvvv 144,224,200\nMvvv 148,228,200\nMvvv 152,232,200\n"
"s_waitcnt vmcnt(10) lgkmcnt(0)\nMvvv 156,236,200\ns_barrier\ns_nop 7\ns_cmp_eq_u32 s83, 0\ns_movk_i32 s88, 0x6000\ns_movk_i32 s89, 0xa000\n"
"s_cselect_b32 s88, s88, s89\nv_add_u32 v204, s88, v248\nRa 0,204,0\nRa 4,204,1024\nRa 8,204,2048\nRa 12,204,3072\nRa 16,204,4096\nRa 20,204,5120\n"
"Ra 24,204,6144\nRa 28,204,7168\nRa 32,204,8192\nRa 36,204,9216\nRa 40,204,10240\nRa 44,204,11264\nv_xor_b32 v249, 0x2000, v244\n"
"v_xor_b32 v250, 0x2000, v245\nv_xor_b32 v251, 0x2000, v246\nv_xor_b32 v252, 0x2000, v247\nTa 184,244,0\nTa 186,244,2048\nTa 188,244,4096\n"
"Ta 190,244,6144\nTa 192,249,0\nTa 194,249,2048\nTa 196,249,4096\nTa 198,249,6144\nTa 200,244,16384\nTa 202,244,18432\nTa 204,244,20480\n"
"Ta 206,244,22528\nTa 208,249,16384\nTa 210,249,18432\nTa 212,249,20480\nTa 214,249,22528\nv_accvgpr_read_b32 v160, a160\n"
"v_accvgpr_read_b32 v161, a161\nv_accvgpr_read_b32 v162, a162\nv_accvgpr_read_b32 v163, a163\nv_accvgpr_read_b32 v164, a164\n"
"v_accvgpr_read_b32 v165, a165\nv_accvgpr_read_b32 v166, a166\nv_accvgpr_read_b32 v167, a167\nv_accvgpr_read_b32 v168, a168\n"
"v_accvgpr_read_b32 v169, a169\nv_accvgpr_read_b32 v170, a170\nv_accvgpr_read_b32 v171, a171\nv_accvgpr_read_b32 v172, a172\n"
"v_accvgpr_read_b32 v173, a173\nv_accvgpr_read_b32 v174, a174\nv_accvgpr_read_b32 v175, a175\nv_accvgpr_read_b32 v176, a176\n"
"v_accvgpr_read_b32 v177, a177\nv_accvgpr_read_b32 v178, a178\nv_accvgpr_read_b32 v179, a179\nAD 160,161\nAD 162,163\nAD 164,165\nAD 166,167\n"
"AD 168,169\nAD 170,171\nAD 172,173\nAD 174,175\nAD 176,177\nAD 178,179\nv_add_f32 v180, v160, v170\nv_add_f32 v182, v172, v162\n"
"v_add_f32 v184, v164, v174\nv_add_f32 v186, v176, v166\nv_add_f32 v188, v168, v178\nv_add_f32 v161, v180, v182\nv_add_f32 v162, v184, v186\n"
"AD 161,188\nAD 161,162\nv_rcp_f32 v180, v180\nv_rcp_f32 v182, v182\nv_rcp_f32 v184, v184\nv_rcp_f32 v186, v186\nv_rcp_f32 v188, v188\n"
"v_add_f32 v162, v180, v182\nv_add_f32 v163, v184, v186\nAD 161,162\nAD 163,188\nAD 161,163\nv_cmp_ngt_f32 vcc, s82, v161\ns_nop 1\n"
"s_cmp_lg_u64 vcc, 0\ns_cselect_b32 s79, 1, s79\nPV 64,184\nPV 66,184\nPV 68,184\nPV 70,184\nCV 64,64\nCV 65,66\nCV 66,68\nCV 67,70\n"
"ds_write_b128 v248, v[64:67] offset:49152\nPV 0,180\nPV 2,180\nPV 4,180\nPV 72,184\nPV 74,184\nPV 76,184\nPV 78,184\nCV 72,72\nCV 73,74\nCV 74,76\n"
"CV 75,78\nds_write_b128 v248, v[72:75] offset:50176\nPV 6,180\nPV 8,180\nPV 10,180\nPV 80,184\nPV 82,184\nPV 84,184\nPV 86,184\nCV 80,80\nCV 81,82\n"
"CV 82,84\nCV 83,86\nds_write_b128 v248, v[80:83] offset:51200\nPV 12,180\nPV 14,180\nPV 16,180\nPV 88,184\nPV 90,184\nPV 92,184\nPV 94,184\nCV 88,88\n"
"CV 89,90\nCV 90,92\nCV 91,94\nds_write_b128 v248, v[88:91] offset:52224\nPV 18,180\nPV 20,180\nPV 22,180\nPV 32,182\nPV 34,182\nPV 36,182\nPV 38,182\n"
"CV 32,32\nCV 33,34\nCV 34,36\nCV 35,38\nds_write_b128 v248, v[32:35] offset:53248\nPV 24,180\nPV 26,180\nPV 28,180\nPV 40,182\nPV 42,182\nPV 44,182\n"
"PV 46,182\nCV 40,40\nCV 41,42\nCV 42,44\nCV 43,46\nds_write_b128 v248, v[40:43] offset:54272\nPV 30,180\nPV 96,186\nPV 98,186\nPV 48,182\nPV 50,182\n"
".Lfa_pd10212:\ns_cbranch_execz .Lfa_wr4\nPV 52,182\nPV 54,182\nCV 48,48\nCV 49,50\nCV 50,52\nCV 51,54\nds_write_b128 v248, v[48:51] offset:55296\n"
"PV 100,186\nPV 102,186\nPV 104,186\nPV 56,182\nPV 58,182\nPV 60,182\nPV 62,182\nCV 56,56\nCV 57,58\nCV 58,60\nCV 59,62\n"
"ds_write_b128 v248, v[56:59] offset:56320\nPV 106,186\nPV 108,186\nPV 110,186\nPV 128,188\nPV 130,188\nPV 132,188\nPV 134,188\nCV 128,128\n"
"CV 129,130\nCV 130,132\nCV 131,134\nds_write_b128 v248, v[128:131] offset:57344\nPV 112,186\nPV 114,186\nPV 116,186\nPV 136,188\nPV 138,188\n"
"PV 140,188\nPV 142,188\nCV 136,136\nCV 137,138\nCV 138,140\nCV 139,142\nds_write_b128 v248, v[136:139] offset:58368\nPV 118,186\nPV 120,186\n"
"PV 122,186\nPV 144,188\nPV 146,188\nPV 148,188\nPV 150,188\nCV 144,144\nCV 145,146\nCV 146,148\nCV 147,150\n"
"ds_write_b128 v248, v[144:147] offset:59392\nPV 124,186\nPV 126,186\nPV 152,188\nPV 154,188\nPV 156,188\nPV 158,188\nCV 152,152\nCV 153,154\n"
"CV 154,156\nCV 155,158\nds_write_b128 v248, v[152:155] offset:60416\ns_waitcnt vmcnt(10) lgkmcnt(0)\ns_barrier\ns_branch .Lfa_h\n"
".p2align 12\n.Lfa_x:\ns_lshl_b32 s88, s83, 12\ns_xor_b32 s89, s88, 0x1000\nv_add_u32 v196, s88, v240\nv_add_u32 v198, s89, v240\n"
"v_add_u32 v197, s88, v241\nv_add_u32 v199, s89, v241\nv_mbcnt_lo_u32_b32 v253, -1, 0\nv_mbcnt_hi_u32_b32 v253, -1, v253\ns_lshl_b32 s88, s83, 14\n"
"v_and_b32 v202, 15, v253\nv_lshl_add_u32 v203, v202, 4, s88\nv_lshrrev_b32 v202, 5, v253\nv_lshl_add_u32 v203, v202, 10, v203\n"
"v_bfe_u32 v253, v253, 4, 1\nv_lshl_add_u32 v203, v253, 12, v203\nv_lshl_add_u32 v202, v253, 3, v203\n"
"s_mov_b64 exec, 0\ns_cmp_eq_u32 s60, 0\ns_cbranch_scc1 .Lfa_w6_0\ns_cmp_eq_u32 s60, 1\ns_cbranch_scc1 .Lfa_w6_1\ns_cmp_eq_u32 s60, 2\ns_cbranch_scc1 .Lfa_w6_2\ns_branch .Lfa_w6_3\n.Lfa_wr6:\ns_mov_b64 exec, -1\ns_nop 4\n"
"s_waitcnt vmcnt(16) lgkmcnt(0)\ns_barrier\nRa 0,196,34816\nRa 4,240,36864\nRa 8,198,34816\nRa 20,197,34816\nRa 24,241,36864\nRa 28,199,34816\n"
"Ra 12,196,0\nRa 16,198,0\nRa 32,197,0\nRa 36,199,0\nTa 184,202,32768\nTa 186,202,33280\nTa 188,202,34816\nTa 190,202,35328\nTa 192,202,32776\n"
"Ta 194,202,33288\nTa 196,202,34824\nTa 198,202,35336\nTa 200,202,33024\nTa 202,202,33536\nTa 204,202,35072\nTa 206,202,35584\nTa 208,202,33032\n"
"Ta 210,202,33544\nTa 212,202,35080\nTa 214,202,35592\ns_add_u32 s76, s90, 0x80080\ns_and_b32 s76, s76, 0x3fffff\ns_add_u32 m0, s63, 0x10000\ns_nop 0\n"
"DL 242,40,76,0\nDL 243,40,76,1024\nDL 244,40,76,2048\nDL 245,40,76,3072\ns_add_u32 s77, s91, 0x100\ns_and_b32 s77, s77, 0x3ff\n"
"s_add_u32 m0, s74, 0x10000\ns_nop 0\nDL 249,36,77,0\nDL 250,36,77,1024\ns_waitcnt vmcnt(16) lgkmcnt(0)\ns_barrier\nZvaa 0,184,0\n"
"s_add_u32 s76, s90, 0x100000\nRa 52,196,2048\nMvaa 0,188,20\ns_and_b32 s76, s76, 0x3fffff\nRa 56,198,2048\nv_add_u32 v240, 0x20000, v240\n"
"Zvaa 32,184,4\ns_add_u32 m0, s63, 0x18000\nRa 72,197,2048\nv_add_u32 v196, 0x20000, v196\nMvaa 32,188,24\nDL 242,40,76,0\nZvaa 64,184,8\n"
"Ra 76,199,2048\nv_add_u32 v198, 0x20000, v198\nMvaa 64,188,28\nDL 243,40,76,1024\nZvaa 96,184,12\nTa 216,203,0\nv_add_u32 v197, 0x20000, v197\n"
"Mvaa 96,188,32\nDL 244,40,76,2048\nZvaa 128,184,16\nTa 218,203,512\nv_add_u32 v241, 0x20000, v241\nMvaa 128,188,36\nDL 245,40,76,3072\nZvaa 4,192,0\n"
"s_add_u32 s77, s91, 0x100\nTa 220,203,2048\nv_add_u32 v199, 0x20000, v199\nMvaa 4,196,20\ns_and_b32 s77, s77, 0x3ff\nTa 222,203,2560\n"
"v_add_u32 v202, 0x20000, v202\nZvaa 36,192,4\ns_add_u32 m0, s73, 0x18000\nTa 224,203,8\nMvaa 36,196,24\nDL 246,36,77,0\nZvaa 68,192,8\n"
"Ta 226,203,520\nMvaa 68,196,28\nDL 247,36,77,1024\nZvaa 100,192,12\nTa 228,203,2056\nMvaa 100,196,32\nDL 248,36,77,2048\nZvaa 132,192,16\n"
"s_add_u32 s76, s90, 0x100080\nTa 230,203,2568\nMvaa 132,196,36\ns_and_b32 s76, s76, 0x3fffff\nTa 232,203,256\nZvaa 8,200,0\n"
"s_add_u32 m0, s63, 0x8000\nTa 234,203,768\nMvaa 8,204,20\nDL 242,40,76,0\nZvaa 40,200,4\nTa 236,203,2304\nMvaa 40,204,24\nDL 243,40,76,1024\n"
"Zvaa 72,200,8\nTa 238,203,2816\nMvaa 72,204,28\nDL 244,40,76,2048\nZvaa 104,200,12\nTa 240,203,264\nMvaa 104,204,32\nDL 245,40,76,3072\n"
"Zvaa 136,200,16\ns_add_u32 s77, s91, 0x180\nTa 242,203,776\nMvaa 136,204,36\ns_and_b32 s77, s77, 0x3ff\nTa 244,203,2312\nZvaa 12,208,0\n"
"s_add_u32 m0, s74, 0x8000\nTa 246,203,2824\nMvaa 12,212,20\nDL 249,36,77,0\nZvaa 44,208,4\nMvaa 44,212,24\nDL 250,36,77,1024\nZvaa 76,208,8\n"
"Mvaa 76,212,28\nZvaa 108,208,12\nMvaa 108,212,32\nZvaa 140,208,16\ns_waitcnt vmcnt(20) lgkmcnt(0)\nMvaa 140,212,36\ns_barrier\nZvaa 16,216,0\n"
"s_add_u32 s76, s90, 0x180000\nRa 40,196,2048\nMvaa 16,220,20\ns_and_b32 s76, s76, 0x3fffff\nRa 44,240,4096\nZvaa 48,216,4\ns_add_u32 m0, s63, 0x0\n"
"Ra 48,198,2048\nv_add_u32 v196, 0xffff0000, v196\nMvaa 48,220,24\nDL 242,40,76,0\nZvaa 80,216,8\nRa 60,197,2048\nv_add_u32 v198, 0xffff0000, v198\n"
"Mvaa 80,220,28\nRa 64,241,4096\nZvaa 112,216,12\nRa 68,199,2048\nv_add_u32 v197, 0xffff0000, v197\nMvaa 112,220,32\nDL 243,40,76,1024\n"
"Zvaa 144,216,16\nTa 248,202,0\nv_add_u32 v199, 0xffff0000, v199\nMvaa 144,220,36\nTa 250,202,512\nZvaa 20,224,0\nTa 252,202,2048\nMvaa 20,228,20\n"
"DL 244,40,76,2048\nZvaa 52,224,4\nTa 254,202,2560\nMvaa 52,228,24\nTv 216,202,8\nZvaa 84,224,8\nTv 218,202,520\nMvaa 84,228,28\nDL 245,40,76,3072\n"
"Zvaa 116,224,12\ns_add_u32 s77, s91, 0x180\nTv 220,202,2056\nMvaa 116,228,32\ns_and_b32 s77, s77, 0x3ff\nTv 222,202,2568\nZvaa 148,224,16\n"
"s_add_u32 m0, s73, 0x0\nTv 224,202,256\nMvaa 148,228,36\nDL 246,36,77,0\nZvaa 24,232,0\nTv 226,202,768\nMvaa 24,236,20\nTv 228,202,2304\n"
"Zvaa 56,232,4\nTv 230,202,2816\nMvaa 56,236,24\nDL 247,36,77,1024\nZvaa 88,232,8\nTv 232,202,264\nMvaa 88,236,28\nTv 234,202,776\nZvaa 120,232,12\n"
"Tv 236,202,2312\nMvaa 120,236,32\nDL 248,36,77,2048\nZvaa 152,232,16\nTv 238,202,2824\nMvaa 152,236,36\nZvaa 28,240,0\n"
"v_add_u32 v202, 0xffff0000, v202\nMvaa 28,244,20\nZvaa 60,240,4\nMvaa 60,244,24\nZvaa 92,240,8\nMvaa 92,244,28\nZvaa 124,240,12\nMvaa 124,244,32\n"
"Zvaa 156,240,16\ns_waitcnt vmcnt(20) lgkmcnt(0)\nMvaa 156,244,36\ns_barrier\nMvaa 0,248,40\ns_add_u32 s76, s90, 0x180080\nRa 12,196,2048\n"
"v_add_u32 v240, 0xffff0000, v240\nMvaa 0,252,60\ns_and_b32 s76, s76, 0x3fffff\nRa 16,198,2048\nv_add_u32 v241, 0xffff0000, v241\nMvaa 32,248,44\n"
"s_add_u32 m0, s63, 0x20000\nRa 32,197,2048\nMvaa 32,252,64\nDL 242,40,76,0\nMvaa 64,248,48\nRa 36,199,2048\nMvaa 64,252,68\nTa 184,202,0\n"
"Mvaa 96,248,52\nTa 186,202,512\nMvaa 96,252,72\nDL 243,40,76,1024\nMvaa 128,248,56\nTa 188,202,2048\nMvaa 128,252,76\nTa 190,202,2560\nMvva 4,216,40\n"
"Ta 192,202,8\nMvva 4,220,60\nDL 244,40,76,2048\nMvva 36,216,44\nTa 194,202,520\nMvva 36,220,64\nTa 196,202,2056\nMvva 68,216,48\nTa 198,202,2568\n"
"Mvva 68,220,68\nDL 245,40,76,3072\nMvva 100,216,52\ns_add_u32 s77, s91, 0x200\nTa 200,202,256\nMvva 100,220,72\ns_and_b32 s77, s77, 0x3ff\n"
"Ta 202,202,768\nMvva 132,216,56\ns_add_u32 m0, s74, 0x20000\nTa 204,202,2304\nMvva 132,220,76\nDL 249,36,77,0\nMvva 8,224,40\nTa 206,202,2816\n"
"Mvva 8,228,60\nTa 208,202,264\nMvva 40,224,44\nTa 210,202,776\nMvva 40,228,64\nDL 250,36,77,1024\nMvva 72,224,48\nTa 212,202,2312\nMvva 72,228,68\n"
"Ta 214,202,2824\nMvva 104,224,52\nMvva 104,228,72\nMvva 136,224,56\nMvva 136,228,76\nMvva 12,232,40\nMvva 12,236,60\nMvva 44,232,44\nMvva 44,236,64\n"
"Mvva 76,232,48\nMvva 76,236,68\nMvva 108,232,52\nMvva 108,236,72\nMvva 140,232,56\ns_waitcnt vmcnt(19) lgkmcnt(0)\nMvva 140,236,76\ns_barrier\n"
"Mvaa 16,184,40\ns_add_u32 s76, s90, 0x200000\nRa 0,196,34816\nMvaa 16,188,60\ns_and_b32 s76, s76, 0x3fffff\nRa 4,240,36864\nMvaa 48,184,44\n"
"s_add_u32 m0, s63, 0x10000\nRa 8,198,34816\nv_add_u32 v196, 0xffff0000, v196\nMvaa 48,188,64\nDL 242,40,76,0\nMvaa 80,184,48\nRa 20,197,34816\n"
"v_add_u32 v198, 0xffff0000, v198\nMvaa 80,188,68\nRa 24,241,36864\nMvaa 112,184,52\nRa 28,199,34816\nv_add_u32 v197, 0xffff0000, v197\n"
"Mvaa 112,188,72\nDL 243,40,76,1024\nMvaa 144,184,56\nTa 216,202,32768\nv_add_u32 v199, 0xffff0000, v199\nMvaa 144,188,76\nTa 218,202,33280\n"
"Mvaa 20,192,40\nTa 220,202,34816\nMvaa 20,196,60\nDL 244,40,76,2048\nMvaa 52,192,44\nTa 222,202,35328\nMvaa 52,196,64\nTa 224,202,32776\n"
"Mvaa 84,192,48\nTa 226,202,33288\nMvaa 84,196,68\nDL 245,40,76,3072\nMvaa 116,192,52\ns_add_u32 s77, s91, 0x200\nTa 228,202,34824\nMvaa 116,196,72\n"
"s_and_b32 s77, s77, 0x3ff\nTa 230,202,35336\nMvaa 148,192,56\ns_add_u32 m0, s73, 0x10000\nTa 232,202,33024\nMvaa 148,196,76\nDL 246,36,77,0\n"
"Mvaa 24,200,40\nTa 234,202,33536\nMvaa 24,204,60\nTa 236,202,35072\nMvaa 56,200,44\nTa 238,202,35584\nMvaa 56,204,64\nDL 247,36,77,1024\n"
"Mvaa 88,200,48\nTa 240,202,33032\nMvaa 88,204,68\nTa 242,202,33544\nMvaa 120,200,52\nTa 244,202,35080\nMvaa 120,204,72\nDL 248,36,77,2048\n"
"Mvaa 152,200,56\nTa 246,202,35592\nMvaa 152,204,76\nMvaa 28,208,40\nv_add_u32 v202, 0xffff0000, v202\nMvaa 28,212,60\nMvaa 60,208,44\nMvaa 60,212,64\n"
"Mvaa 92,208,48\nMvaa 92,212,68\nMvaa 124,208,52\nMvaa 124,212,72\nMvaa 156,208,56\ns_waitcnt vmcnt(20) lgkmcnt(0)\nMvaa 156,212,76\ns_barrier\n"
"Mvaa 0,216,0\ns_add_u32 s76, s90, 0x200080\nRa 52,196,34816\nv_add_u32 v240, 0xffff0000, v240\nMvaa 0,220,20\ns_and_b32 s76, s76, 0x3fffff\n"
"Ra 56,198,34816\nv_add_u32 v241, 0xffff0000, v241\nMvaa 32,216,4\ns_add_u32 m0, s63, 0x18000\nRa 72,197,34816\nMvaa 32,220,24\nDL 242,40,76,0\n"
"Mvaa 64,216,8\nRa 76,199,34816\nMvaa 64,220,28\nTa 248,202,32768\nMvaa 96,216,12\nTa 250,202,33280\nMvaa 96,220,32\nDL 243,40,76,1024\n"
"Mvaa 128,216,16\nTa 252,202,34816\nMvaa 128,220,36\nTa 254,202,35328\nMvaa 4,224,0\nTv 216,202,32776\nMvaa 4,228,20\nDL 244,40,76,2048\n"
"Mvaa 36,224,4\nTv 218,202,33288\nMvaa 36,228,24\nTv 220,202,34824\nMvaa 68,224,8\nTv 222,202,35336\nMvaa 68,228,28\nDL 245,40,76,3072\n"
"Mvaa 100,224,12\ns_add_u32 s77, s91, 0x280\nTv 224,202,33024\nMvaa 100,228,32\ns_and_b32 s77, s77, 0x3ff\nTv 226,202,33536\nMvaa 132,224,16\n"
"s_add_u32 m0, s74, 0x18000\nTv 228,202,35072\nMvaa 132,228,36\nDL 249,36,77,0\nMvaa 8,232,0\nTv 230,202,35584\nMvaa 8,236,20\nTv 232,202,33032\n"
"Mvaa 40,232,4\nTv 234,202,33544\nMvaa 40,236,24\nDL 250,36,77,1024\nMvaa 72,232,8\nTv 236,202,35080\nMvaa 72,236,28\nTv 238,202,35592\n"
"Mvaa 104,232,12\nMvaa 104,236,32\nbuffer_load_dword v252, v210, s[68:71], s96 offen offset:0\nMvaa 136,232,16\nMvaa 136,236,36\nMvaa 12,240,0\n"
"Mvaa 12,244,20\nbuffer_load_dword v252, v210, s[68:71], s96 offen offset:1024\nMvaa 44,240,4\nMvaa 44,244,24\nMvaa 76,240,8\nMvaa 76,244,28\n"
"Mvaa 108,240,12\nMvaa 108,244,32\nMvaa 140,240,16\ns_waitcnt vmcnt(21) lgkmcnt(0)\nMvaa 140,244,36\ns_barrier\nMvaa 16,248,0\n"
"s_add_u32 s76, s90, 0x280000\ns_waitcnt vmcnt(47)\nMvaa 16,252,20\ns_and_b32 s76, s76, 0x3fffff\nRa 40,196,2048\nv_readfirstlane_b32 s86, v180\n"
"Mvaa 48,248,4\ns_add_u32 m0, s63, 0x8000\nRa 44,240,4096\nv_readfirstlane_b32 s87, v181\nMvaa 48,252,24\nDL 242,40,76,0\nMvaa 80,248,8\n"
"Ra 48,198,2048\nv_add_u32 v196, 0x20000, v196\nMvaa 80,252,28\nRa 60,197,2048\ns_nop 3\nMvaa 112,248,12\ns_mul_i32 s87, s87, 0x19660d\n"
"Ra 64,241,4096\nv_add_u32 v198, 0x20000, v198\nMvaa 112,252,32\ns_add_u32 s87, s87, 0x3c6ef35f\nDL 243,40,76,1024\nMvaa 144,248,16\n"
"s_mul_i32 s86, s86, 0x41c64e6d\nRa 68,199,2048\nv_add_u32 v197, 0x20000, v197\nMvaa 144,252,36\ns_add_u32 s86, s86, 0x9e3779b9\nTa 184,202,0\n"
"Mvva 20,216,0\nTa 186,202,512\nv_add_u32 v199, 0x20000, v199\nMvva 20,220,20\nDL 244,40,76,2048\nMvva 52,216,4\nTa 188,202,2048\nMvva 52,220,24\n"
"Ta 190,202,2560\nMvva 84,216,8\nTa 192,202,8\nMvva 84,220,28\nDL 245,40,76,3072\nMvva 116,216,12\ns_add_u32 s77, s91, 0x280\nTa 194,202,520\n"
"Mvva 116,220,32\ns_and_b32 s77, s77, 0x3ff\nTa 196,202,2056\nMvva 148,216,16\ns_add_u32 m0, s73, 0x8000\nTa 198,202,2568\nMvva 148,220,36\n"
".Lfa_pd10812:\ns_cbranch_execz .Lfa_wr6\nDL 246,36,77,0\nMvva 24,224,0\nTa 200,202,256\nMvva 24,228,20\nTa 202,202,768\nMvva 56,224,4\n"
"Ta 204,202,2304\nMvva 56,228,24\nDL 247,36,77,1024\nMvva 88,224,8\nTa 206,202,2816\nMvva 88,228,28\nTa 208,202,264\nMvva 120,224,12\nTa 210,202,776\n"
"Mvva 120,228,32\nDL 248,36,77,2048\nMvva 152,224,16\nTa 212,202,2312\nMvva 152,228,36\nTa 214,202,2824\nMvva 28,232,0\nMvva 28,236,20\n"
"v_add_u32 v202, 0x20000, v202\nMvva 60,232,4\nMvva 60,236,24\nMvva 92,232,8\nMvva 92,236,28\nMvva 124,232,12\nMvva 124,236,32\nMvva 156,232,16\n"
"s_waitcnt vmcnt(22) lgkmcnt(0)\nMvva 156,236,36\ns_barrier\nMvaa 0,184,40\ns_add_u32 s76, s90, 0x280080\nRa 12,196,2048\n"
"v_add_u32 v240, 0x10000, v240\nMvaa 0,188,60\ns_and_b32 s76, s76, 0x3fffff\nRa 16,198,2048\nv_add_u32 v241, 0x10000, v241\nMvaa 32,184,44\n"
"s_add_u32 m0, s63, 0x0\nRa 32,197,2048\nv_add_u32 v196, 0xffff0000, v196\nMvaa 32,188,64\nDL 242,40,76,0\nMvaa 64,184,48\nRa 36,199,2048\n"
"v_add_u32 v198, 0xffff0000, v198\nMvaa 64,188,68\nTa 216,202,0\nv_add_u32 v197, 0xffff0000, v197\nMvaa 96,184,52\nTa 218,202,512\n"
"v_add_u32 v199, 0xffff0000, v199\nMvaa 96,188,72\nDL 243,40,76,1024\nMvaa 128,184,56\nTa 220,202,2048\nMvaa 128,188,76\nTa 222,202,2560\n"
"Mvaa 4,192,40\nTa 224,202,8\nMvaa 4,196,60\nDL 244,40,76,2048\nMvaa 36,192,44\nTa 226,202,520\nMvaa 36,196,64\nTa 228,202,2056\nMvaa 68,192,48\n"
"Ta 230,202,2568\nMvaa 68,196,68\nDL 245,40,76,3072\nMvaa 100,192,52\ns_add_u32 s77, s91, 0x300\nTa 232,202,256\nMvaa 100,196,72\n"
"s_and_b32 s77, s77, 0x3ff\nTa 234,202,768\nMvaa 132,192,56\ns_add_u32 m0, s74, 0x0\nTa 236,202,2304\nMvaa 132,196,76\nDL 249,36,77,0\nMvaa 8,200,40\n"
"Ta 238,202,2816\nMvaa 8,204,60\nTa 240,202,264\nMvaa 40,200,44\nTa 242,202,776\nMvaa 40,204,64\nDL 250,36,77,1024\nMvaa 72,200,48\nTa 244,202,2312\n"
"Mvaa 72,204,68\nTa 246,202,2824\nMvaa 104,200,52\nMvaa 104,204,72\nv_add_u32 v202, 0xffff0000, v202\nMvaa 136,200,56\nMvaa 136,204,76\n"
"Mvaa 12,208,40\nMvaa 12,212,60\nMvaa 44,208,44\nMvaa 44,212,64\nMvaa 76,208,48\nMvaa 76,212,68\nMvaa 108,208,52\nMvaa 108,212,72\nMvaa 140,208,56\n"
"s_waitcnt vmcnt(21) lgkmcnt(0)\nMvaa 140,212,76\ns_barrier\nMvaa 16,216,40\ns_add_u32 s76, s90, 0x300000\nRa 0,196,2048\nMvaa 16,220,60\n"
"s_and_b32 s76, s76, 0x3fffff\nRa 4,240,4096\nMvaa 48,216,44\ns_add_u32 m0, s63, 0x20000\nRa 8,198,2048\nMvaa 48,220,64\nDL 242,40,76,0\n"
"Mvaa 80,216,48\nRa 20,197,2048\nMvaa 80,220,68\nRa 24,241,4096\nMvaa 112,216,52\nRa 28,199,2048\nMvaa 112,220,72\nDL 243,40,76,1024\nMvaa 144,216,56\n"
"Ta 248,202,0\nMvaa 144,220,76\nTa 250,202,512\nMvaa 20,224,40\nTa 252,202,2048\nMvaa 20,228,60\nDL 244,40,76,2048\nMvaa 52,224,44\nTa 254,202,2560\n"
"Mvaa 52,228,64\nTv 216,202,8\nMvaa 84,224,48\nTv 218,202,520\nMvaa 84,228,68\nDL 245,40,76,3072\nMvaa 116,224,52\ns_add_u32 s77, s91, 0x300\n"
"Tv 220,202,2056\nMvaa 116,228,72\ns_and_b32 s77, s77, 0x3ff\nTv 222,202,2568\nMvaa 148,224,56\ns_add_u32 m0, s73, 0x20000\nTv 224,202,256\n"
"Mvaa 148,228,76\nDL 246,36,77,0\nMvaa 24,232,40\nTv 226,202,768\nMvaa 24,236,60\nTv 228,202,2304\nMvaa 56,232,44\nTv 230,202,2816\nMvaa 56,236,64\n"
"DL 247,36,77,1024\nMvaa 88,232,48\nTv 232,202,264\nMvaa 88,236,68\nTv 234,202,776\nMvaa 120,232,52\nTv 236,202,2312\nMvaa 120,236,72\n"
"DL 248,36,77,2048\nMvaa 152,232,56\nTv 238,202,2824\nMvaa 152,236,76\nMvaa 28,240,40\nMvaa 28,244,60\nMvaa 60,240,44\nMvaa 60,244,64\nMvaa 92,240,48\n"
"Mvaa 92,244,68\nMvaa 124,240,52\nMvaa 124,244,72\nMvaa 156,240,56\ns_waitcnt vmcnt(22) lgkmcnt(0)\nMvaa 156,244,76\ns_barrier\nMvaa 0,248,0\n"
"s_add_u32 s75, s97, 0x0\nRa 52,196,34816\nv_add_u32 v240, 0xffff0000, v240\nMvaa 0,252,20\ns_and_b32 s75, s75, 0x1ffff\nRa 56,198,34816\n"
"v_add_u32 v241, 0xffff0000, v241\nMvaa 32,248,4\ns_add_u32 m0, s72, 0x10000\nRa 72,197,34816\nv_add_u32 v196, 0xffff0000, v196\nMvaa 32,252,24\n"
"DtL 210,44,75,0\nMvaa 64,248,8\nRa 76,199,34816\nv_add_u32 v198, 0xffff0000, v198\nMvaa 64,252,28\nTa 184,202,32768\n"
"v_add_u32 v197, 0xffff0000, v197\nMvaa 96,248,12\nTa 186,202,33280\nv_add_u32 v199, 0xffff0000, v199\nMvaa 96,252,32\nDtL 211,44,75,1024\n"
"Mvaa 128,248,16\nTa 188,202,34816\nMvaa 128,252,36\nTa 190,202,35328\nMvva 4,216,0\nTa 192,202,32776\nMvva 4,220,20\nDtL 212,44,75,2048\n"
"Mvva 36,216,4\nTa 194,202,33288\nMvva 36,220,24\nTa 196,202,34824\nMvva 68,216,8\nTa 198,202,35336\nMvva 68,220,28\nDtL 213,44,75,3072\n"
"Mvva 100,216,12\nTa 200,202,33024\nMvva 100,220,32\nTa 202,202,33536\nMvva 132,216,16\nTa 204,202,35072\nMvva 132,220,36\nTa 206,202,35584\n"
"Mvva 8,224,0\nTa 208,202,33032\nMvva 8,228,20\nTa 210,202,33544\nMvva 40,224,4\nTa 212,202,35080\nMvva 40,228,24\nTa 214,202,35592\nMvva 72,224,8\n"
"Mvva 72,228,28\nv_add_u32 v202, 0xffff0000, v202\nMvva 104,224,12\nMvva 104,228,32\nMvva 136,224,16\nMvva 136,228,36\nMvva 12,232,0\nMvva 12,236,20\n"
"Mvva 44,232,4\nMvva 44,236,24\nMvva 76,232,8\nMvva 76,236,28\nMvva 108,232,12\nMvva 108,236,32\nMvva 140,232,16\ns_waitcnt vmcnt(17) lgkmcnt(0)\n"
"Mvva 140,236,36\ns_barrier\nMvaa 16,184,0\nRa 40,196,34816\nMvaa 16,188,20\ns_add_u32 m0, s72, 0x12000\nRa 44,240,36864\nMvaa 48,184,4\n"
"DtL 214,48,75,0\nMvaa 48,188,24\nRa 48,198,34816\nMvaa 80,184,8\nDtL 215,48,75,1024\nMvaa 80,188,28\nRa 60,197,34816\nMvaa 112,184,12\n"
"DtL 214,48,75,2048\nMvaa 112,188,32\nRa 64,241,36864\nMvaa 144,184,16\nDtL 215,48,75,3072\nMvaa 144,188,36\ns_add_u32 s76, s90, 0x300080\n"
"Ra 68,199,34816\nMvaa 20,192,0\ns_and_b32 s76, s76, 0x3fffff\nTa 216,202,32768\nMvaa 20,196,20\ns_add_u32 m0, s63, 0x18000\nTa 218,202,33280\n"
"Mvaa 52,192,4\nDL 242,40,76,0\nMvaa 52,196,24\nTa 220,202,34816\nMvaa 84,192,8\nDL 243,40,76,1024\nMvaa 84,196,28\nTa 222,202,35328\nMvaa 116,192,12\n"
"DL 244,40,76,2048\nMvaa 116,196,32\nTa 224,202,32776\nMvaa 148,192,16\nDL 245,40,76,3072\nMvaa 148,196,36\ns_add_u32 s77, s91, 0x380\n"
"Ta 226,202,33288\nMvaa 24,200,0\ns_and_b32 s77, s77, 0x3ff\nTa 228,202,34824\nMvaa 24,204,20\ns_add_u32 m0, s74, 0x18000\nTa 230,202,35336\n"
"Mvaa 56,200,4\nDL 249,36,77,0\nMvaa 56,204,24\nTa 232,202,33024\nMvaa 88,200,8\nDL 250,36,77,1024\nMvaa 88,204,28\nTa 234,202,33536\nMvaa 120,200,12\n"
"Ta 236,202,35072\nMvaa 120,204,32\nTa 238,202,35584\nMvaa 152,200,16\nTa 240,202,33032\nMvaa 152,204,36\nTa 242,202,33544\nMvaa 28,208,0\n"
"Ta 244,202,35080\nMvaa 28,212,20\nTa 246,202,35592\nMvaa 60,208,4\nMvaa 60,212,24\nMvaa 92,208,8\nMvaa 92,212,28\nMvaa 124,208,12\nMvaa 124,212,32\n"
"Mvaa 156,208,16\ns_waitcnt vmcnt(21) lgkmcnt(0)\nMvaa 156,212,36\ns_barrier\nMvaa 0,216,40\ns_add_u32 s76, s90, 0x380000\nRa 12,196,2048\n"
"v_add_u32 v240, 0x20000, v240\nMvaa 0,220,60\ns_and_b32 s76, s76, 0x3fffff\nRa 16,198,2048\nv_add_u32 v241, 0x20000, v241\nMvaa 32,216,44\n"
"s_add_u32 m0, s63, 0x8000\nRa 32,197,2048\nv_add_u32 v196, 0x20000, v196\nMvaa 32,220,64\nDL 242,40,76,0\nMvaa 64,216,48\nRa 36,199,2048\n"
"v_add_u32 v198, 0x20000, v198\nMvaa 64,220,68\nTa 248,202,0\nv_add_u32 v197, 0x20000, v197\nMvaa 96,216,52\nTa 250,202,512\n"
"v_add_u32 v199, 0x20000, v199\nMvaa 96,220,72\nDL 243,40,76,1024\nMvaa 128,216,56\nTa 252,202,2048\nv_mbcnt_lo_u32_b32 v253, -1, 0\nMvaa 128,220,76\n"
"Ta 254,202,2560\nv_mbcnt_hi_u32_b32 v253, -1, v253\nMvaa 4,224,40\ns_lshl_b32 s88, s83, 14\nTv 216,202,8\nMvaa 4,228,60\ns_add_u32 s88, s88, 0x10000\n"
"DL 244,40,76,2048\nMvaa 36,224,44\nTv 218,202,520\nv_and_b32 v253, 48, v253\nMvaa 36,228,64\nTv 220,202,2056\nv_add_u32 v253, s88, v253\n"
"Mvaa 68,224,48\nTv 222,202,2568\nMvaa 68,228,68\nDL 245,40,76,3072\nMvaa 100,224,52\ns_add_u32 s77, s91, 0x380\nTv 224,202,256\nMvaa 100,228,72\n"
"s_and_b32 s77, s77, 0x3ff\nTv 226,202,768\nMvaa 132,224,56\ns_add_u32 m0, s73, 0x8000\nTv 228,202,2304\nMvaa 132,228,76\nDL 246,36,77,0\n"
"Mvaa 8,232,40\nTv 230,202,2816\nMvaa 8,236,60\nTv 232,202,264\nMvaa 40,232,44\nTv 234,202,776\nMvaa 40,236,64\nDL 247,36,77,1024\nMvaa 72,232,48\n"
"Tv 236,202,2312\nMvaa 72,236,68\nTv 238,202,2824\nMvaa 104,232,52\nMvaa 104,236,72\nDL 248,36,77,2048\nMvaa 136,232,56\n"
"v_add_u32 v202, 0x20000, v202\nMvaa 136,236,76\nMvaa 12,240,40\nMvaa 12,244,60\nMvaa 44,240,44\nMvaa 44,244,64\nMvaa 76,240,48\nMvaa 76,244,68\n"
"Mvaa 108,240,52\nMvaa 108,244,72\nMvaa 140,240,56\ns_waitcnt vmcnt(21) lgkmcnt(0)\nMvaa 140,244,76\ns_barrier\nMvaa 16,248,40\n"
"s_add_u32 s76, s90, 0x380080\nRa 0,196,2048\nMvaa 16,252,60\ns_and_b32 s76, s76, 0x3fffff\nRa 4,240,4096\nMvaa 48,248,44\ns_add_u32 m0, s63, 0x0\n"
"Ra 8,198,2048\nv_add_u32 v196, 0xffff0000, v196\nMvaa 48,252,64\nDL 242,40,76,0\nMvaa 80,248,48\nRa 20,197,2048\nv_add_u32 v198, 0xffff0000, v198\n"
"Mvaa 80,252,68\nRa 24,241,4096\nMvaa 112,248,52\nRa 28,199,2048\nv_add_u32 v197, 0xffff0000, v197\nMvaa 112,252,72\nDL 243,40,76,1024\n"
"Mvaa 144,248,56\nTa 184,202,0\nv_add_u32 v199, 0xffff0000, v199\nMvaa 144,252,76\nTa 186,202,512\nMvva 20,216,40\nTa 188,202,2048\nMvva 20,220,60\n"
"DL 244,40,76,2048\nMvva 52,216,44\nTa 190,202,2560\nMvva 52,220,64\nTa 192,202,8\nMvva 84,216,48\nTa 194,202,520\nMvva 84,220,68\nDL 245,40,76,3072\n"
"Mvva 116,216,52\nTa 196,202,2056\nMvva 116,220,72\nTa 198,202,2568\nMvva 148,216,56\nTa 200,202,256\nMvva 148,220,76\nTa 202,202,768\nMvva 24,224,40\n"
"Ta 204,202,2304\nMvva 24,228,60\nTa 206,202,2816\nMvva 56,224,44\nTa 208,202,264\nMvva 56,228,64\nTa 210,202,776\nMvva 88,224,48\nTa 212,202,2312\n"
"Mvva 88,228,68\nTa 214,202,2824\nMvva 120,224,52\nMvva 120,228,72\nv_add_u32 v202, 0xffff0000, v202\nMvva 152,224,56\nMvva 152,228,76\n"
"Mvva 28,232,40\nMvva 28,236,60\nMvva 60,232,44\nMvva 60,236,64\nMvva 92,232,48\nMvva 92,236,68\nMvva 124,232,52\nMvva 124,236,72\nMvva 156,232,56\n"
"s_waitcnt vmcnt(11) lgkmcnt(0)\nMvva 156,236,76\ns_barrier\nMvaa 0,184,0\ns_add_u32 s75, s97, 0x2000\nRa 52,196,34816\n"
".Lfa_pd11329:\ns_cbranch_execz .Lfa_wr6\nv_add_u32 v240, 0xfffe0000, v240\nMvaa 0,188,20\ns_and_b32 s75, s75, 0x1ffff\nRa 56,198,34816\n"
"v_add_u32 v241, 0xfffe0000, v241\nMvaa 32,184,4\ns_add_u32 m0, s72, 0x20000\nRa 72,197,34816\nv_add_u32 v196, 0xffff0000, v196\nMvaa 32,188,24\n"
"DtL 210,44,75,0\nMvaa 64,184,8\nRa 76,199,34816\nv_add_u32 v198, 0xffff0000, v198\nMvaa 64,188,28\nTa 216,202,32768\n"
"v_add_u32 v197, 0xffff0000, v197\nMvaa 96,184,12\nTa 218,202,33280\nv_add_u32 v199, 0xffff0000, v199\nMvaa 96,188,32\nDtL 211,44,75,1024\n"
"Mvaa 128,184,16\nTa 220,202,34816\nMvaa 128,188,36\nTa 222,202,35328\nMvaa 4,192,0\nTa 224,202,32776\nMvaa 4,196,20\nDtL 212,44,75,2048\n"
"Mvaa 36,192,4\nTa 226,202,33288\nMvaa 36,196,24\nTa 228,202,34824\nMvaa 68,192,8\nTa 230,202,35336\nMvaa 68,196,28\nDtL 213,44,75,3072\n"
"Mvaa 100,192,12\nTa 232,202,33024\nMvaa 100,196,32\nTa 234,202,33536\nMvaa 132,192,16\nTa 236,202,35072\nMvaa 132,196,36\nTa 238,202,35584\n"
"Mvaa 8,200,0\nTa 240,202,33032\nMvaa 8,204,20\nTa 242,202,33544\nMvaa 40,200,4\nTa 244,202,35080\nMvaa 40,204,24\nTa 246,202,35592\nMvaa 72,200,8\n"
"Mvaa 72,204,28\nv_add_u32 v202, 0xffff0000, v202\nMvaa 104,200,12\nMvaa 104,204,32\nMvaa 136,200,16\nMvaa 136,204,36\nMvaa 12,208,0\nMvaa 12,212,20\n"
"Mvaa 44,208,4\nMvaa 44,212,24\nMvaa 76,208,8\nMvaa 76,212,28\nMvaa 108,208,12\nMvaa 108,212,32\nMvaa 140,208,16\ns_waitcnt vmcnt(8) lgkmcnt(0)\n"
"Mvaa 140,212,36\ns_barrier\nMvaa 16,216,0\nRa 40,196,34816\nMvaa 16,220,20\ns_add_u32 m0, s72, 0x22000\nRa 44,240,36864\nMvaa 48,216,4\n"
"DtL 214,48,75,0\nMvaa 48,220,24\nRa 48,198,34816\nMvaa 80,216,8\nRa 60,197,34816\nMvaa 80,220,28\nRa 64,241,36864\nMvaa 112,216,12\n"
"DtL 215,48,75,1024\nMvaa 112,220,32\nRa 68,199,34816\nMvaa 144,216,16\nTa 248,202,32768\nMvaa 144,220,36\nTa 250,202,33280\nMvaa 20,224,0\n"
"DtL 214,48,75,2048\nMvaa 20,228,20\nTa 252,202,34816\nMvaa 52,224,4\nTa 254,202,35328\nMvaa 52,228,24\nTv 216,202,32776\nMvaa 84,224,8\n"
"DtL 215,48,75,3072\nMvaa 84,228,28\nTv 218,202,33288\nMvaa 116,224,12\nTv 220,202,34824\nMvaa 116,228,32\nTv 222,202,35336\nMvaa 148,224,16\n"
"Tv 224,202,33024\nMvaa 148,228,36\nTv 226,202,33536\nMvaa 24,232,0\nTv 228,202,35072\nMvaa 24,236,20\nTv 230,202,35584\nMvaa 56,232,4\n"
"Tv 232,202,33032\nMvaa 56,236,24\nTv 234,202,33544\nMvaa 88,232,8\nTv 236,202,35080\nMvaa 88,236,28\nTv 238,202,35592\nMvaa 120,232,12\n"
"Mvaa 120,236,32\nMvaa 152,232,16\nMvaa 152,236,36\nMvaa 28,240,0\nMvaa 28,244,20\nMvaa 60,240,4\nMvaa 60,244,24\nMvaa 92,240,8\nMvaa 92,244,28\n"
"Mvaa 124,240,12\nMvaa 124,244,32\nMvaa 156,240,16\ns_waitcnt vmcnt(8) lgkmcnt(0)\nMvaa 156,244,36\ns_barrier\nMvaa 0,248,40\n"
"s_add_u32 s75, s97, 0x4000\nTa 184,202,0\nv_add_u32 v204, 0x10000, v204\nMvaa 0,252,60\ns_and_b32 s75, s75, 0x1ffff\nTa 186,202,512\n"
"v_add_u32 v205, 0x10000, v205\nMvaa 32,248,44\ns_add_u32 m0, s72, 0x18000\nTa 188,202,2048\nv_add_u32 v206, 0x10000, v206\nMvaa 32,252,64\n"
"DtL 210,44,75,0\nMvaa 64,248,48\nTa 190,202,2560\nv_add_u32 v207, 0x10000, v207\nMvaa 64,252,68\nTa 192,202,8\nv_add_u32 v208, 0x10000, v208\n"
"Mvaa 96,248,52\nTa 194,202,520\nv_add_u32 v209, 0x10000, v209\nMvaa 96,252,72\nDtL 211,44,75,1024\nMvaa 128,248,56\nTa 196,202,2056\n"
"v_accvgpr_write_b32 a160, 0\nMvaa 128,252,76\nTa 198,202,2568\nv_accvgpr_write_b32 a161, 0\nMvva 4,216,40\nTa 200,202,256\n"
"v_accvgpr_write_b32 a162, 0\nMvva 4,220,60\nDtL 212,44,75,2048\nMvva 36,216,44\nTa 202,202,768\nv_accvgpr_write_b32 a163, 0\nMvva 36,220,64\n"
"Ta 204,202,2304\nv_accvgpr_write_b32 a164, 0\nMvva 68,216,48\nTa 206,202,2816\nv_accvgpr_write_b32 a165, 0\nMvva 68,220,68\nDtL 213,44,75,3072\n"
"Mvva 100,216,52\nTa 208,202,264\nv_accvgpr_write_b32 a166, 0\nMvva 100,220,72\nTa 210,202,776\nv_accvgpr_write_b32 a167, 0\nMvva 132,216,56\n"
"Ta 212,202,2312\nv_accvgpr_write_b32 a168, 0\nMvva 132,220,76\nTa 214,202,2824\nv_accvgpr_write_b32 a169, 0\nMvva 8,224,40\n"
"v_accvgpr_write_b32 a170, 0\nv_accvgpr_write_b32 a171, 0\nMvva 8,228,60\nv_accvgpr_write_b32 a172, 0\nv_accvgpr_write_b32 a173, 0\nMvva 40,224,44\n"
"v_accvgpr_write_b32 a174, 0\nv_accvgpr_write_b32 a175, 0\nMvva 40,228,64\nv_accvgpr_write_b32 a176, 0\nv_accvgpr_write_b32 a177, 0\nMvva 72,224,48\n"
"v_accvgpr_write_b32 a178, 0\nv_accvgpr_write_b32 a179, 0\nMvva 72,228,68\nMvva 104,224,52\nv_mul_f32 v0, s80, v0\nv_mul_f32 v1, s80, v1\n"
"Mvva 104,228,72\nv_mul_f32 v2, s80, v2\nv_mul_f32 v3, s80, v3\nMvva 136,224,56\nv_mul_f32 v4, s80, v4\nv_mul_f32 v5, s80, v5\nMvva 136,228,76\n"
"v_mul_f32 v6, s80, v6\nv_mul_f32 v7, s80, v7\nMvva 12,232,40\nCV 0,0\nCV 1,2\nMvva 12,236,60\nCV 2,4\nCV 3,6\nMvva 44,232,44\n"
"v_mul_f32 v32, s80, v32\nv_mul_f32 v33, s80, v33\nMvva 44,236,64\nv_mul_f32 v34, s80, v34\nv_mul_f32 v35, s80, v35\nMvva 76,232,48\n"
"v_mul_f32 v36, s80, v36\nv_mul_f32 v37, s80, v37\nMvva 76,236,68\nv_mul_f32 v38, s80, v38\nv_mul_f32 v39, s80, v39\nMvva 108,232,52\nCV 32,32\n"
"CV 33,34\nMvva 108,236,72\nCV 34,36\nCV 35,38\nMvva 140,232,56\ns_waitcnt vmcnt(29) lgkmcnt(0)\nv_mul_f32 v64, s80, v64\nv_mul_f32 v65, s80, v65\n"
"Mvva 140,236,76\ns_barrier\nMvaa 16,184,40\nRv 180,253,0\nMvaa 16,188,60\ns_add_u32 m0, s72, 0x1a000\nRv 184,253,64\nMvaa 48,184,44\nDtL 214,48,75,0\n"
"Mvaa 48,188,64\nRv 188,253,128\nMvaa 80,184,48\nRv 192,253,192\nv_mul_f32 v66, s80, v66\nMvaa 80,188,68\nRa 216,204,0\nv_mul_f32 v67, s80, v67\n"
"Mvaa 112,184,52\nDtL 215,48,75,1024\nMvaa 112,188,72\nRa 220,205,0\nv_mul_f32 v68, s80, v68\nMvaa 144,184,56\nRa 224,206,0\nv_mul_f32 v69, s80, v69\n"
"Mvaa 144,188,76\nRa 228,207,0\nv_mul_f32 v70, s80, v70\nMvaa 20,192,40\nDtL 214,48,75,2048\nMvaa 20,196,60\nRa 232,204,4096\nv_mul_f32 v71, s80, v71\n"
"Mvaa 52,192,44\nRa 236,205,4096\nCV 64,64\nMvaa 52,196,64\nRa 240,206,4096\nv_add_u32 v204, 0x10000, v204\nMvaa 84,192,48\nDtL 215,48,75,3072\n"
"Mvaa 84,196,68\nRa 244,207,4096\nv_add_u32 v205, 0x10000, v205\nMvaa 116,192,52\nTa 248,208,8192\nv_add_u32 v206, 0x10000, v206\nMvaa 116,196,72\n"
"Ta 250,208,12288\nv_add_u32 v207, 0x10000, v207\nMvaa 148,192,56\nTa 252,209,8192\nCV 65,66\nMvaa 148,196,76\nTa 254,209,12288\nCV 66,68\n"
"Mvaa 24,200,40\nTv 216,208,8448\nCV 67,70\nMvaa 24,204,60\nTv 218,208,12544\nv_mul_f32 v96, s80, v96\nMvaa 56,200,44\nTv 220,209,8448\n"
"v_mul_f32 v97, s80, v97\nMvaa 56,204,64\nTv 222,209,12544\nv_mul_f32 v98, s80, v98\nMvaa 88,200,48\nTv 224,208,8704\nv_mul_f32 v99, s80, v99\n"
"Mvaa 88,204,68\nTv 226,208,12800\nv_mul_f32 v100, s80, v100\nMvaa 120,200,52\nTv 228,209,8704\nv_mul_f32 v101, s80, v101\nMvaa 120,204,72\n"
"Tv 230,209,12800\nv_mul_f32 v102, s80, v102\nMvaa 152,200,56\nTv 232,208,8960\nv_mul_f32 v103, s80, v103\nMvaa 152,204,76\nTv 234,208,13056\n"
"CV 96,96\nMvaa 28,208,40\nTv 236,209,8960\nCV 97,98\nMvaa 28,212,60\nTv 238,209,13056\nv_add_u32 v208, 0x10000, v208\nMvaa 60,208,44\nCV 98,100\n"
"CV 99,102\nMvaa 60,212,64\nv_add_u32 v209, 0x10000, v209\nv_mul_f32 v128, s80, v128\nMvaa 92,208,48\nv_mul_f32 v129, s80, v129\n"
"v_mul_f32 v130, s80, v130\nMvaa 92,212,68\nv_mul_f32 v131, s80, v131\nv_mul_f32 v132, s80, v132\nMvaa 124,208,52\nv_mul_f32 v133, s80, v133\n"
"v_mul_f32 v134, s80, v134\nMvaa 124,212,72\nv_mul_f32 v135, s80, v135\nCV 128,128\nMvaa 156,208,56\nCV 129,130\nCV 130,132\nMvaa 156,212,76\n"
"s_waitcnt lgkmcnt(0)\nCV 131,134\nv_xor_b32 v180, s81, v180\nv_xor_b32 v181, s81, v181\nv_xor_b32 v182, s81, v182\nv_xor_b32 v183, s81, v183\n"
"v_xor_b32 v184, s81, v184\nv_xor_b32 v185, s81, v185\nv_xor_b32 v186, s81, v186\nv_xor_b32 v187, s81, v187\nv_xor_b32 v188, s81, v188\n"
"v_xor_b32 v189, s81, v189\nv_xor_b32 v190, s81, v190\nv_xor_b32 v191, s81, v191\nv_xor_b32 v192, s81, v192\nv_xor_b32 v193, s81, v193\n"
"v_xor_b32 v194, s81, v194\nv_xor_b32 v195, s81, v195\ns_nop 7\nPS 8,80\nPS 10,80\nPS 12,80\nPS 14,80\nPS 16,80\nPS 18,80\nPS 20,80\nPS 22,80\n"
"PS 24,80\nPS 26,80\nPS 28,80\nPS 30,80\nPS 40,80\nPS 42,80\nPS 44,80\nPS 46,80\nPS 48,80\nPS 50,80\nPS 52,80\nPS 54,80\nPS 56,80\nPS 58,80\nPS 60,80\n"
"PS 62,80\nPS 72,80\nPS 74,80\nPS 76,80\nPS 78,80\nPS 80,80\nPS 82,80\nPS 84,80\nPS 86,80\nPS 88,80\nPS 90,80\nPS 92,80\nPS 94,80\nPS 104,80\n"
"PS 106,80\nPS 108,80\nPS 110,80\nPS 112,80\nPS 114,80\nPS 116,80\nPS 118,80\nPS 120,80\nPS 122,80\nPS 124,80\nPS 126,80\nPS 136,80\nPS 138,80\n"
"PS 140,80\nPS 142,80\nPS 144,80\nPS 146,80\nPS 148,80\nPS 150,80\nPS 152,80\nPS 154,80\nPS 156,80\nPS 158,80\nCV 8,8\nCV 9,10\nCV 10,12\nCV 11,14\n"
"CV 16,16\nCV 17,18\nCV 18,20\nCV 19,22\nCV 24,24\nCV 25,26\nCV 26,28\nCV 27,30\nAW 0,0\nAW 1,1\nAW 2,2\nAW 3,3\nAW 4,8\nAW 5,9\nAW 6,10\nAW 7,11\n"
"AW 8,16\nAW 9,17\nAW 10,18\nAW 11,19\nAW 12,24\nAW 13,25\nAW 14,26\nAW 15,27\nCV 40,40\nCV 41,42\nZvva 160,180,0\nCV 42,44\nCV 43,46\nCV 48,48\n"
"CV 49,50\nCV 50,52\nCV 51,54\nCV 56,56\nMvva 160,184,4\nCV 57,58\nCV 58,60\nCV 59,62\nAW 16,32\nAW 17,33\nAW 18,34\nAW 19,35\nMvva 160,188,8\n"
"AW 20,40\nAW 21,41\nAW 22,42\nAW 23,43\nAW 24,48\nAW 25,49\nAW 26,50\nMvva 160,192,12\nAW 27,51\nAW 28,56\nAW 29,57\nAW 30,58\nAW 31,59\nCV 72,72\n"
"CV 73,74\nZvva 164,180,16\nCV 74,76\nCV 75,78\nCV 80,80\nCV 81,82\nCV 82,84\nCV 83,86\nCV 88,88\nMvva 164,184,20\nCV 89,90\nCV 90,92\nCV 91,94\n"
"AW 32,64\nAW 33,65\nAW 34,66\nAW 35,67\nMvva 164,188,24\nAW 36,72\nAW 37,73\nAW 38,74\nAW 39,75\nAW 40,80\nAW 41,81\nAW 42,82\nMvva 164,192,28\n"
"AW 43,83\nAW 44,88\nAW 45,89\nAW 46,90\nAW 47,91\nCV 104,104\nCV 105,106\nZvva 168,180,32\n.Lfa_pd11875:\ns_cbranch_execz .Lfa_wr6\nCV 106,108\n"
"CV 107,110\nCV 112,112\nCV 113,114\nCV 114,116\nCV 115,118\nCV 120,120\nMvva 168,184,36\nCV 121,122\nCV 122,124\nCV 123,126\nAW 48,96\nAW 49,97\n"
"AW 50,98\nAW 51,99\nMvva 168,188,40\nAW 52,104\nAW 53,105\nAW 54,106\nAW 55,107\nAW 56,112\nAW 57,113\nAW 58,114\nMvva 168,192,44\nAW 59,115\n"
"AW 60,120\nAW 61,121\nAW 62,122\nAW 63,123\nCV 136,136\nCV 137,138\nZvva 172,180,48\nCV 138,140\nCV 139,142\nCV 144,144\nCV 145,146\nCV 146,148\n"
"CV 147,150\nCV 152,152\nMvva 172,184,52\nCV 153,154\nCV 154,156\nCV 155,158\nAW 64,128\nAW 65,129\nAW 66,130\nAW 67,131\nMvva 172,188,56\nAW 68,136\n"
"AW 69,137\nAW 70,138\nAW 71,139\nAW 72,144\nAW 73,145\nAW 74,146\nMvva 172,192,60\nAW 75,147\nAW 76,152\nAW 77,153\nAW 78,154\nAW 79,155\ns_nop 1\n"
"Zvva 176,180,64\nMvva 176,184,68\nMvva 176,188,72\nMvva 176,192,76\nCvaa 180,216,0,160\nMvaa 180,220,4\nMvaa 180,224,8\nMvaa 180,228,12\n"
"Cvaa 184,232,0,160\nMvaa 184,236,4\nMvaa 184,240,8\nMvaa 184,244,12\ns_nop 3\ns_memrealtime s[98:99]\ns_waitcnt vmcnt(8) lgkmcnt(0)\ns_barrier\n"
"s_mov_b32 s93, s98\nCvaa 188,216,16,164\ns_add_u32 s75, s97, 0x6000\nEX 180\nMvaa 188,220,20\ns_and_b32 s75, s75, 0x1ffff\nEX 181\nMvaa 188,224,24\n"
"s_add_u32 m0, s72, 0x8000\nEX 182\nCV 196,180\nMvaa 188,228,28\ns_waitcnt vmcnt(8)\nEX 183\nCvaa 192,232,16,164\nEX 184\nCV 197,182\nMvaa 192,236,20\n"
"EX 185\nMvaa 192,240,24\nEX 186\nCV 198,184\nMvaa 192,244,28\nEX 187\ns_nop 0\nCV 199,186\nCvaa 180,216,32,168\nEX 188\nMvaa 180,220,36\n"
"DtL 210,44,75,0\nMvaa 180,224,40\nEX 189\nMvaa 180,228,44\nRa 184,204,0\nCV 200,188\nCvaa 184,232,32,168\nEX 190\nMvaa 184,236,36\n"
"DtL 211,44,75,1024\nMvaa 184,240,40\nEX 191\nMvaa 184,244,44\nRa 188,205,0\nCV 201,190\nZvav 0,248,196\ns_waitcnt vmcnt(8)\nEX 192\nZvav 4,252,196\n"
"DtL 212,44,75,2048\nZvvv 8,216,196\nEX 193\nZvvv 12,220,196\nRa 192,206,0\nCV 202,192\nZvvv 16,224,196\nEX 194\nZvvv 20,228,196\nDtL 213,44,75,3072\n"
"Zvvv 24,232,196\nEX 195\nZvvv 28,236,196\ns_add_u32 m0, s72, 0xa000\nRa 196,207,0\nCV 203,194\nCvaa 188,216,48,172\ns_waitcnt vmcnt(8)\n"
"Ra 200,204,4096\nMvaa 188,220,52\nEX 180\nMvaa 188,224,56\nDtL 214,48,75,0\nMvaa 188,228,60\nEX 181\n"
"v_smfmac_f32_16x16x64_bf16 a[160:163], a[180:183], v[196:203], v251\nRa 204,205,4096\nCV 196,180\nCvaa 192,232,48,172\nEX 182\nMvaa 192,236,52\n"
"DtL 215,48,75,1024\nMvaa 192,240,56\nEX 183\nMvaa 192,244,60\nRa 208,206,4096\nCV 197,182\nZvav 32,248,200\ns_waitcnt vmcnt(8)\nEX 184\n"
"Zvav 36,252,200\nDtL 214,48,75,2048\nZvvv 40,216,200\nEX 185\nZvvv 44,220,200\nRa 212,207,4096\nCV 198,184\nZvvv 48,224,200\nEX 186\nZvvv 52,228,200\n"
"DtL 215,48,75,3072\nZvvv 56,232,200\ns_add_u32 s75, s97, 0x8000\nEX 187\nZvvv 60,236,200\ns_and_b32 s75, s75, 0x1ffff\nCV 199,186\n"
"v_add_u32 v204, 0xffff0000, v204\nCvaa 180,216,64,176\ns_add_u32 m0, s72, 0x0\nEX 188\nMvaa 180,220,68\ns_waitcnt vmcnt(8)\nTa 216,208,8192\n"
"v_add_u32 v205, 0xffff0000, v205\nMvaa 180,224,72\nEX 189\nMvaa 180,228,76\nTa 218,208,12288\nCV 200,188\nCvaa 184,232,64,176\nEX 190\n"
"Mvaa 184,236,68\nDtL 210,44,75,0\nMvaa 184,240,72\nEX 191\nMvaa 184,244,76\nTa 220,209,8192\nCV 201,190\nZvav 64,248,196\nEX 192\nZvav 68,252,196\n"
"DtL 211,44,75,1024\nZvvv 72,216,196\nEX 193\nZvvv 76,220,196\nTa 222,209,12288\nCV 202,192\nZvvv 80,224,196\ns_waitcnt vmcnt(8)\nEX 194\n"
"Zvvv 84,228,196\nDtL 212,44,75,2048\nZvvv 88,232,196\nEX 195\nZvvv 92,236,196\ns_waitcnt lgkmcnt(1)\nTa 224,208,8448\nCV 203,194\nCvaa 188,184,0,160\n"
"Ta 226,208,12544\nv_add_u32 v206, 0xffff0000, v206\nMvaa 188,188,4\nEX 180\nMvaa 188,192,8\nDtL 213,44,75,3072\nMvaa 188,196,12\nEX 181\n"
"v_smfmac_f32_16x16x64_bf16 a[164:167], a[180:183], v[196:203], v251\nTa 228,209,8448\nCV 196,180\nCvaa 192,200,0,160\nEX 182\nMvaa 192,204,4\n"
"Ta 230,209,12544\nv_add_u32 v207, 0xffff0000, v207\nMvaa 192,208,8\nEX 183\nMvaa 192,212,12\nTa 232,208,8704\nCV 197,182\nZvav 96,248,200\nEX 184\n"
"Zvav 100,252,200\nTa 234,208,12800\nZvvv 104,216,200\nEX 185\nZvvv 108,220,200\nTa 236,209,8704\nCV 198,184\nZvvv 112,224,200\nEX 186\n"
"Zvvv 116,228,200\nTa 238,209,12800\nZvvv 120,232,200\nEX 187\nZvvv 124,236,200\nTa 240,208,8960\nCV 199,186\nCvaa 180,184,16,164\nEX 188\n"
"Mvaa 180,188,20\nTa 242,208,13056\nMvaa 180,192,24\nEX 189\nMvaa 180,196,28\ns_memrealtime s[98:99]\nTa 244,209,8960\nCV 200,188\n"
"Cvaa 184,200,16,164\nEX 190\nMvaa 184,204,20\nTa 246,209,13056\nv_add_u32 v208, 0xffff0000, v208\nMvaa 184,208,24\nEX 191\nMvaa 184,212,28\n"
"CV 201,190\nv_add_u32 v209, 0xffff0000, v209\nZvav 128,248,196\ns_waitcnt vmcnt(12) lgkmcnt(0)\nEX 192\nZvav 132,252,196\ns_barrier\n"
"s_mov_b64 exec, 0\ns_cmp_eq_u32 s60, 0\ns_cbranch_scc1 .Lfa_w7_0\ns_cmp_eq_u32 s60, 1\ns_cbranch_scc1 .Lfa_w7_1\ns_cmp_eq_u32 s60, 2\ns_cbranch_scc1 .Lfa_w7_2\ns_branch .Lfa_w7_3\n.Lfa_wr7:\ns_mov_b64 exec, -1\ns_nop 4\n"
"s_sub_u32 s95, s98, s93\ns_mov_b32 s93, s98\ns_sub_u32 s95, 92, s95\ns_cbranch_scc1 .Lfa_t44b\ns_add_u32 s93, s93, s95\n.Lfa_t44a:\ns_sub_u32 s95, s95, 4\ns_cbranch_scc1 .Lfa_t44b\ns_sleep 1\ns_branch .Lfa_t44a\n.Lfa_t44b:\n"
"Zvvv 136,216,196\ns_add_u32 m0, s72, 0x2000\nEX 193\nZvvv 140,220,196\ns_waitcnt vmcnt(8)\nRa 248,204,32768\nCV 202,192\nZvvv 144,224,196\nEX 194\n"
"Zvvv 148,228,196\nDtL 214,48,75,0\nZvvv 152,232,196\nEX 195\nZvvv 156,236,196\nRa 252,205,32768\nCV 203,194\nCvaa 188,184,32,168\nRv 216,206,32768\n"
"Mvaa 188,188,36\nEX 180\nMvaa 188,192,40\nRv 220,207,32768\nMvaa 188,196,44\nEX 181\n"
"v_smfmac_f32_16x16x64_bf16 a[168:171], a[180:183], v[196:203], v251\nRv 224,204,36864\nCV 196,180\nCvaa 192,200,32,168\nEX 182\nMvaa 192,204,36\n"
"DtL 215,48,75,1024\nMvaa 192,208,40\nEX 183\nMvaa 192,212,44\nRv 228,205,36864\nCV 197,182\nMvav 0,216,200\nEX 184\nMvav 4,220,200\nRv 232,206,36864\n"
"v_add_u32 v204, 0xffff0000, v204\nMvav 8,224,200\nEX 185\nMvav 12,228,200\nRv 236,207,36864\nCV 198,184\nMvav 16,232,200\ns_waitcnt vmcnt(8)\nEX 186\n"
"Mvav 20,236,200\nDtL 214,48,75,2048\nMvav 24,240,200\nEX 187\nMvav 28,244,200\nCV 199,186\nv_add_u32 v205, 0xffff0000, v205\nCvaa 180,184,48,172\n"
"EX 188\nMvaa 180,188,52\nv_add_u32 v206, 0xffff0000, v206\nv_add_u32 v207, 0xffff0000, v207\nMvaa 180,192,56\nEX 189\nMvaa 180,196,60\nCV 200,188\n"
"Cvaa 184,200,48,172\nEX 190\nMvaa 184,204,52\nDtL 215,48,75,3072\nMvaa 184,208,56\nEX 191\nMvaa 184,212,60\nCV 201,190\nMvav 32,216,196\nEX 192\n"
"Mvav 36,220,196\ns_add_u32 s75, s97, 0xa000\nMvav 40,224,196\ns_and_b32 s75, s75, 0x1ffff\nEX 193\nMvav 44,228,196\ns_add_u32 m0, s72, 0x10000\n"
"CV 202,192\nMvav 48,232,196\ns_waitcnt vmcnt(8)\nEX 194\nMvav 52,236,196\nDtL 210,44,75,0\nMvav 56,240,196\nEX 195\nMvav 60,244,196\nCV 203,194\n"
"Cvaa 188,184,64,176\nTa 184,208,40960\nMvaa 188,188,68\nEX 180\nMvaa 188,192,72\nTa 186,208,45056\nMvaa 188,196,76\nEX 181\n"
"v_smfmac_f32_16x16x64_bf16 a[172:175], a[180:183], v[196:203], v251\nTa 188,209,40960\nCV 196,180\nCvaa 192,200,64,176\nEX 182\nMvaa 192,204,68\n"
"DtL 211,44,75,1024\nMvaa 192,208,72\nEX 183\nMvaa 192,212,76\nTa 190,209,45056\nCV 197,182\nMvav 64,216,200\nEX 184\nMvav 68,220,200\n"
"Ta 192,208,41216\nMvav 72,224,200\nEX 185\nMvav 76,228,200\nTa 194,208,45312\nCV 198,184\nMvav 80,232,200\ns_waitcnt vmcnt(8)\nEX 186\n"
"Mvav 84,236,200\nDtL 212,44,75,2048\nMvav 88,240,200\nEX 187\nMvav 92,244,200\ns_waitcnt lgkmcnt(2)\nTa 196,209,41216\nCV 199,186\n"
"Cvaa 180,248,0,160\nEX 188\nMvaa 180,252,4\nTa 198,209,45312\nMvva 180,216,8\nEX 189\nMvva 180,220,12\nTa 200,208,41472\nCV 200,188\n"
"Cvva 184,224,0,160\nEX 190\nMvva 184,228,4\nDtL 213,44,75,3072\nMvva 184,232,8\nEX 191\nMvva 184,236,12\nTa 202,208,45568\nCV 201,190\n"
"Mvav 96,216,196\nEX 192\nMvav 100,220,196\nTa 204,209,41472\nMvav 104,224,196\nEX 193\nMvav 108,228,196\nTa 206,209,45568\nCV 202,192\n"
"Mvav 112,232,196\nEX 194\nMvav 116,236,196\nTa 208,208,41728\nMvav 120,240,196\nEX 195\nMvav 124,244,196\nTa 210,208,45824\nCV 203,194\n"
"Cvaa 188,248,16,164\nTa 212,209,41728\nMvaa 188,252,20\nEX 180\nMvva 188,216,24\nTa 214,209,45824\nv_add_u32 v208, 0xffff0000, v208\nMvva 188,220,28\n"
"EX 181\nv_smfmac_f32_16x16x64_bf16 a[176:179], a[180:183], v[196:203], v251\ns_memrealtime s[98:99]\nCV 196,180\nv_add_u32 v209, 0xffff0000, v209\n"
"Cvva 192,224,16,164\nEX 182\nMvva 192,228,20\nMvva 192,232,24\nEX 183\nMvva 192,236,28\nCV 197,182\nMvav 128,216,200\ns_waitcnt vmcnt(12) lgkmcnt(0)\n"
"EX 184\nMvav 132,220,200\ns_barrier\n"
"s_sub_u32 s95, s98, s93\ns_mov_b32 s93, s98\ns_sub_u32 s95, 92, s95\ns_cbranch_scc1 .Lfa_t45b\ns_add_u32 s93, s93, s95\n.Lfa_t45a:\ns_sub_u32 s95, s95, 4\ns_cbranch_scc1 .Lfa_t45b\ns_sleep 1\ns_branch .Lfa_t45a\n.Lfa_t45b:\n"
"Mvav 136,224,200\ns_add_u32 m0, s72, 0x12000\nEX 185\nMvav 140,228,200\ns_waitcnt vmcnt(8)\nRa 216,204,32768\nCV 198,184\nMvav 144,232,200\nEX 186\n"
"Mvav 148,236,200\nDtL 214,48,75,0\nMvav 152,240,200\nEX 187\nMvav 156,244,200\nRa 220,205,32768\nCV 199,186\nCvaa 180,248,32,168\nEX 188\n"
"Mvaa 180,252,36\nDtL 215,48,75,1024\nMvva 180,216,40\nEX 189\nMvva 180,220,44\nRa 224,206,32768\nCV 200,188\nCvva 184,224,32,168\ns_waitcnt vmcnt(8)\n"
"EX 190\nMvva 184,228,36\nDtL 214,48,75,2048\nMvva 184,232,40\nEX 191\nMvva 184,236,44\nRa 228,207,32768\nCV 201,190\nMvav 0,184,196\nEX 192\n"
"Mvav 4,188,196\nDtL 215,48,75,3072\nMvav 8,192,196\ns_add_u32 s75, s97, 0xc000\nEX 193\nMvav 12,196,196\ns_and_b32 s75, s75, 0x1ffff\n"
"Ra 232,204,36864\nCV 202,192\nMvav 16,200,196\ns_add_u32 m0, s72, 0x20000\nEX 194\nMvav 20,204,196\ns_waitcnt vmcnt(8)\nRa 236,205,36864\n"
"Mvav 24,208,196\nEX 195\nMvav 28,212,196\nRa 240,206,36864\nCV 203,194\nCvaa 188,248,48,172\nDtL 210,44,75,0\nMvaa 188,252,52\nEX 180\n"
"Mvva 188,216,56\nRa 244,207,36864\nMvva 188,220,60\nEX 181\nv_smfmac_f32_16x16x64_bf16 a[160:163], a[180:183], v[196:203], v251\nCV 196,180\n"
"Cvva 192,224,48,172\nEX 182\nMvva 192,228,52\nDtL 211,44,75,1024\nMvva 192,232,56\nEX 183\nMvva 192,236,60\nCV 197,182\nMvav 32,184,200\n"
"s_waitcnt vmcnt(8)\nEX 184\nMvav 36,188,200\n.Lfa_pd12441:\ns_cbranch_execz .Lfa_wr6\nDtL 212,44,75,2048\nMvav 40,192,200\nEX 185\nMvav 44,196,200\n"
"CV 198,184\nMvav 48,200,200\nEX 186\nMvav 52,204,200\nDtL 213,44,75,3072\nMvav 56,208,200\nEX 187\nMvav 60,212,200\ns_add_u32 m0, s72, 0x22000\n"
"CV 199,186\nCvaa 180,248,64,176\ns_waitcnt vmcnt(8)\nEX 188\nMvaa 180,252,68\nDtL 214,48,75,0\nMvva 180,216,72\nEX 189\nMvva 180,220,76\n"
"Ta 248,208,40960\nCV 200,188\nCvva 184,224,64,176\nEX 190\nMvva 184,228,68\nDtL 215,48,75,1024\nMvva 184,232,72\nEX 191\nMvva 184,236,76\n"
"Ta 250,208,45056\nCV 201,190\nMvav 64,184,196\ns_waitcnt vmcnt(8)\nEX 192\nMvav 68,188,196\nDtL 214,48,75,2048\nMvav 72,192,196\nEX 193\n"
"Mvav 76,196,196\nTa 252,209,40960\nCV 202,192\nMvav 80,200,196\nEX 194\nMvav 84,204,196\nDtL 215,48,75,3072\nMvav 88,208,196\nEX 195\n"
"Mvav 92,212,196\ns_waitcnt lgkmcnt(1)\nTa 254,209,45056\nCV 203,194\nCvaa 188,216,0,160\nTv 216,208,41216\nMvaa 188,220,4\nEX 180\nMvaa 188,224,8\n"
"buffer_load_dword v252, v210, s[68:71], s96 offen offset:0\nMvaa 188,228,12\nEX 181\n"
"v_smfmac_f32_16x16x64_bf16 a[164:167], a[180:183], v[196:203], v251\nTv 218,208,45312\nCV 196,180\nCvaa 192,232,0,160\nEX 182\nMvaa 192,236,4\n"
"buffer_load_dword v252, v210, s[68:71], s96 offen offset:1024\nMvaa 192,240,8\nEX 183\nMvaa 192,244,12\nTv 220,209,41216\nCV 197,182\n"
"Mvav 96,184,200\nEX 184\nMvav 100,188,200\nTv 222,209,45312\nMvav 104,192,200\nEX 185\nMvav 108,196,200\nTv 224,208,41472\nCV 198,184\n"
"Mvav 112,200,200\nEX 186\nMvav 116,204,200\nTv 226,208,45568\nMvav 120,208,200\nEX 187\nMvav 124,212,200\nTv 228,209,41472\nCV 199,186\n"
"Cvaa 180,216,16,164\nEX 188\nMvaa 180,220,20\nTv 230,209,45568\nMvaa 180,224,24\nEX 189\nMvaa 180,228,28\ns_memrealtime s[98:99]\nTv 232,208,41728\n"
"CV 200,188\nCvaa 184,232,16,164\nEX 190\nMvaa 184,236,20\nTv 234,208,45824\nMvaa 184,240,24\nEX 191\nMvaa 184,244,28\nTv 236,209,41728\nCV 201,190\n"
"Mvav 128,184,196\nEX 192\nMvav 132,188,196\nTv 238,209,45824\ns_waitcnt vmcnt(18) lgkmcnt(0)\ns_barrier\n"
"s_sub_u32 s95, s98, s93\ns_mov_b32 s93, s98\ns_sub_u32 s95, 92, s95\ns_cbranch_scc1 .Lfa_t46b\ns_add_u32 s93, s93, s95\n.Lfa_t46a:\ns_sub_u32 s95, s95, 4\ns_cbranch_scc1 .Lfa_t46b\ns_sleep 1\ns_branch .Lfa_t46a\n.Lfa_t46b:\n"
"Mvav 136,192,196\ns_add_u32 s75, s97, 0xe000\nEX 193\nMvav 140,196,196\ns_and_b32 s75, s75, 0x1ffff\nRa 184,204,0\nCV 202,192\nMvav 144,200,196\n"
"s_add_u32 m0, s72, 0x18000\nEX 194\nMvav 148,204,196\ns_waitcnt vmcnt(8)\nRa 188,205,0\nMvav 152,208,196\nEX 195\nMvav 156,212,196\nRa 192,206,0\n"
"CV 203,194\nCvaa 188,216,32,168\nDtL 210,44,75,0\nMvaa 188,220,36\nEX 180\nMvaa 188,224,40\nRa 196,207,0\nMvaa 188,228,44\nEX 181\n"
"v_smfmac_f32_16x16x64_bf16 a[168:171], a[180:183], v[196:203], v251\nRa 200,204,4096\nCV 196,180\nCvaa 192,232,32,168\nEX 182\nMvaa 192,236,36\n"
"Ra 204,205,4096\nv_add_u32 v204, 0x10000, v204\nMvaa 192,240,40\nEX 183\nMvaa 192,244,44\nRa 208,206,4096\nCV 197,182\nMvav 0,248,200\nEX 184\n"
"Mvav 4,252,200\nDtL 211,44,75,1024\nMvvv 8,216,200\nEX 185\nMvvv 12,220,200\nRa 212,207,4096\nCV 198,184\nMvvv 16,224,200\nEX 186\nMvvv 20,228,200\n"
"v_add_u32 v205, 0x10000, v205\nv_add_u32 v206, 0x10000, v206\nMvvv 24,232,200\nEX 187\nMvvv 28,236,200\nCV 199,186\nv_add_u32 v207, 0x10000, v207\n"
"Cvaa 180,216,48,172\ns_waitcnt vmcnt(8)\nEX 188\nMvaa 180,220,52\nDtL 212,44,75,2048\nMvaa 180,224,56\nEX 189\nMvaa 180,228,60\nCV 200,188\n"
"Cvaa 184,232,48,172\nEX 190\nMvaa 184,236,52\nMvaa 184,240,56\nEX 191\nMvaa 184,244,60\nCV 201,190\nMvav 32,248,196\nEX 192\nMvav 36,252,196\n"
"DtL 213,44,75,3072\nMvvv 40,216,196\nEX 193\nMvvv 44,220,196\nCV 202,192\nMvvv 48,224,196\nEX 194\nMvvv 52,228,196\nMvvv 56,232,196\nEX 195\n"
"Mvvv 60,236,196\ns_add_u32 m0, s72, 0x1a000\nCV 203,194\nCvaa 188,216,64,176\ns_waitcnt vmcnt(8)\nTa 216,208,8192\nMvaa 188,220,68\nEX 180\n"
"Mvaa 188,224,72\nDtL 214,48,75,0\nMvaa 188,228,76\nEX 181\nv_smfmac_f32_16x16x64_bf16 a[172:175], a[180:183], v[196:203], v251\nTa 218,208,12288\n"
"CV 196,180\nCvaa 192,232,64,176\nEX 182\nMvaa 192,236,68\nTa 220,209,8192\nMvaa 192,240,72\nEX 183\nMvaa 192,244,76\nTa 222,209,12288\nCV 197,182\n"
"Mvav 64,248,200\nEX 184\nMvav 68,252,200\nDtL 215,48,75,1024\nMvvv 72,216,200\nEX 185\nMvvv 76,220,200\nTa 224,208,8448\nCV 198,184\nMvvv 80,224,200\n"
"EX 186\nMvvv 84,228,200\nTa 226,208,12544\nMvvv 88,232,200\nEX 187\nMvvv 92,236,200\ns_waitcnt lgkmcnt(2)\nTa 228,209,8448\nCV 199,186\n"
"Cvaa 180,184,0,160\ns_waitcnt vmcnt(8)\nEX 188\nMvaa 180,188,4\nDtL 214,48,75,2048\nMvaa 180,192,8\nEX 189\nMvaa 180,196,12\nTa 230,209,12544\n"
"CV 200,188\nCvaa 184,200,0,160\nEX 190\nMvaa 184,204,4\nTa 232,208,8704\nMvaa 184,208,8\nEX 191\nMvaa 184,212,12\nTa 234,208,12800\nCV 201,190\n"
"Mvav 96,248,196\nEX 192\nMvav 100,252,196\nDtL 215,48,75,3072\nMvvv 104,216,196\nEX 193\nMvvv 108,220,196\nTa 236,209,8704\nCV 202,192\n"
"Mvvv 112,224,196\nEX 194\nMvvv 116,228,196\nTa 238,209,12800\nMvvv 120,232,196\nEX 195\nMvvv 124,236,196\nTa 240,208,8960\nCV 203,194\n"
"Cvaa 188,184,16,164\nTa 242,208,13056\nMvaa 188,188,20\nEX 180\nMvaa 188,192,24\nTa 244,209,8960\nv_add_u32 v208, 0x10000, v208\nMvaa 188,196,28\n"
"EX 181\nv_smfmac_f32_16x16x64_bf16 a[176:179], a[180:183], v[196:203], v251\ns_memrealtime s[98:99]\nTa 246,209,13056\nCV 196,180\n"
"Cvaa 192,200,16,164\nEX 182\nMvaa 192,204,20\nv_add_u32 v209, 0x10000, v209\nMvaa 192,208,24\nEX 183\nMvaa 192,212,28\nCV 197,182\nMvav 128,248,200\n"
"s_waitcnt vmcnt(18) lgkmcnt(0)\nEX 184\nMvav 132,252,200\ns_barrier\n"
"s_sub_u32 s95, s98, s93\ns_mov_b32 s93, s98\ns_sub_u32 s95, 92, s95\ns_cbranch_scc1 .Lfa_t47b\ns_add_u32 s93, s93, s95\n.Lfa_t47a:\ns_sub_u32 s95, s95, 4\ns_cbranch_scc1 .Lfa_t47b\ns_sleep 1\ns_branch .Lfa_t47a\n.Lfa_t47b:\n"
"Mvvv 136,216,200\ns_add_u32 s75, s97, 0x10000\nEX 185\nMvvv 140,220,200\ns_and_b32 s75, s75, 0x1ffff\nRa 248,204,0\nCV 198,184\nMvvv 144,224,200\n"
"s_add_u32 m0, s72, 0x8000\nEX 186\nMvvv 148,228,200\ns_waitcnt vmcnt(8)\nRa 252,205,0\nMvvv 152,232,200\nEX 187\nMvvv 156,236,200\nRv 216,206,0\n"
"CV 199,186\nCvaa 180,184,32,168\nEX 188\nMvaa 180,188,36\nDtL 210,44,75,0\nMvaa 180,192,40\nEX 189\nMvaa 180,196,44\nRv 220,207,0\nCV 200,188\n"
"Cvaa 184,200,32,168\nEX 190\nMvaa 184,204,36\nDtL 211,44,75,1024\nMvaa 184,208,40\nEX 191\nMvaa 184,212,44\nRv 224,204,4096\nCV 201,190\n"
"Mvav 0,216,196\ns_waitcnt vmcnt(8)\nEX 192\nMvav 4,220,196\nDtL 212,44,75,2048\nMvav 8,224,196\nEX 193\nMvav 12,228,196\nRv 228,205,4096\nCV 202,192\n"
"Mvav 16,232,196\nEX 194\nMvav 20,236,196\nDtL 213,44,75,3072\nMvav 24,240,196\nEX 195\nMvav 28,244,196\ns_add_u32 m0, s72, 0xa000\nRv 232,206,4096\n"
"CV 203,194\nCvaa 188,184,48,172\ns_waitcnt vmcnt(8)\nRv 236,207,4096\nv_add_u32 v204, 0x10000, v204\nMvaa 188,188,52\nEX 180\nMvaa 188,192,56\n"
"DtL 214,48,75,0\nMvaa 188,196,60\nEX 181\nv_smfmac_f32_16x16x64_bf16 a[160:163], a[180:183], v[196:203], v251\nCV 196,180\n"
"v_add_u32 v205, 0x10000, v205\nCvaa 192,200,48,172\nEX 182\nMvaa 192,204,52\nDtL 215,48,75,1024\nMvaa 192,208,56\nEX 183\nMvaa 192,212,60\n"
"CV 197,182\nv_add_u32 v206, 0x10000, v206\nMvav 32,216,200\ns_waitcnt vmcnt(8)\nEX 184\nMvav 36,220,200\nDtL 214,48,75,2048\nMvav 40,224,200\nEX 185\n"
"Mvav 44,228,200\nCV 198,184\nv_add_u32 v207, 0x10000, v207\nMvav 48,232,200\nEX 186\nMvav 52,236,200\nDtL 215,48,75,3072\nMvav 56,240,200\n"
"s_add_u32 s75, s97, 0x12000\nEX 187\nMvav 60,244,200\ns_and_b32 s75, s75, 0x1ffff\nCV 199,186\nCvaa 180,184,64,176\ns_add_u32 m0, s72, 0x0\nEX 188\n"
"Mvaa 180,188,68\ns_waitcnt vmcnt(8)\nTa 184,208,8192\nMvaa 180,192,72\nEX 189\nMvaa 180,196,76\nTa 186,208,12288\nCV 200,188\nCvaa 184,200,64,176\n"
"EX 190\nMvaa 184,204,68\nDtL 210,44,75,0\nMvaa 184,208,72\nEX 191\nMvaa 184,212,76\nTa 188,209,8192\nCV 201,190\nMvav 64,216,196\nEX 192\n"
"Mvav 68,220,196\nDtL 211,44,75,1024\nMvav 72,224,196\nEX 193\nMvav 76,228,196\nTa 190,209,12288\nCV 202,192\nMvav 80,232,196\ns_waitcnt vmcnt(8)\n"
"EX 194\nMvav 84,236,196\nDtL 212,44,75,2048\nMvav 88,240,196\nEX 195\nMvav 92,244,196\ns_waitcnt lgkmcnt(1)\nTa 192,208,8448\nCV 203,194\n"
"Cvaa 188,248,0,160\nTa 194,208,12544\nMvaa 188,252,4\nEX 180\nMvva 188,216,8\nDtL 213,44,75,3072\nMvva 188,220,12\nEX 181\n"
"v_smfmac_f32_16x16x64_bf16 a[164:167], a[180:183], v[196:203], v251\nTa 196,209,8448\nCV 196,180\nCvva 192,224,0,160\nEX 182\nMvva 192,228,4\n"
"Ta 198,209,12544\nMvva 192,232,8\nEX 183\nMvva 192,236,12\nTa 200,208,8704\nCV 197,182\nMvav 96,216,200\nEX 184\nMvav 100,220,200\nTa 202,208,12800\n"
"Mvav 104,224,200\nEX 185\nMvav 108,228,200\nTa 204,209,8704\nCV 198,184\nMvav 112,232,200\nEX 186\nMvav 116,236,200\nTa 206,209,12800\n"
"Mvav 120,240,200\nEX 187\nMvav 124,244,200\nTa 208,208,8960\nCV 199,186\nCvaa 180,248,16,164\nEX 188\nMvaa 180,252,20\nTa 210,208,13056\n"
"Mvva 180,216,24\nEX 189\nMvva 180,220,28\ns_memrealtime s[98:99]\nTa 212,209,8960\nCV 200,188\nCvva 184,224,16,164\nEX 190\nMvva 184,228,20\n"
"Ta 214,209,13056\nv_add_u32 v208, 0x10000, v208\nMvva 184,232,24\nEX 191\nMvva 184,236,28\nCV 201,190\nv_add_u32 v209, 0x10000, v209\n"
"Mvav 128,216,196\ns_waitcnt vmcnt(22) lgkmcnt(0)\nEX 192\nMvav 132,220,196\ns_barrier\n"
"s_sub_u32 s95, s98, s93\ns_mov_b32 s93, s98\ns_sub_u32 s95, 92, s95\ns_cbranch_scc1 .Lfa_t48b\ns_add_u32 s93, s93, s95\n.Lfa_t48a:\ns_sub_u32 s95, s95, 4\ns_cbranch_scc1 .Lfa_t48b\ns_sleep 1\ns_branch .Lfa_t48a\n.Lfa_t48b:\n"
"Mvav 136,224,196\ns_add_u32 m0, s72, 0x2000\nEX 193\nMvav 140,228,196\ns_waitcnt vmcnt(8)\nRa 216,204,0\nCV 202,192\nMvav 144,232,196\nEX 194\n"
"Mvav 148,236,196\nDtL 214,48,75,0\nMvav 152,240,196\nEX 195\nMvav 156,244,196\nRa 220,205,0\nCV 203,194\nCvaa 188,248,32,168\nRa 224,206,0\n"
"Mvaa 188,252,36\nEX 180\nMvva 188,216,40\nRa 228,207,0\nMvva 188,220,44\nEX 181\nv_smfmac_f32_16x16x64_bf16 a[168:171], a[180:183], v[196:203], v251\n"
"Ra 232,204,4096\nCV 196,180\nCvva 192,224,32,168\nEX 182\nMvva 192,228,36\nDtL 215,48,75,1024\nMvva 192,232,40\nEX 183\nMvva 192,236,44\n"
"Ra 236,205,4096\nCV 197,182\nMvav 0,184,200\nEX 184\nMvav 4,188,200\nRa 240,206,4096\nv_add_u32 v204, 0xffff0000, v204\nMvav 8,192,200\nEX 185\n"
"Mvav 12,196,200\nRa 244,207,4096\nCV 198,184\nMvav 16,200,200\ns_waitcnt vmcnt(8)\nEX 186\nMvav 20,204,200\nDtL 214,48,75,2048\nMvav 24,208,200\n"
"EX 187\nMvav 28,212,200\n.Lfa_pd13009:\ns_cbranch_execz .Lfa_wr7\nCV 199,186\nv_add_u32 v205, 0xffff0000, v205\nCvaa 180,248,48,172\nEX 188\n"
"Mvaa 180,252,52\nv_add_u32 v206, 0xffff0000, v206\nv_add_u32 v207, 0xffff0000, v207\nMvva 180,216,56\nEX 189\nMvva 180,220,60\nCV 200,188\n"
"Cvva 184,224,48,172\nEX 190\nMvva 184,228,52\nDtL 215,48,75,3072\nMvva 184,232,56\nEX 191\nMvva 184,236,60\nCV 201,190\nMvav 32,184,196\nEX 192\n"
"Mvav 36,188,196\ns_add_u32 s75, s97, 0x14000\nMvav 40,192,196\ns_and_b32 s75, s75, 0x1ffff\nEX 193\nMvav 44,196,196\ns_add_u32 m0, s72, 0x10000\n"
"CV 202,192\nMvav 48,200,196\ns_waitcnt vmcnt(8)\nEX 194\nMvav 52,204,196\nDtL 210,44,75,0\nMvav 56,208,196\nEX 195\nMvav 60,212,196\nCV 203,194\n"
"Cvaa 188,248,64,176\nTa 248,208,8192\nMvaa 188,252,68\nEX 180\nMvva 188,216,72\nTa 250,208,12288\nMvva 188,220,76\nEX 181\n"
"v_smfmac_f32_16x16x64_bf16 a[172:175], a[180:183], v[196:203], v251\nTa 252,209,8192\nCV 196,180\nCvva 192,224,64,176\nEX 182\nMvva 192,228,68\n"
"DtL 211,44,75,1024\nMvva 192,232,72\nEX 183\nMvva 192,236,76\nTa 254,209,12288\nCV 197,182\nMvav 64,184,200\nEX 184\nMvav 68,188,200\n"
"Tv 216,208,8448\nMvav 72,192,200\nEX 185\nMvav 76,196,200\nTv 218,208,12544\nCV 198,184\nMvav 80,200,200\ns_waitcnt vmcnt(8)\nEX 186\n"
"Mvav 84,204,200\nDtL 212,44,75,2048\nMvav 88,208,200\nEX 187\nMvav 92,212,200\ns_waitcnt lgkmcnt(2)\nTv 220,209,8448\nCV 199,186\nCvaa 180,216,0,160\n"
"EX 188\nMvaa 180,220,4\nTv 222,209,12544\nMvaa 180,224,8\nEX 189\nMvaa 180,228,12\nTv 224,208,8704\nCV 200,188\nCvaa 184,232,0,160\nEX 190\n"
"Mvaa 184,236,4\nDtL 213,44,75,3072\nMvaa 184,240,8\nEX 191\nMvaa 184,244,12\nTv 226,208,12800\nCV 201,190\nMvav 96,184,196\nEX 192\nMvav 100,188,196\n"
"Tv 228,209,8704\nMvav 104,192,196\nEX 193\nMvav 108,196,196\nTv 230,209,12800\nCV 202,192\nMvav 112,200,196\nEX 194\nMvav 116,204,196\n"
"Tv 232,208,8960\nMvav 120,208,196\nEX 195\nMvav 124,212,196\nTv 234,208,13056\nCV 203,194\nCvaa 188,216,16,164\nTv 236,209,8960\nMvaa 188,220,20\n"
"EX 180\nMvaa 188,224,24\nTv 238,209,13056\nv_add_u32 v208, 0xffff0000, v208\nMvaa 188,228,28\nEX 181\n"
"v_smfmac_f32_16x16x64_bf16 a[176:179], a[180:183], v[196:203], v251\ns_memrealtime s[98:99]\nCV 196,180\nv_add_u32 v209, 0xffff0000, v209\n"
"Cvaa 192,232,16,164\nEX 182\nMvaa 192,236,20\nMvaa 192,240,24\nEX 183\nMvaa 192,244,28\nCV 197,182\nMvav 128,184,200\ns_waitcnt vmcnt(20) lgkmcnt(0)\n"
"EX 184\nMvav 132,188,200\ns_barrier\n"
"s_sub_u32 s95, s98, s93\ns_mov_b32 s93, s98\ns_sub_u32 s95, 92, s95\ns_cbranch_scc1 .Lfa_t49b\ns_add_u32 s93, s93, s95\n.Lfa_t49a:\ns_sub_u32 s95, s95, 4\ns_cbranch_scc1 .Lfa_t49b\ns_sleep 1\ns_branch .Lfa_t49a\n.Lfa_t49b:\n"
"Mvav 136,192,200\ns_add_u32 m0, s72, 0x12000\nEX 185\nMvav 140,196,200\ns_waitcnt vmcnt(8)\nRa 184,204,32768\nCV 198,184\nMvav 144,200,200\nEX 186\n"
"Mvav 148,204,200\nDtL 214,48,75,0\nMvav 152,208,200\nEX 187\nMvav 156,212,200\nRa 188,205,32768\nCV 199,186\nCvaa 180,216,32,168\nEX 188\n"
"Mvaa 180,220,36\nRa 192,206,32768\nMvaa 180,224,40\nEX 189\nMvaa 180,228,44\nRa 196,207,32768\nCV 200,188\nCvaa 184,232,32,168\nEX 190\n"
"Mvaa 184,236,36\nDtL 215,48,75,1024\nMvaa 184,240,40\nEX 191\nMvaa 184,244,44\nRa 200,204,36864\nCV 201,190\nMvav 0,248,196\nEX 192\nMvav 4,252,196\n"
"Ra 204,205,36864\nv_add_u32 v204, 0xffff0000, v204\nMvvv 8,216,196\nEX 193\nMvvv 12,220,196\nRa 208,206,36864\nCV 202,192\nMvvv 16,224,196\n"
"s_waitcnt vmcnt(8)\nEX 194\nMvvv 20,228,196\nDtL 214,48,75,2048\nMvvv 24,232,196\nEX 195\nMvvv 28,236,196\nRa 212,207,36864\nCV 203,194\n"
"Cvaa 188,216,48,172\nv_add_u32 v205, 0xffff0000, v205\nv_add_u32 v206, 0xffff0000, v206\nMvaa 188,220,52\nEX 180\nMvaa 188,224,56\n"
"v_add_u32 v207, 0xffff0000, v207\nMvaa 188,228,60\nEX 181\nv_smfmac_f32_16x16x64_bf16 a[160:163], a[180:183], v[196:203], v251\nCV 196,180\n"
"Cvaa 192,232,48,172\nEX 182\nMvaa 192,236,52\nDtL 215,48,75,3072\nMvaa 192,240,56\nEX 183\nMvaa 192,244,60\nCV 197,182\nMvav 32,248,200\nEX 184\n"
"Mvav 36,252,200\ns_add_u32 s75, s97, 0x16000\nMvvv 40,216,200\ns_and_b32 s75, s75, 0x1ffff\nEX 185\nMvvv 44,220,200\ns_add_u32 m0, s72, 0x20000\n"
"CV 198,184\nMvvv 48,224,200\ns_waitcnt vmcnt(8)\nEX 186\nMvvv 52,228,200\nDtL 210,44,75,0\nMvvv 56,232,200\nEX 187\nMvvv 60,236,200\nCV 199,186\n"
"Cvaa 180,216,64,176\nEX 188\nMvaa 180,220,68\nTa 216,208,40960\nMvaa 180,224,72\nEX 189\nMvaa 180,228,76\nTa 218,208,45056\nCV 200,188\n"
"Cvaa 184,232,64,176\nEX 190\nMvaa 184,236,68\nDtL 211,44,75,1024\nMvaa 184,240,72\nEX 191\nMvaa 184,244,76\nTa 220,209,40960\nCV 201,190\n"
"Mvav 64,248,196\nEX 192\nMvav 68,252,196\nTa 222,209,45056\nMvvv 72,216,196\nEX 193\nMvvv 76,220,196\nTa 224,208,41216\nCV 202,192\nMvvv 80,224,196\n"
"s_waitcnt vmcnt(8)\nEX 194\nMvvv 84,228,196\nDtL 212,44,75,2048\nMvvv 88,232,196\nEX 195\nMvvv 92,236,196\ns_waitcnt lgkmcnt(2)\nTa 226,208,45312\n"
"CV 203,194\nCvaa 188,184,0,160\nTa 228,209,41216\nMvaa 188,188,4\nEX 180\nMvaa 188,192,8\nTa 230,209,45312\nMvaa 188,196,12\nEX 181\n"
"v_smfmac_f32_16x16x64_bf16 a[164:167], a[180:183], v[196:203], v251\nTa 232,208,41472\nCV 196,180\nCvaa 192,200,0,160\nEX 182\nMvaa 192,204,4\n"
"DtL 213,44,75,3072\nMvaa 192,208,8\nEX 183\nMvaa 192,212,12\nTa 234,208,45568\nCV 197,182\nMvav 96,248,200\nEX 184\nMvav 100,252,200\n"
"Ta 236,209,41472\nMvvv 104,216,200\nEX 185\nMvvv 108,220,200\nTa 238,209,45568\nCV 198,184\nMvvv 112,224,200\nEX 186\nMvvv 116,228,200\n"
"Ta 240,208,41728\nMvvv 120,232,200\nEX 187\nMvvv 124,236,200\nTa 242,208,45824\nCV 199,186\nCvaa 180,184,16,164\nEX 188\nMvaa 180,188,20\n"
"Ta 244,209,41728\nv_add_u32 v208, 0xffff0000, v208\nMvaa 180,192,24\nEX 189\nMvaa 180,196,28\ns_memrealtime s[98:99]\nTa 246,209,45824\nCV 200,188\n"
"Cvaa 184,200,16,164\nEX 190\nMvaa 184,204,20\nv_add_u32 v209, 0xffff0000, v209\nMvaa 184,208,24\nEX 191\nMvaa 184,212,28\nCV 201,190\n"
"Mvav 128,248,196\ns_waitcnt vmcnt(20) lgkmcnt(0)\nEX 192\nMvav 132,252,196\ns_barrier\n"
"s_mov_b64 exec, 0\ns_cmp_eq_u32 s60, 0\ns_cbranch_scc1 .Lfa_w8_0\ns_cmp_eq_u32 s60, 1\ns_cbranch_scc1 .Lfa_w8_1\ns_cmp_eq_u32 s60, 2\ns_cbranch_scc1 .Lfa_w8_2\ns_branch .Lfa_w8_3\n.Lfa_wr8:\ns_mov_b64 exec, -1\ns_nop 4\n"
"s_sub_u32 s95, s98, s93\ns_mov_b32 s93, s98\ns_sub_u32 s95, 92, s95\ns_cbranch_scc1 .Lfa_t50b\ns_add_u32 s93, s93, s95\n.Lfa_t50a:\ns_sub_u32 s95, s95, 4\ns_cbranch_scc1 .Lfa_t50b\ns_sleep 1\ns_branch .Lfa_t50a\n.Lfa_t50b:\n"
"Mvvv 136,216,196\ns_add_u32 m0, s72, 0x22000\nEX 193\nMvvv 140,220,196\ns_waitcnt vmcnt(8)\nRa 248,204,32768\nCV 202,192\nMvvv 144,224,196\nEX 194\n"
"Mvvv 148,228,196\nDtL 214,48,75,0\nMvvv 152,232,196\nEX 195\nMvvv 156,236,196\nRa 252,205,32768\nCV 203,194\nCvaa 188,184,32,168\nRv 216,206,32768\n"
"Mvaa 188,188,36\nEX 180\nMvaa 188,192,40\nRv 220,207,32768\nMvaa 188,196,44\nEX 181\n"
"v_smfmac_f32_16x16x64_bf16 a[168:171], a[180:183], v[196:203], v251\nRv 224,204,36864\nCV 196,180\nCvaa 192,200,32,168\nEX 182\nMvaa 192,204,36\n"
"DtL 215,48,75,1024\nMvaa 192,208,40\nEX 183\nMvaa 192,212,44\nRv 228,205,36864\nCV 197,182\nMvav 0,216,200\nEX 184\nMvav 4,220,200\nRv 232,206,36864\n"
"Mvav 8,224,200\nEX 185\nMvav 12,228,200\nRv 236,207,36864\nCV 198,184\nMvav 16,232,200\ns_waitcnt vmcnt(8)\nEX 186\nMvav 20,236,200\n"
"DtL 214,48,75,2048\nMvav 24,240,200\nEX 187\nMvav 28,244,200\nCV 199,186\nCvaa 180,184,48,172\nEX 188\nMvaa 180,188,52\nMvaa 180,192,56\nEX 189\n"
"Mvaa 180,196,60\nCV 200,188\nCvaa 184,200,48,172\nEX 190\nMvaa 184,204,52\nDtL 215,48,75,3072\nMvaa 184,208,56\nEX 191\nMvaa 184,212,60\nCV 201,190\n"
"Mvav 32,216,196\nEX 192\nMvav 36,220,196\ns_add_u32 s75, s97, 0x18000\nMvav 40,224,196\ns_and_b32 s75, s75, 0x1ffff\nEX 193\nMvav 44,228,196\n"
"s_add_u32 m0, s72, 0x18000\nCV 202,192\nMvav 48,232,196\ns_waitcnt vmcnt(8)\nEX 194\nMvav 52,236,196\nDtL 210,44,75,0\nMvav 56,240,196\nEX 195\n"
"Mvav 60,244,196\nCV 203,194\nCvaa 188,184,64,176\nTa 184,208,40960\nMvaa 188,188,68\nEX 180\nMvaa 188,192,72\nTa 186,208,45056\nMvaa 188,196,76\n"
"EX 181\nv_smfmac_f32_16x16x64_bf16 a[172:175], a[180:183], v[196:203], v251\nTa 188,209,40960\nCV 196,180\nCvaa 192,200,64,176\nEX 182\n"
"Mvaa 192,204,68\nDtL 211,44,75,1024\nMvaa 192,208,72\nEX 183\nMvaa 192,212,76\nTa 190,209,45056\nCV 197,182\nMvav 64,216,200\nEX 184\n"
"Mvav 68,220,200\nTa 192,208,41216\nMvav 72,224,200\nEX 185\nMvav 76,228,200\nTa 194,208,45312\nCV 198,184\nMvav 80,232,200\ns_waitcnt vmcnt(8)\n"
"EX 186\nMvav 84,236,200\nDtL 212,44,75,2048\nMvav 88,240,200\nEX 187\nMvav 92,244,200\ns_waitcnt lgkmcnt(2)\nTa 196,209,41216\nCV 199,186\n"
"Cvaa 180,248,0,160\nEX 188\nMvaa 180,252,4\nTa 198,209,45312\nMvva 180,216,8\nEX 189\nMvva 180,220,12\nTa 200,208,41472\nCV 200,188\n"
"Cvva 184,224,0,160\nEX 190\nMvva 184,228,4\nDtL 213,44,75,3072\nMvva 184,232,8\nEX 191\nMvva 184,236,12\nTa 202,208,45568\nCV 201,190\n"
"Mvav 96,216,196\nEX 192\nMvav 100,220,196\nTa 204,209,41472\nMvav 104,224,196\nEX 193\nMvav 108,228,196\nTa 206,209,45568\nCV 202,192\n"
"Mvav 112,232,196\nEX 194\nMvav 116,236,196\nTa 208,208,41728\nMvav 120,240,196\nEX 195\nMvav 124,244,196\nTa 210,208,45824\nCV 203,194\n"
"Cvaa 188,248,16,164\nTa 212,209,41728\nMvaa 188,252,20\nEX 180\nMvva 188,216,24\nTa 214,209,45824\nMvva 188,220,28\nEX 181\n"
"v_smfmac_f32_16x16x64_bf16 a[176:179], a[180:183], v[196:203], v251\ns_memrealtime s[98:99]\nCV 196,180\nCvva 192,224,16,164\nEX 182\n"
"Mvva 192,228,20\nMvva 192,232,24\nEX 183\nMvva 192,236,28\nCV 197,182\nMvav 128,216,200\ns_waitcnt vmcnt(20) lgkmcnt(0)\nEX 184\nMvav 132,220,200\n"
"s_barrier\n"
"s_sub_u32 s95, s98, s93\ns_mov_b32 s93, s98\ns_sub_u32 s95, 92, s95\ns_cbranch_scc1 .Lfa_t51b\ns_add_u32 s93, s93, s95\n.Lfa_t51a:\ns_sub_u32 s95, s95, 4\ns_cbranch_scc1 .Lfa_t51b\ns_sleep 1\ns_branch .Lfa_t51a\n.Lfa_t51b:\n"
"Mvav 136,224,200\ns_add_u32 m0, s72, 0x1a000\nEX 185\nMvav 140,228,200\ns_waitcnt vmcnt(8)\nRa 216,204,0\nCV 198,184\nMvav 144,232,200\nEX 186\n"
"Mvav 148,236,200\nDtL 214,48,75,0\nMvav 152,240,200\nEX 187\nMvav 156,244,200\nRa 220,205,0\nCV 199,186\nCvaa 180,248,32,168\nEX 188\n"
"Mvaa 180,252,36\nRa 224,206,0\nMvva 180,216,40\nEX 189\nMvva 180,220,44\nRa 228,207,0\nCV 200,188\nCvva 184,224,32,168\nEX 190\nMvva 184,228,36\n"
"DtL 215,48,75,1024\nMvva 184,232,40\nEX 191\nMvva 184,236,44\nRa 232,204,4096\nCV 201,190\nMvav 0,184,196\nEX 192\nMvav 4,188,196\nRa 236,205,4096\n"
"v_add_u32 v204, 0x10000, v204\nMvav 8,192,196\nEX 193\nMvav 12,196,196\n.Lfa_pd13574:\ns_cbranch_execz .Lfa_wr7\nRa 240,206,4096\nCV 202,192\n"
"Mvav 16,200,196\ns_waitcnt vmcnt(8)\nEX 194\nMvav 20,204,196\nDtL 214,48,75,2048\nMvav 24,208,196\nEX 195\nMvav 28,212,196\nRa 244,207,4096\n"
"CV 203,194\nCvaa 188,248,48,172\nv_add_u32 v205, 0x10000, v205\nv_add_u32 v206, 0x10000, v206\nMvaa 188,252,52\nEX 180\nMvva 188,216,56\n"
"v_add_u32 v207, 0x10000, v207\nMvva 188,220,60\nEX 181\nv_smfmac_f32_16x16x64_bf16 a[160:163], a[180:183], v[196:203], v251\nCV 196,180\n"
"Cvva 192,224,48,172\nEX 182\nMvva 192,228,52\nDtL 215,48,75,3072\nMvva 192,232,56\nEX 183\nMvva 192,236,60\nCV 197,182\nMvav 32,184,200\nEX 184\n"
"Mvav 36,188,200\ns_add_u32 s75, s97, 0x1a000\nMvav 40,192,200\ns_and_b32 s75, s75, 0x1ffff\nEX 185\nMvav 44,196,200\ns_add_u32 m0, s72, 0x8000\n"
"CV 198,184\nMvav 48,200,200\ns_waitcnt vmcnt(8)\nEX 186\nMvav 52,204,200\nDtL 210,44,75,0\nMvav 56,208,200\nEX 187\nMvav 60,212,200\nCV 199,186\n"
"Cvaa 180,248,64,176\nEX 188\nMvaa 180,252,68\nTa 248,208,8192\nMvva 180,216,72\nEX 189\nMvva 180,220,76\nTa 250,208,12288\nCV 200,188\n"
"Cvva 184,224,64,176\nEX 190\nMvva 184,228,68\nDtL 211,44,75,1024\nMvva 184,232,72\nEX 191\nMvva 184,236,76\nTa 252,209,8192\nCV 201,190\n"
"Mvav 64,184,196\nEX 192\nMvav 68,188,196\nTa 254,209,12288\nMvav 72,192,196\nEX 193\nMvav 76,196,196\nTv 216,208,8448\nCV 202,192\nMvav 80,200,196\n"
"s_waitcnt vmcnt(8)\nEX 194\nMvav 84,204,196\nDtL 212,44,75,2048\nMvav 88,208,196\nEX 195\nMvav 92,212,196\ns_waitcnt lgkmcnt(2)\nTv 218,208,12544\n"
"CV 203,194\nCvaa 188,216,0,160\nTv 220,209,8448\nMvaa 188,220,4\nEX 180\nMvaa 188,224,8\nTv 222,209,12544\nMvaa 188,228,12\nEX 181\n"
"v_smfmac_f32_16x16x64_bf16 a[164:167], a[180:183], v[196:203], v251\nTv 224,208,8704\nCV 196,180\nCvaa 192,232,0,160\nEX 182\nMvaa 192,236,4\n"
"DtL 213,44,75,3072\nMvaa 192,240,8\nEX 183\nMvaa 192,244,12\nTv 226,208,12800\nCV 197,182\nMvav 96,184,200\nEX 184\nMvav 100,188,200\n"
"Tv 228,209,8704\nMvav 104,192,200\nEX 185\nMvav 108,196,200\nTv 230,209,12800\nCV 198,184\nMvav 112,200,200\nEX 186\nMvav 116,204,200\n"
"Tv 232,208,8960\nMvav 120,208,200\nEX 187\nMvav 124,212,200\nTv 234,208,13056\nCV 199,186\nCvaa 180,216,16,164\nEX 188\nMvaa 180,220,20\n"
"Tv 236,209,8960\nv_add_u32 v208, 0x10000, v208\nMvaa 180,224,24\nEX 189\nMvaa 180,228,28\ns_memrealtime s[98:99]\nTv 238,209,13056\nCV 200,188\n"
"Cvaa 184,232,16,164\nEX 190\nMvaa 184,236,20\nv_add_u32 v209, 0x10000, v209\nMvaa 184,240,24\nEX 191\nMvaa 184,244,28\nCV 201,190\nMvav 128,184,196\n"
"s_waitcnt vmcnt(20) lgkmcnt(0)\nEX 192\nMvav 132,188,196\ns_barrier\n"
"s_sub_u32 s95, s98, s93\ns_mov_b32 s93, s98\ns_sub_u32 s95, 102, s95\ns_cbranch_scc1 .Lfa_t52b\ns_add_u32 s93, s93, s95\n.Lfa_t52a:\ns_sub_u32 s95, s95, 4\ns_cbranch_scc1 .Lfa_t52b\ns_sleep 1\ns_branch .Lfa_t52a\n.Lfa_t52b:\n"
"Mvav 136,192,196\ns_add_u32 m0, s72, 0xa000\nEX 193\nMvav 140,196,196\ns_waitcnt vmcnt(8)\nRa 184,204,0\nCV 202,192\nMvav 144,200,196\nEX 194\n"
"Mvav 148,204,196\nDtL 214,48,75,0\nMvav 152,208,196\nEX 195\nMvav 156,212,196\nRa 188,205,0\nCV 203,194\nCvaa 188,216,32,168\nRa 192,206,0\n"
"Mvaa 188,220,36\nEX 180\nMvaa 188,224,40\nRa 196,207,0\nMvaa 188,228,44\nEX 181\nv_smfmac_f32_16x16x64_bf16 a[168:171], a[180:183], v[196:203], v251\n"
"Ra 200,204,4096\nCV 196,180\nCvaa 192,232,32,168\nEX 182\nMvaa 192,236,36\nDtL 215,48,75,1024\nMvaa 192,240,40\nEX 183\nMvaa 192,244,44\n"
"Ra 204,205,4096\nCV 197,182\nMvav 0,248,200\nEX 184\nMvav 4,252,200\nRa 208,206,4096\nv_add_u32 v204, 0x10000, v204\nMvvv 8,216,200\nEX 185\n"
"Mvvv 12,220,200\nRa 212,207,4096\nCV 198,184\nMvvv 16,224,200\ns_waitcnt vmcnt(8)\nEX 186\nMvvv 20,228,200\nDtL 214,48,75,2048\nMvvv 24,232,200\n"
"EX 187\nMvvv 28,236,200\nCV 199,186\nv_add_u32 v205, 0x10000, v205\nCvaa 180,216,48,172\nEX 188\nMvaa 180,220,52\nv_add_u32 v206, 0x10000, v206\n"
"v_add_u32 v207, 0x10000, v207\nMvaa 180,224,56\nEX 189\nMvaa 180,228,60\nCV 200,188\nCvaa 184,232,48,172\nEX 190\nMvaa 184,236,52\n"
"DtL 215,48,75,3072\nMvaa 184,240,56\nEX 191\nMvaa 184,244,60\nCV 201,190\nMvav 32,248,196\nEX 192\nMvav 36,252,196\ns_add_u32 s75, s97, 0x1c000\n"
"Mvvv 40,216,196\ns_and_b32 s75, s75, 0x1ffff\nEX 193\nMvvv 44,220,196\ns_add_u32 m0, s72, 0x0\nCV 202,192\nMvvv 48,224,196\ns_waitcnt vmcnt(8)\n"
"EX 194\nMvvv 52,228,196\nDtL 210,44,75,0\nMvvv 56,232,196\nEX 195\nMvvv 60,236,196\nCV 203,194\nCvaa 188,216,64,176\nTa 216,208,8192\n"
"Mvaa 188,220,68\nEX 180\nMvaa 188,224,72\nTa 218,208,12288\nMvaa 188,228,76\nEX 181\n"
"v_smfmac_f32_16x16x64_bf16 a[172:175], a[180:183], v[196:203], v251\nTa 220,209,8192\nCV 196,180\nCvaa 192,232,64,176\nEX 182\nMvaa 192,236,68\n"
"DtL 211,44,75,1024\nMvaa 192,240,72\nEX 183\nMvaa 192,244,76\nTa 222,209,12288\nCV 197,182\nMvav 64,248,200\nEX 184\nMvav 68,252,200\n"
"Ta 224,208,8448\nMvvv 72,216,200\nEX 185\nMvvv 76,220,200\nTa 226,208,12544\nCV 198,184\nMvvv 80,224,200\ns_waitcnt vmcnt(8)\nEX 186\n"
"Mvvv 84,228,200\nDtL 212,44,75,2048\nMvvv 88,232,200\nEX 187\nMvvv 92,236,200\ns_waitcnt lgkmcnt(2)\nTa 228,209,8448\nCV 199,186\nCvaa 180,184,0,160\n"
"EX 188\nMvaa 180,188,4\nTa 230,209,12544\nMvaa 180,192,8\nEX 189\nMvaa 180,196,12\nTa 232,208,8704\nCV 200,188\nCvaa 184,200,0,160\nEX 190\n"
"Mvaa 184,204,4\nDtL 213,44,75,3072\nMvaa 184,208,8\nEX 191\nMvaa 184,212,12\nTa 234,208,12800\nCV 201,190\nMvav 96,248,196\nEX 192\nMvav 100,252,196\n"
"Ta 236,209,8704\nMvvv 104,216,196\nEX 193\nMvvv 108,220,196\nTa 238,209,12800\nCV 202,192\nMvvv 112,224,196\nEX 194\nMvvv 116,228,196\n"
"Ta 240,208,8960\nMvvv 120,232,196\nEX 195\nMvvv 124,236,196\nTa 242,208,13056\nCV 203,194\nCvaa 188,184,16,164\nTa 244,209,8960\nMvaa 188,188,20\n"
"EX 180\nMvaa 188,192,24\nTa 246,209,13056\nv_add_u32 v208, 0x10000, v208\nMvaa 188,196,28\nEX 181\n"
"v_smfmac_f32_16x16x64_bf16 a[176:179], a[180:183], v[196:203], v251\ns_memrealtime s[98:99]\nCV 196,180\nv_add_u32 v209, 0x10000, v209\n"
"Cvaa 192,200,16,164\nEX 182\nMvaa 192,204,20\nMvaa 192,208,24\nEX 183\nMvaa 192,212,28\nCV 197,182\nMvav 128,248,200\ns_waitcnt vmcnt(20) lgkmcnt(0)\n"
"EX 184\nMvav 132,252,200\ns_barrier\n"
"s_sub_u32 s95, s98, s93\ns_mov_b32 s93, s98\ns_sub_u32 s95, 102, s95\ns_cbranch_scc1 .Lfa_t53b\ns_add_u32 s93, s93, s95\n.Lfa_t53a:\ns_sub_u32 s95, s95, 4\ns_cbranch_scc1 .Lfa_t53b\ns_sleep 1\ns_branch .Lfa_t53a\n.Lfa_t53b:\n"
"Mvvv 136,216,200\ns_add_u32 m0, s72, 0x2000\nEX 185\nMvvv 140,220,200\ns_waitcnt vmcnt(8)\nRa 248,204,0\nCV 198,184\nMvvv 144,224,200\nEX 186\n"
"Mvvv 148,228,200\nDtL 214,48,75,0\nMvvv 152,232,200\nEX 187\nMvvv 156,236,200\nRa 252,205,0\nCV 199,186\nCvaa 180,184,32,168\nEX 188\n"
"Mvaa 180,188,36\nRv 216,206,0\nMvaa 180,192,40\nEX 189\nMvaa 180,196,44\nRv 220,207,0\nCV 200,188\nCvaa 184,200,32,168\nEX 190\nMvaa 184,204,36\n"
"DtL 215,48,75,1024\nMvaa 184,208,40\nEX 191\nMvaa 184,212,44\nRv 224,204,4096\nCV 201,190\nMvav 0,216,196\nEX 192\nMvav 4,220,196\nRv 228,205,4096\n"
"v_add_u32 v204, 0xffff0000, v204\nMvav 8,224,196\nEX 193\nMvav 12,228,196\nRv 232,206,4096\nCV 202,192\nMvav 16,232,196\ns_waitcnt vmcnt(8)\nEX 194\n"
"Mvav 20,236,196\nDtL 214,48,75,2048\nMvav 24,240,196\nEX 195\nMvav 28,244,196\nRv 236,207,4096\nCV 203,194\nCvaa 188,184,48,172\n"
"v_add_u32 v205, 0xffff0000, v205\nv_add_u32 v206, 0xffff0000, v206\nMvaa 188,188,52\nEX 180\nMvaa 188,192,56\nv_add_u32 v207, 0xffff0000, v207\n"
"Mvaa 188,196,60\nEX 181\nv_smfmac_f32_16x16x64_bf16 a[160:163], a[180:183], v[196:203], v251\nCV 196,180\nCvaa 192,200,48,172\nEX 182\n"
"Mvaa 192,204,52\nDtL 215,48,75,3072\nMvaa 192,208,56\nEX 183\nMvaa 192,212,60\nCV 197,182\nMvav 32,216,200\nEX 184\nMvav 36,220,200\nMvav 40,224,200\n"
"EX 185\nMvav 44,228,200\nCV 198,184\nMvav 48,232,200\nEX 186\nMvav 52,236,200\nMvav 56,240,200\nEX 187\nMvav 60,244,200\nCV 199,186\n"
"Cvaa 180,184,64,176\nEX 188\nMvaa 180,188,68\nTa 184,208,8192\nMvaa 180,192,72\nEX 189\nMvaa 180,196,76\nTa 186,208,12288\nCV 200,188\n"
"Cvaa 184,200,64,176\nEX 190\nMvaa 184,204,68\nTa 188,209,8192\nMvaa 184,208,72\nEX 191\nMvaa 184,212,76\nTa 190,209,12288\nCV 201,190\n"
"Mvav 64,216,196\nEX 192\nMvav 68,220,196\nTa 192,208,8448\nMvav 72,224,196\nEX 193\nMvav 76,228,196\nTa 194,208,12544\nCV 202,192\nMvav 80,232,196\n"
"EX 194\nMvav 84,236,196\nTa 196,209,8448\nMvav 88,240,196\nEX 195\nMvav 92,244,196\ns_waitcnt lgkmcnt(3)\nTa 198,209,12544\nCV 203,194\n"
"Cvaa 188,248,0,160\nTa 200,208,8704\nMvaa 188,252,4\nEX 180\nMvva 188,216,8\nTa 202,208,12800\nMvva 188,220,12\nEX 181\n"
"v_smfmac_f32_16x16x64_bf16 a[164:167], a[180:183], v[196:203], v251\nTa 204,209,8704\nCV 196,180\nCvva 192,224,0,160\nEX 182\nMvva 192,228,4\n"
"Ta 206,209,12800\nMvva 192,232,8\nEX 183\nMvva 192,236,12\nTa 208,208,8960\nCV 197,182\nMvav 96,216,200\nEX 184\nMvav 100,220,200\nTa 210,208,13056\n"
"Mvav 104,224,200\nEX 185\nMvav 108,228,200\nTa 212,209,8960\nCV 198,184\nMvav 112,232,200\nEX 186\nMvav 116,236,200\nTa 214,209,13056\n"
"v_add_u32 v208, 0xffff0000, v208\nMvav 120,240,200\nEX 187\nMvav 124,244,200\nCV 199,186\nv_add_u32 v209, 0xffff0000, v209\nCvaa 180,248,16,164\n"
"EX 188\nMvaa 180,252,20\nMvva 180,216,24\nEX 189\nMvva 180,220,28\ns_memrealtime s[98:99]\nCV 200,188\nCvva 184,224,16,164\nEX 190\nMvva 184,228,20\n"
"Mvva 184,232,24\nEX 191\nMvva 184,236,28\nCV 201,190\nMvav 128,216,196\ns_waitcnt vmcnt(16) lgkmcnt(0)\nEX 192\nMvav 132,220,196\ns_barrier\n"
"s_mov_b64 exec, 0\ns_cmp_eq_u32 s60, 0\ns_cbranch_scc1 .Lfa_w9_0\ns_cmp_eq_u32 s60, 1\ns_cbranch_scc1 .Lfa_w9_1\ns_cmp_eq_u32 s60, 2\ns_cbranch_scc1 .Lfa_w9_2\ns_branch .Lfa_w9_3\n.Lfa_wr9:\ns_mov_b64 exec, -1\ns_nop 4\n"
"s_sub_u32 s95, s98, s93\ns_mov_b32 s93, s98\ns_sub_u32 s95, 102, s95\ns_cbranch_scc1 .Lfa_t54b\ns_add_u32 s93, s93, s95\n.Lfa_t54a:\ns_sub_u32 s95, s95, 4\ns_cbranch_scc1 .Lfa_t54b\ns_sleep 1\ns_branch .Lfa_t54a\n.Lfa_t54b:\n"
"Mvav 136,224,196\ns_add_u32 s75, s97, 0x1e000\nEX 193\nMvav 140,228,196\ns_and_b32 s75, s75, 0x1ffff\nRa 216,204,32768\nCV 202,192\nMvav 144,232,196\n"
"s_add_u32 m0, s72, 0x10000\nEX 194\nMvav 148,236,196\ns_waitcnt vmcnt(8)\nRa 220,205,32768\nMvav 152,240,196\nEX 195\nMvav 156,244,196\n"
"Ra 224,206,32768\nCV 203,194\nCvaa 188,248,32,168\nDtL 210,44,75,0\nMvaa 188,252,36\nEX 180\nMvva 188,216,40\nRa 228,207,32768\nMvva 188,220,44\n"
"EX 181\nv_smfmac_f32_16x16x64_bf16 a[168:171], a[180:183], v[196:203], v251\nRa 232,204,36864\nCV 196,180\nCvva 192,224,32,168\nEX 182\n"
"Mvva 192,228,36\nRa 236,205,36864\nv_add_u32 v204, 0xffff0000, v204\nMvva 192,232,40\n.Lfa_pd14140:\ns_cbranch_execz .Lfa_wr7\nEX 183\n"
"Mvva 192,236,44\nRa 240,206,36864\nCV 197,182\nMvav 0,184,200\nEX 184\nMvav 4,188,200\nDtL 211,44,75,1024\nMvav 8,192,200\nEX 185\nMvav 12,196,200\n"
"Ra 244,207,36864\nCV 198,184\nMvav 16,200,200\nEX 186\nMvav 20,204,200\nv_add_u32 v205, 0xffff0000, v205\nv_add_u32 v206, 0xffff0000, v206\n"
"Mvav 24,208,200\nEX 187\nMvav 28,212,200\nCV 199,186\nv_add_u32 v207, 0xffff0000, v207\nCvaa 180,248,48,172\ns_waitcnt vmcnt(8)\nEX 188\n"
"Mvaa 180,252,52\nDtL 212,44,75,2048\nMvva 180,216,56\nEX 189\nMvva 180,220,60\nCV 200,188\nCvva 184,224,48,172\nEX 190\nMvva 184,228,52\n"
"Mvva 184,232,56\nEX 191\nMvva 184,236,60\nCV 201,190\nMvav 32,184,196\nEX 192\nMvav 36,188,196\nDtL 213,44,75,3072\nMvav 40,192,196\nEX 193\n"
"Mvav 44,196,196\nCV 202,192\nMvav 48,200,196\nEX 194\nMvav 52,204,196\nMvav 56,208,196\nEX 195\nMvav 60,212,196\ns_add_u32 m0, s72, 0x12000\n"
"CV 203,194\nCvaa 188,248,64,176\ns_waitcnt vmcnt(8)\nTa 248,208,40960\nMvaa 188,252,68\nEX 180\nMvva 188,216,72\nDtL 214,48,75,0\nMvva 188,220,76\n"
"EX 181\nv_smfmac_f32_16x16x64_bf16 a[172:175], a[180:183], v[196:203], v251\nTa 250,208,45056\nCV 196,180\nCvva 192,224,64,176\nEX 182\n"
"Mvva 192,228,68\nTa 252,209,40960\nMvva 192,232,72\nEX 183\nMvva 192,236,76\nTa 254,209,45056\nCV 197,182\nMvav 64,184,200\nEX 184\nMvav 68,188,200\n"
"DtL 215,48,75,1024\nMvav 72,192,200\nEX 185\nMvav 76,196,200\nTv 216,208,41216\nCV 198,184\nMvav 80,200,200\nEX 186\nMvav 84,204,200\n"
"Tv 218,208,45312\nMvav 88,208,200\nEX 187\nMvav 92,212,200\ns_waitcnt lgkmcnt(2)\nTv 220,209,41216\nCV 199,186\nCvaa 180,216,0,160\n"
"s_waitcnt vmcnt(8)\nEX 188\nMvaa 180,220,4\nDtL 214,48,75,2048\nMvaa 180,224,8\nEX 189\nMvaa 180,228,12\nTv 222,209,45312\nCV 200,188\n"
"Cvaa 184,232,0,160\nEX 190\nMvaa 184,236,4\nTv 224,208,41472\nMvaa 184,240,8\nEX 191\nMvaa 184,244,12\nTv 226,208,45568\nCV 201,190\nMvav 96,184,196\n"
"EX 192\nMvav 100,188,196\nDtL 215,48,75,3072\nMvav 104,192,196\nEX 193\nMvav 108,196,196\nTv 228,209,41472\nCV 202,192\nMvav 112,200,196\nEX 194\n"
"Mvav 116,204,196\nTv 230,209,45568\nMvav 120,208,196\nEX 195\nMvav 124,212,196\nTv 232,208,41728\nCV 203,194\nCvaa 188,216,16,164\nTv 234,208,45824\n"
"Mvaa 188,220,20\nEX 180\nMvaa 188,224,24\nTv 236,209,41728\nv_add_u32 v208, 0xffff0000, v208\nMvaa 188,228,28\nEX 181\n"
"v_smfmac_f32_16x16x64_bf16 a[176:179], a[180:183], v[196:203], v251\ns_memrealtime s[98:99]\nTv 238,209,45824\nCV 196,180\nCvaa 192,232,16,164\n"
"EX 182\nMvaa 192,236,20\nv_add_u32 v209, 0xffff0000, v209\nMvaa 192,240,24\nEX 183\nMvaa 192,244,28\nCV 197,182\nMvav 128,184,200\n"
"s_waitcnt vmcnt(16) lgkmcnt(0)\nEX 184\nMvav 132,188,200\ns_barrier\n"
"s_sub_u32 s95, s98, s93\ns_mov_b32 s93, s98\ns_sub_u32 s95, 102, s95\ns_cbranch_scc1 .Lfa_t55b\ns_add_u32 s93, s93, s95\n.Lfa_t55a:\ns_sub_u32 s95, s95, 4\ns_cbranch_scc1 .Lfa_t55b\ns_sleep 1\ns_branch .Lfa_t55a\n.Lfa_t55b:\n"
"Mvav 136,192,200\ns_add_u32 s75, s97, 0x0\nEX 185\nMvav 140,196,200\ns_and_b32 s75, s75, 0x1ffff\nRa 184,204,32768\nCV 198,184\nMvav 144,200,200\n"
"s_add_u32 s75, s75, 0x40000\nEX 186\nMvav 148,204,200\ns_add_u32 m0, s72, 0x20000\nRa 188,205,32768\nMvav 152,208,200\ns_waitcnt vmcnt(8)\nEX 187\n"
"Mvav 156,212,200\nRa 192,206,32768\nCV 199,186\nCvaa 180,216,32,168\nEX 188\nMvaa 180,220,36\nDtL 210,44,75,0\nMvaa 180,224,40\nEX 189\n"
"Mvaa 180,228,44\nRa 196,207,32768\nCV 200,188\nCvaa 184,232,32,168\nEX 190\nMvaa 184,236,36\nRa 200,204,36864\nMvaa 184,240,40\nEX 191\n"
"Mvaa 184,244,44\nRa 204,205,36864\nCV 201,190\nMvav 0,248,196\nEX 192\nMvav 4,252,196\nDtL 211,44,75,1024\nMvvv 8,216,196\nEX 193\nMvvv 12,220,196\n"
"Ra 208,206,36864\nCV 202,192\nMvvv 16,224,196\nEX 194\nMvvv 20,228,196\nRa 212,207,36864\nMvvv 24,232,196\nEX 195\nMvvv 28,236,196\nCV 203,194\n"
"Cvaa 188,216,48,172\ns_waitcnt vmcnt(8)\nMvaa 188,220,52\nEX 180\nMvaa 188,224,56\nDtL 212,44,75,2048\nMvaa 188,228,60\nEX 181\n"
"v_smfmac_f32_16x16x64_bf16 a[160:163], a[180:183], v[196:203], v251\nCV 196,180\nCvaa 192,232,48,172\nEX 182\nMvaa 192,236,52\nMvaa 192,240,56\n"
"EX 183\nMvaa 192,244,60\nCV 197,182\nMvav 32,248,200\nEX 184\nMvav 36,252,200\nDtL 213,44,75,3072\nMvvv 40,216,200\nEX 185\nMvvv 44,220,200\n"
"CV 198,184\nMvvv 48,224,200\nEX 186\nMvvv 52,228,200\nMvvv 56,232,200\nEX 187\nMvvv 60,236,200\nCV 199,186\nCvaa 180,216,64,176\nEX 188\n"
"Mvaa 180,220,68\nTa 216,208,40960\nMvaa 180,224,72\nEX 189\nMvaa 180,228,76\nTa 218,208,45056\nCV 200,188\nCvaa 184,232,64,176\nEX 190\n"
"Mvaa 184,236,68\nTa 220,209,40960\nMvaa 184,240,72\nEX 191\nMvaa 184,244,76\nTa 222,209,45056\nCV 201,190\nMvav 64,248,196\nEX 192\nMvav 68,252,196\n"
"Ta 224,208,41216\nMvvv 72,216,196\nEX 193\nMvvv 76,220,196\nTa 226,208,45312\nCV 202,192\nMvvv 80,224,196\nEX 194\nMvvv 84,228,196\nTa 228,209,41216\n"
"Mvvv 88,232,196\nEX 195\nMvvv 92,236,196\ns_waitcnt lgkmcnt(3)\nTa 230,209,45312\nCV 203,194\nCvaa 188,184,0,160\nTa 232,208,41472\nMvaa 188,188,4\n"
"EX 180\nMvaa 188,192,8\nTa 234,208,45568\nMvaa 188,196,12\nEX 181\nv_smfmac_f32_16x16x64_bf16 a[164:167], a[180:183], v[196:203], v251\n"
"Ta 236,209,41472\nCV 196,180\nCvaa 192,200,0,160\nEX 182\nMvaa 192,204,4\nTa 238,209,45568\nMvaa 192,208,8\nEX 183\nMvaa 192,212,12\n"
"Ta 240,208,41728\nCV 197,182\nMvav 96,248,200\nEX 184\nMvav 100,252,200\nTa 242,208,45824\nMvvv 104,216,200\nEX 185\nMvvv 108,220,200\n"
"Ta 244,209,41728\nCV 198,184\nMvvv 112,224,200\nEX 186\nMvvv 116,228,200\nTa 246,209,45824\nMvvv 120,232,200\nEX 187\nMvvv 124,236,200\nCV 199,186\n"
"Cvaa 180,184,16,164\nEX 188\nMvaa 180,188,20\nMvaa 180,192,24\nEX 189\nMvaa 180,196,28\ns_memrealtime s[98:99]\nCV 200,188\nCvaa 184,200,16,164\n"
"EX 190\nMvaa 184,204,20\nMvaa 184,208,24\nEX 191\nMvaa 184,212,28\nCV 201,190\nMvav 128,248,196\ns_waitcnt vmcnt(12) lgkmcnt(0)\nEX 192\n"
"Mvav 132,252,196\ns_barrier\n"
"s_sub_u32 s95, s98, s93\ns_mov_b32 s93, s98\ns_sub_u32 s95, 102, s95\ns_cbranch_scc1 .Lfa_t56b\ns_add_u32 s93, s93, s95\n.Lfa_t56a:\ns_sub_u32 s95, s95, 4\ns_cbranch_scc1 .Lfa_t56b\ns_sleep 1\ns_branch .Lfa_t56a\n.Lfa_t56b:\n"
"Mvvv 136,216,196\ns_add_u32 m0, s72, 0x22000\nEX 193\nMvvv 140,220,196\ns_waitcnt vmcnt(8)\nRa 248,204,0\nCV 202,192\nMvvv 144,224,196\nEX 194\n"
"Mvvv 148,228,196\nDL 214,48,75,0\nMvvv 152,232,196\nEX 195\nMvvv 156,236,196\nRa 252,205,0\nCV 203,194\nCvaa 188,184,32,168\nRv 216,206,0\n"
"Mvaa 188,188,36\nEX 180\nMvaa 188,192,40\nDL 215,48,75,1024\nMvaa 188,196,44\nEX 181\n"
"v_smfmac_f32_16x16x64_bf16 a[168:171], a[180:183], v[196:203], v251\nRv 220,207,0\nCV 196,180\nCvaa 192,200,32,168\ns_waitcnt vmcnt(8)\nEX 182\n"
"Mvaa 192,204,36\nDL 214,48,75,2048\nMvaa 192,208,40\nEX 183\nMvaa 192,212,44\nRv 224,204,4096\nCV 197,182\nMvav 0,216,200\nEX 184\nMvav 4,220,200\n"
"DL 215,48,75,3072\nMvav 8,224,200\ns_add_u32 s76, s90, 0x200\nEX 185\nMvav 12,228,200\ns_and_b32 s76, s76, 0x3fffff\nRv 228,205,4096\nCV 198,184\n"
"Mvav 16,232,200\ns_add_u32 m0, s63, 0x18000\nEX 186\nMvav 20,236,200\nDL 242,40,76,0\nMvav 24,240,200\nEX 187\nMvav 28,244,200\nRv 232,206,4096\n"
"CV 199,186\nCvaa 180,184,48,172\nEX 188\nMvaa 180,188,52\nDL 243,40,76,1024\nMvaa 180,192,56\nEX 189\nMvaa 180,196,60\nRv 236,207,4096\nCV 200,188\n"
"Cvaa 184,200,48,172\nEX 190\nMvaa 184,204,52\nDL 244,40,76,2048\nMvaa 184,208,56\nEX 191\nMvaa 184,212,60\nCV 201,190\nv_add_u32 v204, 0x10000, v204\n"
"Mvav 32,216,196\nEX 192\nMvav 36,220,196\nDL 245,40,76,3072\nMvav 40,224,196\ns_add_u32 s77, s91, 0x0\nEX 193\nMvav 44,228,196\n"
"s_and_b32 s77, s77, 0x3ff\nCV 202,192\nv_add_u32 v205, 0x10000, v205\nMvav 48,232,196\ns_add_u32 m0, s73, 0x18000\nEX 194\nMvav 52,236,196\n"
"DL 246,36,77,0\nMvav 56,240,196\nEX 195\nMvav 60,244,196\nCV 203,194\nv_add_u32 v206, 0x10000, v206\nCvaa 188,184,64,176\nTa 184,208,8192\n"
"v_add_u32 v207, 0x10000, v207\nMvaa 188,188,68\nEX 180\nMvaa 188,192,72\nDL 247,36,77,1024\nMvaa 188,196,76\nEX 181\n"
"v_smfmac_f32_16x16x64_bf16 a[172:175], a[180:183], v[196:203], v251\nTa 186,208,12288\nCV 196,180\nCvaa 192,200,64,176\nEX 182\nMvaa 192,204,68\n"
"DL 248,36,77,2048\nMvaa 192,208,72\nEX 183\nMvaa 192,212,76\nTa 188,209,8192\nCV 197,182\nMvav 64,216,200\ns_add_u32 m0, s72, 0xa000\nEX 184\n"
"Mvav 68,220,200\nDL 249,36,77,0\nMvav 72,224,200\nEX 185\nMvav 76,228,200\nTa 190,209,12288\nCV 198,184\nMvav 80,232,200\nEX 186\nMvav 84,236,200\n"
"DL 250,36,77,1024\nMvav 88,240,200\ns_add_u32 s76, s90, 0x280\nEX 187\nMvav 92,244,200\ns_and_b32 s76, s76, 0x3fffff\nTa 192,208,8448\nCV 199,186\n"
"s_waitcnt lgkmcnt(2)\nCvaa 180,248,0,160\ns_add_u32 m0, s72, 0x8000\nEX 188\nMvaa 180,252,4\nDL 242,40,76,0\nMvva 180,216,8\nEX 189\nMvva 180,220,12\n"
"Ta 194,208,12544\nCV 200,188\nCvva 184,224,0,160\nEX 190\nMvva 184,228,4\nDL 243,40,76,1024\nMvva 184,232,8\nEX 191\nMvva 184,236,12\n"
"Ta 196,209,8448\nCV 201,190\nMvav 96,216,196\nEX 192\nMvav 100,220,196\nDL 244,40,76,2048\nMvav 104,224,196\nEX 193\nMvav 108,228,196\n"
"Ta 198,209,12544\nCV 202,192\nMvav 112,232,196\nEX 194\nMvav 116,236,196\nDL 245,40,76,3072\nMvav 120,240,196\ns_add_u32 s77, s91, 0x80\nEX 195\n"
"Mvav 124,244,196\ns_and_b32 s77, s77, 0x3ff\nTa 200,208,8704\nCV 203,194\nCvaa 188,248,16,164\ns_add_u32 m0, s74, 0x8000\nTa 202,208,12800\n"
"Mvaa 188,252,20\nEX 180\nMvva 188,216,24\nDL 249,36,77,0\nMvva 188,220,28\nEX 181\n"
"v_smfmac_f32_16x16x64_bf16 a[176:179], a[180:183], v[196:203], v251\ns_memrealtime s[98:99]\nTa 204,209,8704\nCV 196,180\nCvva 192,224,16,164\n"
"EX 182\nMvva 192,228,20\nDL 250,36,77,1024\nMvva 192,232,24\nEX 183\nMvva 192,236,28\nTa 206,209,12800\nCV 197,182\nMvav 128,216,200\nEX 184\n"
"Mvav 132,220,200\nTa 208,208,8960\nTa 210,208,13056\nTa 212,209,8960\nTa 214,209,13056\ns_waitcnt vmcnt(23) lgkmcnt(0)\ns_barrier\n"
"s_sub_u32 s95, s98, s93\ns_mov_b32 s93, s98\ns_sub_u32 s95, 102, s95\ns_cbranch_scc1 .Lfa_t57b\ns_add_u32 s93, s93, s95\n.Lfa_t57a:\ns_sub_u32 s95, s95, 4\ns_cbranch_scc1 .Lfa_t57b\ns_sleep 1\ns_branch .Lfa_t57a\n.Lfa_t57b:\n"
"Mvav 136,224,200\ns_add_u32 s75, s97, 0x2000\nEX 185\nMvav 140,228,200\ns_and_b32 s75, s75, 0x1ffff\nRa 216,204,0\nCV 198,184\nMvav 144,232,200\n"
"s_add_u32 s75, s75, 0x40000\nEX 186\nMvav 148,236,200\ns_add_u32 m0, s72, 0x0\nRa 220,205,0\nv_add_u32 v208, 0x10000, v208\nMvav 152,240,200\n"
"s_waitcnt vmcnt(8)\nEX 187\nMvav 156,244,200\n.Lfa_pd14710:\ns_cbranch_execz .Lfa_wr7\nRa 224,206,0\nCV 199,186\nCvaa 180,248,32,168\nEX 188\n"
"Mvaa 180,252,36\nDtL 210,44,75,0\nMvva 180,216,40\nEX 189\nMvva 180,220,44\nRa 228,207,0\nCV 200,188\nCvva 184,224,32,168\nEX 190\nMvva 184,228,36\n"
"Ra 232,204,4096\nv_add_u32 v209, 0x10000, v209\nMvva 184,232,40\nEX 191\nMvva 184,236,44\nRa 236,205,4096\nCV 201,190\nMvav 0,184,196\nEX 192\n"
"Mvav 4,188,196\nDtL 211,44,75,1024\nMvav 8,192,196\nEX 193\nMvav 12,196,196\nRa 240,206,4096\nCV 202,192\nMvav 16,200,196\nEX 194\nMvav 20,204,196\n"
"Ra 244,207,4096\nMvav 24,208,196\nEX 195\nMvav 28,212,196\nCV 203,194\nCvaa 188,248,48,172\ns_waitcnt vmcnt(8)\nMvaa 188,252,52\nEX 180\n"
"Mvva 188,216,56\nDtL 212,44,75,2048\nMvva 188,220,60\nEX 181\nv_smfmac_f32_16x16x64_bf16 a[160:163], a[180:183], v[196:203], v251\nCV 196,180\n"
"Cvva 192,224,48,172\nEX 182\nMvva 192,228,52\nMvva 192,232,56\nEX 183\nMvva 192,236,60\nCV 197,182\nMvav 32,184,200\nEX 184\nMvav 36,188,200\n"
"DtL 213,44,75,3072\nMvav 40,192,200\nEX 185\nMvav 44,196,200\nCV 198,184\nMvav 48,200,200\nEX 186\nMvav 52,204,200\nMvav 56,208,200\nEX 187\n"
"Mvav 60,212,200\nCV 199,186\nCvaa 180,248,64,176\nEX 188\nMvaa 180,252,68\nTa 248,208,8192\nMvva 180,216,72\nEX 189\nMvva 180,220,76\n"
"Ta 250,208,12288\nCV 200,188\nCvva 184,224,64,176\nEX 190\nMvva 184,228,68\nTa 252,209,8192\nMvva 184,232,72\nEX 191\nMvva 184,236,76\n"
"Ta 254,209,12288\nCV 201,190\nMvav 64,184,196\nEX 192\nMvav 68,188,196\nTv 216,208,8448\nMvav 72,192,196\nEX 193\nMvav 76,196,196\nTv 218,208,12544\n"
"CV 202,192\nMvav 80,200,196\nEX 194\nMvav 84,204,196\nTv 220,209,8448\nMvav 88,208,196\nEX 195\nMvav 92,212,196\ns_waitcnt lgkmcnt(3)\n"
"Tv 222,209,12544\nCV 203,194\nCvaa 188,216,0,160\nTv 224,208,8704\nMvaa 188,220,4\nEX 180\nMvaa 188,224,8\nTv 226,208,12800\nMvaa 188,228,12\nEX 181\n"
"v_smfmac_f32_16x16x64_bf16 a[164:167], a[180:183], v[196:203], v251\nTv 228,209,8704\nCV 196,180\nCvaa 192,232,0,160\nEX 182\nMvaa 192,236,4\n"
"Tv 230,209,12800\nMvaa 192,240,8\nEX 183\nMvaa 192,244,12\nTv 232,208,8960\nCV 197,182\nMvav 96,184,200\nEX 184\nMvav 100,188,200\nTv 234,208,13056\n"
"Mvav 104,192,200\nEX 185\nMvav 108,196,200\nTv 236,209,8960\nCV 198,184\nMvav 112,200,200\nEX 186\nMvav 116,204,200\nTv 238,209,13056\n"
"Mvav 120,208,200\nEX 187\nMvav 124,212,200\nCV 199,186\nCvaa 180,216,16,164\nEX 188\nMvaa 180,220,20\nMvaa 180,224,24\nEX 189\nMvaa 180,228,28\n"
"s_memrealtime s[98:99]\nCV 200,188\nCvaa 184,232,16,164\nEX 190\nMvaa 184,236,20\nMvaa 184,240,24\nEX 191\nMvaa 184,244,28\nCV 201,190\n"
"Mvav 128,184,196\ns_waitcnt lgkmcnt(0)\nEX 192\nMvav 132,188,196\ns_barrier\n"
"s_sub_u32 s95, s98, s93\ns_mov_b32 s93, s98\ns_sub_u32 s95, 102, s95\ns_cbranch_scc1 .Lfa_t58b\ns_add_u32 s93, s93, s95\n.Lfa_t58a:\ns_sub_u32 s95, s95, 4\ns_cbranch_scc1 .Lfa_t58b\ns_sleep 1\ns_branch .Lfa_t58a\n.Lfa_t58b:\n"
"Mvav 136,192,196\ns_add_u32 m0, s72, 0x2000\nEX 193\nMvav 140,196,196\ns_waitcnt vmcnt(8)\nCV 202,192\nv_add_u32 v240, 0x10000, v240\n"
"Mvav 144,200,196\nEX 194\nMvav 148,204,196\nDL 214,48,75,0\nMvav 152,208,196\nEX 195\nMvav 156,212,196\nCV 203,194\nv_add_u32 v241, 0x10000, v241\n"
"Cvaa 188,216,32,168\nMvaa 188,220,36\nEX 180\nMvaa 188,224,40\nDL 215,48,75,1024\nMvaa 188,228,44\nEX 181\n"
"v_smfmac_f32_16x16x64_bf16 a[168:171], a[180:183], v[196:203], v251\nCV 196,180\nCvaa 192,232,32,168\ns_waitcnt vmcnt(8)\nEX 182\nMvaa 192,236,36\n"
"DL 214,48,75,2048\nMvaa 192,240,40\nEX 183\nMvaa 192,244,44\nCV 197,182\nMvav 0,248,200\nEX 184\nMvav 4,252,200\nDL 215,48,75,3072\nMvvv 8,216,200\n"
"s_add_u32 s76, s90, 0x80200\nEX 185\nMvvv 12,220,200\ns_and_b32 s76, s76, 0x3fffff\nCV 198,184\nMvvv 16,224,200\ns_add_u32 m0, s63, 0x10000\nEX 186\n"
"Mvvv 20,228,200\nDL 242,40,76,0\nMvvv 24,232,200\nEX 187\nMvvv 28,236,200\nCV 199,186\nCvaa 180,216,48,172\nEX 188\nMvaa 180,220,52\n"
"DL 243,40,76,1024\nMvaa 180,224,56\nEX 189\nMvaa 180,228,60\nCV 200,188\nCvaa 184,232,48,172\nEX 190\nMvaa 184,236,52\nDL 244,40,76,2048\n"
"Mvaa 184,240,56\nEX 191\nMvaa 184,244,60\nCV 201,190\nMvav 32,248,196\nEX 192\nMvav 36,252,196\nDL 245,40,76,3072\nMvvv 40,216,196\n"
"s_add_u32 s77, s91, 0x80\nEX 193\nMvvv 44,220,196\ns_and_b32 s77, s77, 0x3ff\nCV 202,192\nMvvv 48,224,196\ns_add_u32 m0, s73, 0x10000\nEX 194\n"
"Mvvv 52,228,196\nDL 246,36,77,0\nMvvv 56,232,196\nEX 195\nMvvv 60,236,196\nCV 203,194\nCvaa 188,216,64,176\nMvaa 188,220,68\nEX 180\nMvaa 188,224,72\n"
"DL 247,36,77,1024\nMvaa 188,228,76\nEX 181\nv_smfmac_f32_16x16x64_bf16 a[172:175], a[180:183], v[196:203], v251\nCV 196,180\nCvaa 192,232,64,176\n"
"EX 182\nMvaa 192,236,68\nDL 248,36,77,2048\nMvaa 192,240,72\nEX 183\nMvaa 192,244,76\nCV 197,182\nMvav 64,248,200\nEX 184\nMvav 68,252,200\n"
"Mvvv 72,216,200\nEX 185\nMvvv 76,220,200\nCV 198,184\nMvvv 80,224,200\nEX 186\nMvvv 84,228,200\nMvvv 88,232,200\nEX 187\nMvvv 92,236,200\nCV 199,186\n"
"s_nop 1\nMvav 96,248,196\nEX 188\nMvav 100,252,196\nEX 189\nMvvv 104,216,196\nEX 190\nCV 200,188\nMvvv 108,220,196\nEX 191\nMvvv 112,224,196\nEX 192\n"
"CV 201,190\nMvvv 116,228,196\nEX 193\nMvvv 120,232,196\nEX 194\nCV 202,192\nMvvv 124,236,196\nEX 195\ns_nop 0\nCV 203,194\ns_nop 1\nMvav 128,248,200\n"
"Mvav 132,252,200\nMvvv 136,216,200\nMvvv 140,220,200\nv_smfmac_f32_16x16x64_bf16 a[176:179], a[180:183], v[196:203], v251\nMvvv 144,224,200\n"
"Mvvv 148,228,200\nMvvv 152,232,200\ns_waitcnt vmcnt(21) lgkmcnt(0)\nMvvv 156,236,200\ns_barrier\ns_nop 7\ns_lshl_b32 s88, s83, 12\n"
"s_xor_b32 s89, s88, 0x1000\nv_add_u32 v196, s88, v240\nv_add_u32 v198, s89, v240\nv_add_u32 v197, s88, v241\nv_add_u32 v199, s89, v241\n"
"v_mbcnt_lo_u32_b32 v253, -1, 0\nv_mbcnt_hi_u32_b32 v253, -1, v253\ns_lshl_b32 s88, s83, 14\nv_and_b32 v202, 15, v253\n"
"v_lshl_add_u32 v203, v202, 4, s88\nv_lshrrev_b32 v202, 5, v253\nv_lshl_add_u32 v203, v202, 10, v203\nv_bfe_u32 v253, v253, 4, 1\n"
"v_lshl_add_u32 v203, v253, 12, v203\nv_lshl_add_u32 v202, v253, 3, v203\nRa 0,196,34816\nRa 4,240,36864\nRa 8,198,34816\nRa 20,197,34816\n"
"Ra 24,241,36864\nRa 28,199,34816\nv_add_u32 v196, 0xffff0000, v196\nRa 12,196,32768\nv_add_u32 v198, 0xffff0000, v198\nRa 16,198,32768\n"
"v_add_u32 v197, 0xffff0000, v197\nRa 32,197,32768\nv_add_u32 v199, 0xffff0000, v199\nRa 36,199,32768\nv_add_u32 v202, 0x10000, v202\n"
"Ta 184,202,32768\nTa 186,202,33280\nTa 188,202,34816\nTa 190,202,35328\nTa 192,202,32776\nTa 194,202,33288\nTa 196,202,34824\nTa 198,202,35336\n"
"Ta 200,202,33024\nTa 202,202,33536\nTa 204,202,35072\nTa 206,202,35584\nTa 208,202,33032\nTa 210,202,33544\nTa 212,202,35080\nTa 214,202,35592\n"
"v_accvgpr_read_b32 v160, a160\nv_accvgpr_read_b32 v161, a161\nv_accvgpr_read_b32 v162, a162\nv_accvgpr_read_b32 v163, a163\n"
"v_accvgpr_read_b32 v164, a164\nv_accvgpr_read_b32 v165, a165\nv_accvgpr_read_b32 v166, a166\nv_accvgpr_read_b32 v167, a167\n"
"v_accvgpr_read_b32 v168, a168\nv_accvgpr_read_b32 v169, a169\nv_accvgpr_read_b32 v170, a170\nv_accvgpr_read_b32 v171, a171\n"
"v_accvgpr_read_b32 v172, a172\nv_accvgpr_read_b32 v173, a173\nv_accvgpr_read_b32 v174, a174\nv_accvgpr_read_b32 v175, a175\n"
"v_accvgpr_read_b32 v176, a176\nv_accvgpr_read_b32 v177, a177\nv_accvgpr_read_b32 v178, a178\nv_accvgpr_read_b32 v179, a179\nAD 160,161\nAD 162,163\n"
"AD 164,165\nAD 166,167\nAD 168,169\nAD 170,171\nAD 172,173\nAD 174,175\nAD 176,177\nAD 178,179\nv_add_f32 v180, v160, v170\n"
"v_add_f32 v182, v172, v162\nv_add_f32 v184, v164, v174\nv_add_f32 v186, v176, v166\nv_add_f32 v188, v168, v178\nv_add_f32 v161, v180, v182\n"
"v_add_f32 v162, v184, v186\nAD 161,188\nAD 161,162\nv_rcp_f32 v180, v180\nv_rcp_f32 v182, v182\nv_rcp_f32 v184, v184\nv_rcp_f32 v186, v186\n"
"v_rcp_f32 v188, v188\nv_add_f32 v162, v180, v182\nv_add_f32 v163, v184, v186\nAD 161,162\nAD 163,188\nAD 161,163\nv_cmp_ngt_f32 vcc, s82, v161\n"
"s_nop 1\ns_cmp_lg_u64 vcc, 0\ns_cselect_b32 s79, 1, s79\nPV 0,180\nPV 2,180\nPV 4,180\nPV 6,180\nPV 8,180\nPV 10,180\nPV 12,180\nPV 14,180\n"
"PV 16,180\nPV 18,180\nPV 20,180\nPV 22,180\nPV 24,180\nPV 26,180\nPV 28,180\nPV 30,180\nPV 32,182\nPV 34,182\nPV 36,182\nPV 38,182\nPV 40,182\n"
"PV 42,182\nPV 44,182\nPV 46,182\nPV 48,182\nPV 50,182\nPV 52,182\nPV 54,182\nPV 56,182\nPV 58,182\nPV 60,182\nPV 62,182\nPV 64,184\nPV 66,184\n"
"PV 68,184\nPV 70,184\nPV 72,184\nPV 74,184\nPV 76,184\nPV 78,184\nPV 80,184\nPV 82,184\nPV 84,184\nPV 86,184\nPV 88,184\nPV 90,184\nPV 92,184\n"
"PV 94,184\nPV 96,186\nPV 98,186\nPV 100,186\nPV 102,186\nPV 104,186\nPV 106,186\nPV 108,186\nPV 110,186\nPV 112,186\nPV 114,186\nPV 116,186\n"
"PV 118,186\nPV 120,186\nPV 122,186\nPV 124,186\nPV 126,186\nPV 128,188\nPV 130,188\nPV 132,188\nPV 134,188\nPV 136,188\nPV 138,188\nPV 140,188\n"
"PV 142,188\nPV 144,188\nPV 146,188\nPV 148,188\nPV 150,188\nPV 152,188\nPV 154,188\nPV 156,188\nPV 158,188\nCV 0,0\nCV 1,2\nCV 2,4\nCV 3,6\nAW 80,0\n"
"AW 81,1\nAW 82,2\nAW 83,3\nCV 32,32\nCV 33,34\nCV 34,36\nCV 35,38\nAW 96,32\nAW 97,33\nAW 98,34\nAW 99,35\nCV 64,64\nCV 65,66\nCV 66,68\nCV 67,70\n"
"AW 112,64\nAW 113,65\nAW 114,66\nAW 115,67\nCV 96,96\nCV 97,98\nCV 98,100\nCV 99,102\nAW 128,96\nAW 129,97\nAW 130,98\nAW 131,99\nCV 128,128\n"
"CV 129,130\nCV 130,132\nCV 131,134\nAW 144,128\nAW 145,129\nAW 146,130\nAW 147,131\nCV 8,8\nCV 9,10\nCV 10,12\nCV 11,14\nAW 84,8\nAW 85,9\nAW 86,10\n"
"AW 87,11\nCV 40,40\nCV 41,42\nCV 42,44\nCV 43,46\nAW 100,40\nAW 101,41\nAW 102,42\nAW 103,43\nCV 72,72\nCV 73,74\nCV 74,76\nCV 75,78\nAW 116,72\n"
"AW 117,73\nAW 118,74\nAW 119,75\nCV 104,104\nCV 105,106\nCV 106,108\nCV 107,110\nAW 132,104\n.Lfa_pd15279:\ns_cbranch_execz .Lfa_wr8\nAW 133,105\n"
"AW 134,106\nAW 135,107\nCV 136,136\nCV 137,138\nCV 138,140\nCV 139,142\nAW 148,136\nAW 149,137\nAW 150,138\nAW 151,139\ns_memrealtime s[98:99]\n"
"s_waitcnt vmcnt(15) lgkmcnt(0)\ns_barrier\ns_mov_b32 s93, s98\nZvaa 0,184,0\ns_add_u32 s76, s90, 0x80280\nRa 52,196,34816\nCV 16,16\nMvaa 0,188,20\n"
"s_and_b32 s76, s76, 0x3fffff\nRa 56,198,34816\nCV 17,18\nZvaa 32,184,4\ns_add_u32 m0, s63, 0x18000\nRa 72,197,34816\nv_add_u32 v196, 0x10000, v196\n"
"Mvaa 32,188,24\nDL 242,40,76,0\nZvaa 64,184,8\nRa 76,199,34816\nv_add_u32 v198, 0x10000, v198\nMvaa 64,188,28\nTa 216,203,32768\n"
"v_add_u32 v197, 0x10000, v197\nZvaa 96,184,12\nTa 218,203,33280\nv_add_u32 v199, 0x10000, v199\nMvaa 96,188,32\nDL 243,40,76,1024\nZvaa 128,184,16\n"
"Ta 220,203,34816\nCV 18,20\nMvaa 128,188,36\nTa 222,203,35328\nCV 19,22\nZvaa 4,192,0\nTa 224,203,32776\nAW 88,16\nMvaa 4,196,20\nDL 244,40,76,2048\n"
"Zvaa 36,192,4\nTa 226,203,33288\nAW 89,17\nMvaa 36,196,24\nTa 228,203,34824\nAW 90,18\nZvaa 68,192,8\nTa 230,203,35336\nAW 91,19\nMvaa 68,196,28\n"
"DL 245,40,76,3072\nZvaa 100,192,12\ns_add_u32 s77, s91, 0x100\nTa 232,203,33024\nCV 48,48\nMvaa 100,196,32\ns_and_b32 s77, s77, 0x3ff\n"
"Ta 234,203,33536\nCV 49,50\nZvaa 132,192,16\ns_add_u32 m0, s74, 0x18000\nTa 236,203,35072\nCV 50,52\nMvaa 132,196,36\nDL 249,36,77,0\nZvaa 8,200,0\n"
"Ta 238,203,35584\nCV 51,54\nMvaa 8,204,20\nTa 240,203,33032\nAW 104,48\nZvaa 40,200,4\nTa 242,203,33544\nAW 105,49\nMvaa 40,204,24\n"
"DL 250,36,77,1024\nZvaa 72,200,8\nTa 244,203,35080\nAW 106,50\nMvaa 72,204,28\nTa 246,203,35592\nAW 107,51\nZvaa 104,200,12\nCV 80,80\nCV 81,82\n"
"Mvaa 104,204,32\nCV 82,84\nCV 83,86\nZvaa 136,200,16\nAW 120,80\nAW 121,81\nMvaa 136,204,36\nAW 122,82\nAW 123,83\nZvaa 12,208,0\nCV 112,112\n"
"CV 113,114\nMvaa 12,212,20\nCV 114,116\nCV 115,118\nZvaa 44,208,4\nAW 136,112\nAW 137,113\nMvaa 44,212,24\ns_memrealtime s[98:99]\nAW 138,114\n"
"AW 139,115\nZvaa 76,208,8\nCV 144,144\nCV 145,146\nMvaa 76,212,28\nCV 146,148\nCV 147,150\nZvaa 108,208,12\nAW 152,144\nAW 153,145\nMvaa 108,212,32\n"
"AW 154,146\nAW 155,147\nZvaa 140,208,16\ns_waitcnt vmcnt(6) lgkmcnt(0)\nCV 24,24\nCV 25,26\nMvaa 140,212,36\ns_barrier\n"
"s_sub_u32 s95, s98, s93\ns_mov_b32 s93, s98\ns_sub_u32 s95, 43, s95\ns_cbranch_scc1 .Lfa_t59b\ns_add_u32 s93, s93, s95\n.Lfa_t59a:\ns_sub_u32 s95, s95, 4\ns_cbranch_scc1 .Lfa_t59b\ns_sleep 1\ns_branch .Lfa_t59a\n.Lfa_t59b:\n"
"Zvaa 16,216,0\ns_add_u32 s76, s90, 0x100200\nRa 40,196,2048\nCV 26,28\nMvaa 16,220,20\ns_and_b32 s76, s76, 0x3fffff\nRa 44,240,4096\nCV 27,30\n"
"Zvaa 48,216,4\ns_add_u32 m0, s63, 0x8000\nRa 48,198,2048\nAW 92,24\nMvaa 48,220,24\nDL 242,40,76,0\nZvaa 80,216,8\nRa 60,197,2048\nAW 93,25\n"
"Mvaa 80,220,28\nRa 64,241,4096\nAW 94,26\nZvaa 112,216,12\nRa 68,199,2048\nAW 95,27\nMvaa 112,220,32\nDL 243,40,76,1024\nZvaa 144,216,16\n"
"Ta 248,202,0\nCV 56,56\nMvaa 144,220,36\nTa 250,202,512\nCV 57,58\nZvaa 20,224,0\nTa 252,202,2048\nCV 58,60\nMvaa 20,228,20\nDL 244,40,76,2048\n"
"Zvaa 52,224,4\nTa 254,202,2560\nCV 59,62\nMvaa 52,228,24\nTv 216,202,8\nAW 108,56\nZvaa 84,224,8\nTv 218,202,520\nAW 109,57\nMvaa 84,228,28\n"
"DL 245,40,76,3072\nZvaa 116,224,12\ns_add_u32 s77, s91, 0x100\nTv 220,202,2056\nAW 110,58\nMvaa 116,228,32\ns_and_b32 s77, s77, 0x3ff\n"
"Tv 222,202,2568\nAW 111,59\nZvaa 148,224,16\ns_add_u32 m0, s73, 0x8000\nTv 224,202,256\nCV 88,88\nMvaa 148,228,36\nDL 246,36,77,0\nZvaa 24,232,0\n"
"Tv 226,202,768\nCV 89,90\nMvaa 24,236,20\nTv 228,202,2304\nCV 90,92\nZvaa 56,232,4\nTv 230,202,2816\nCV 91,94\nMvaa 56,236,24\nDL 247,36,77,1024\n"
"AW 124,88\nAW 125,89\nAW 126,90\nAW 127,91\nZvaa 88,232,8\nTv 232,202,264\nCV 120,120\nMvaa 88,236,28\nTv 234,202,776\nCV 121,122\nCV 122,124\n"
"CV 123,126\nAW 140,120\nAW 141,121\nAW 142,122\nAW 143,123\nZvaa 120,232,12\nTv 236,202,2312\nCV 152,152\nMvaa 120,236,32\nDL 248,36,77,2048\n"
"CV 153,154\nCV 154,156\nCV 155,158\nAW 156,152\nAW 157,153\nAW 158,154\nAW 159,155\nZvaa 152,232,16\nTv 238,202,2824\nMvaa 152,236,36\nZvaa 28,240,0\n"
"Mvaa 28,244,20\nZvaa 60,240,4\nMvaa 60,244,24\ns_memrealtime s[98:99]\nZvaa 92,240,8\nMvaa 92,244,28\nZvaa 124,240,12\nMvaa 124,244,32\n"
"Zvaa 156,240,16\ns_waitcnt vmcnt(7) lgkmcnt(0)\nMvaa 156,244,36\ns_barrier\n"
"s_sub_u32 s95, s98, s93\ns_mov_b32 s93, s98\ns_sub_u32 s95, 43, s95\ns_cbranch_scc1 .Lfa_t60b\ns_add_u32 s93, s93, s95\n.Lfa_t60a:\ns_sub_u32 s95, s95, 4\ns_cbranch_scc1 .Lfa_t60b\ns_sleep 1\ns_branch .Lfa_t60a\n.Lfa_t60b:\n"
"Mvaa 0,248,40\ns_add_u32 s76, s90, 0x100280\nRa 12,196,34816\nv_add_u32 v240, 0xffff0000, v240\nMvaa 0,252,60\ns_and_b32 s76, s76, 0x3fffff\n"
"Ra 16,198,34816\nv_add_u32 v241, 0xffff0000, v241\nMvaa 32,248,44\ns_add_u32 m0, s63, 0x10000\nRa 32,197,34816\nv_add_u32 v196, 0xffff0000, v196\n"
"Mvaa 32,252,64\nDL 242,40,76,0\nMvaa 64,248,48\nRa 36,199,34816\nv_add_u32 v198, 0xffff0000, v198\nMvaa 64,252,68\nTa 184,202,32768\n"
"v_add_u32 v197, 0xffff0000, v197\nMvaa 96,248,52\nTa 186,202,33280\nv_add_u32 v199, 0xffff0000, v199\nMvaa 96,252,72\nDL 243,40,76,1024\n"
"Mvaa 128,248,56\nTa 188,202,34816\nMvaa 128,252,76\nTa 190,202,35328\nMvva 4,216,40\nTa 192,202,32776\nMvva 4,220,60\nDL 244,40,76,2048\n"
"Mvva 36,216,44\nTa 194,202,33288\nMvva 36,220,64\nTa 196,202,34824\nMvva 68,216,48\nTa 198,202,35336\nMvva 68,220,68\nDL 245,40,76,3072\n"
"Mvva 100,216,52\ns_add_u32 s77, s91, 0x180\nTa 200,202,33024\nMvva 100,220,72\ns_and_b32 s77, s77, 0x3ff\nTa 202,202,33536\nMvva 132,216,56\n"
"s_add_u32 m0, s74, 0x10000\nTa 204,202,35072\nMvva 132,220,76\nDL 249,36,77,0\nMvva 8,224,40\nTa 206,202,35584\nMvva 8,228,60\nTa 208,202,33032\n"
"Mvva 40,224,44\nTa 210,202,33544\nMvva 40,228,64\nDL 250,36,77,1024\nMvva 72,224,48\nTa 212,202,35080\nMvva 72,228,68\nTa 214,202,35592\n"
"Mvva 104,224,52\nMvva 104,228,72\nv_add_u32 v202, 0xffff0000, v202\nMvva 136,224,56\nMvva 136,228,76\nMvva 12,232,40\nMvva 12,236,60\nMvva 44,232,44\n"
"Mvva 44,236,64\ns_memrealtime s[98:99]\nMvva 76,232,48\nMvva 76,236,68\nMvva 108,232,52\nMvva 108,236,72\nMvva 140,232,56\n"
"s_waitcnt vmcnt(6) lgkmcnt(0)\nMvva 140,236,76\ns_barrier\n"
"s_sub_u32 s95, s98, s93\ns_mov_b32 s93, s98\ns_sub_u32 s95, 43, s95\ns_cbranch_scc1 .Lfa_t61b\ns_add_u32 s93, s93, s95\n.Lfa_t61a:\ns_sub_u32 s95, s95, 4\ns_cbranch_scc1 .Lfa_t61b\ns_sleep 1\ns_branch .Lfa_t61a\n.Lfa_t61b:\n"
"Mvaa 16,184,40\ns_add_u32 s76, s90, 0x180200\nRa 0,196,34816\nMvaa 16,188,60\ns_and_b32 s76, s76, 0x3fffff\nRa 4,240,36864\nMvaa 48,184,44\n"
"s_add_u32 m0, s63, 0x18000\nRa 8,198,34816\nv_add_u32 v196, 0x10000, v196\nMvaa 48,188,64\nDL 242,40,76,0\nMvaa 80,184,48\nRa 20,197,34816\n"
"v_add_u32 v198, 0x10000, v198\nMvaa 80,188,68\nRa 24,241,36864\nMvaa 112,184,52\nRa 28,199,34816\nv_add_u32 v197, 0x10000, v197\nMvaa 112,188,72\n"
"DL 243,40,76,1024\nMvaa 144,184,56\nTa 216,202,32768\nv_add_u32 v199, 0x10000, v199\nMvaa 144,188,76\nTa 218,202,33280\nMvaa 20,192,40\n"
"Ta 220,202,34816\nMvaa 20,196,60\nDL 244,40,76,2048\nMvaa 52,192,44\nTa 222,202,35328\nMvaa 52,196,64\nTa 224,202,32776\nMvaa 84,192,48\n"
"Ta 226,202,33288\nMvaa 84,196,68\nDL 245,40,76,3072\nMvaa 116,192,52\ns_add_u32 s77, s91, 0x180\nTa 228,202,34824\nMvaa 116,196,72\n"
"s_and_b32 s77, s77, 0x3ff\nTa 230,202,35336\nMvaa 148,192,56\ns_add_u32 m0, s73, 0x18000\nTa 232,202,33024\nMvaa 148,196,76\nDL 246,36,77,0\n"
"Mvaa 24,200,40\nTa 234,202,33536\nMvaa 24,204,60\nTa 236,202,35072\nMvaa 56,200,44\nTa 238,202,35584\nMvaa 56,204,64\nDL 247,36,77,1024\n"
"Mvaa 88,200,48\nTa 240,202,33032\nMvaa 88,204,68\nTa 242,202,33544\nMvaa 120,200,52\nTa 244,202,35080\nMvaa 120,204,72\nDL 248,36,77,2048\n"
"Mvaa 152,200,56\nTa 246,202,35592\nMvaa 152,204,76\nMvaa 28,208,40\nv_add_u32 v202, 0x10000, v202\nMvaa 28,212,60\nMvaa 60,208,44\nMvaa 60,212,64\n"
"s_memrealtime s[98:99]\nMvaa 92,208,48\nMvaa 92,212,68\nMvaa 124,208,52\nMvaa 124,212,72\nMvaa 156,208,56\ns_waitcnt vmcnt(7) lgkmcnt(0)\n"
"Mvaa 156,212,76\ns_barrier\n"
"s_sub_u32 s95, s98, s93\ns_mov_b32 s93, s98\ns_sub_u32 s95, 43, s95\ns_cbranch_scc1 .Lfa_t62b\ns_add_u32 s93, s93, s95\n.Lfa_t62a:\ns_sub_u32 s95, s95, 4\ns_cbranch_scc1 .Lfa_t62b\ns_sleep 1\ns_branch .Lfa_t62a\n.Lfa_t62b:\n"
"Mvaa 0,216,0\ns_add_u32 s76, s90, 0x180280\nRa 52,196,2048\nv_add_u32 v240, 0x10000, v240\nMvaa 0,220,20\ns_and_b32 s76, s76, 0x3fffff\n"
"Ra 56,198,2048\nv_add_u32 v241, 0x10000, v241\nMvaa 32,216,4\ns_add_u32 m0, s63, 0x8000\nRa 72,197,2048\nMvaa 32,220,24\nDL 242,40,76,0\n"
"Mvaa 64,216,8\nRa 76,199,2048\nMvaa 64,220,28\nTa 248,202,0\nMvaa 96,216,12\nTa 250,202,512\nMvaa 96,220,32\nDL 243,40,76,1024\nMvaa 128,216,16\n"
"Ta 252,202,2048\nMvaa 128,220,36\nTa 254,202,2560\nMvaa 4,224,0\nTv 216,202,8\nMvaa 4,228,20\nDL 244,40,76,2048\nMvaa 36,224,4\nTv 218,202,520\n"
"Mvaa 36,228,24\nTv 220,202,2056\nMvaa 68,224,8\nTv 222,202,2568\nMvaa 68,228,28\nDL 245,40,76,3072\nMvaa 100,224,12\ns_add_u32 s77, s91, 0x200\n"
"Tv 224,202,256\nMvaa 100,228,32\ns_and_b32 s77, s77, 0x3ff\nTv 226,202,768\nMvaa 132,224,16\ns_add_u32 m0, s74, 0x8000\nTv 228,202,2304\n"
"Mvaa 132,228,36\nDL 249,36,77,0\nMvaa 8,232,0\nTv 230,202,2816\nMvaa 8,236,20\nTv 232,202,264\nMvaa 40,232,4\nTv 234,202,776\nMvaa 40,236,24\n"
"DL 250,36,77,1024\nMvaa 72,232,8\nTv 236,202,2312\nMvaa 72,236,28\nTv 238,202,2824\nMvaa 104,232,12\nMvaa 104,236,32\n"
"buffer_load_dword v252, v210, s[68:71], s96 offen offset:0\nMvaa 136,232,16\nMvaa 136,236,36\nMvaa 12,240,0\nMvaa 12,244,20\n"
"buffer_load_dword v252, v210, s[68:71], s96 offen offset:1024\nMvaa 44,240,4\nMvaa 44,244,24\ns_memrealtime s[98:99]\nMvaa 76,240,8\nMvaa 76,244,28\n"
"Mvaa 108,240,12\nMvaa 108,244,32\nMvaa 140,240,16\ns_waitcnt vmcnt(8) lgkmcnt(0)\nMvaa 140,244,36\ns_barrier\n"
"s_sub_u32 s95, s98, s93\ns_mov_b32 s93, s98\ns_sub_u32 s95, 43, s95\ns_cbranch_scc1 .Lfa_t63b\ns_add_u32 s93, s93, s95\n.Lfa_t63a:\ns_sub_u32 s95, s95, 4\ns_cbranch_scc1 .Lfa_t63b\ns_sleep 1\ns_branch .Lfa_t63a\n.Lfa_t63b:\n"
".Lfa_pd15781:\ns_cbranch_execz .Lfa_wr8\nMvaa 16,248,0\ns_add_u32 s76, s90, 0x200200\nRa 40,196,34816\nMvaa 16,252,20\ns_and_b32 s76, s76, 0x3fffff\n"
"Ra 44,240,36864\nMvaa 48,248,4\ns_add_u32 m0, s63, 0x10000\nRa 48,198,34816\nv_add_u32 v196, 0xffff0000, v196\nMvaa 48,252,24\nDL 242,40,76,0\n"
"Mvaa 80,248,8\nRa 60,197,34816\nv_add_u32 v198, 0xffff0000, v198\nMvaa 80,252,28\nRa 64,241,36864\nMvaa 112,248,12\nRa 68,199,34816\n"
"v_add_u32 v197, 0xffff0000, v197\nMvaa 112,252,32\nDL 243,40,76,1024\nMvaa 144,248,16\nTa 184,202,32768\nv_add_u32 v199, 0xffff0000, v199\n"
"Mvaa 144,252,36\nTa 186,202,33280\nMvva 20,216,0\nTa 188,202,34816\nMvva 20,220,20\nDL 244,40,76,2048\nMvva 52,216,4\nTa 190,202,35328\n"
"Mvva 52,220,24\nTa 192,202,32776\nMvva 84,216,8\nTa 194,202,33288\nMvva 84,220,28\nDL 245,40,76,3072\nMvva 116,216,12\ns_add_u32 s77, s91, 0x200\n"
"Ta 196,202,34824\nMvva 116,220,32\ns_and_b32 s77, s77, 0x3ff\nTa 198,202,35336\nMvva 148,216,16\ns_add_u32 m0, s73, 0x10000\nTa 200,202,33024\n"
"Mvva 148,220,36\nDL 246,36,77,0\nMvva 24,224,0\nTa 202,202,33536\nMvva 24,228,20\nTa 204,202,35072\nMvva 56,224,4\nTa 206,202,35584\nMvva 56,228,24\n"
"DL 247,36,77,1024\nMvva 88,224,8\nTa 208,202,33032\nMvva 88,228,28\nTa 210,202,33544\nMvva 120,224,12\nTa 212,202,35080\nMvva 120,228,32\n"
"DL 248,36,77,2048\nMvva 152,224,16\nTa 214,202,35592\nMvva 152,228,36\nMvva 28,232,0\nv_add_u32 v202, 0xffff0000, v202\nMvva 28,236,20\n"
"Mvva 60,232,4\nMvva 60,236,24\ns_memrealtime s[98:99]\nMvva 92,232,8\nMvva 92,236,28\nMvva 124,232,12\nMvva 124,236,32\nMvva 156,232,16\n"
"s_waitcnt vmcnt(9) lgkmcnt(0)\nMvva 156,236,36\ns_barrier\n"
"s_sub_u32 s95, s98, s93\ns_mov_b32 s93, s98\ns_sub_u32 s95, 43, s95\ns_cbranch_scc1 .Lfa_t64b\ns_add_u32 s93, s93, s95\n.Lfa_t64a:\ns_sub_u32 s95, s95, 4\ns_cbranch_scc1 .Lfa_t64b\ns_sleep 1\ns_branch .Lfa_t64a\n.Lfa_t64b:\n"
"Mvaa 0,184,40\ns_add_u32 s76, s90, 0x200280\nRa 12,196,34816\nMvaa 0,188,60\ns_and_b32 s76, s76, 0x3fffff\nRa 16,198,34816\nMvaa 32,184,44\n"
"s_add_u32 m0, s63, 0x18000\nRa 32,197,34816\nv_add_u32 v196, 0x10000, v196\nMvaa 32,188,64\nDL 242,40,76,0\nMvaa 64,184,48\nRa 36,199,34816\n"
"v_add_u32 v198, 0x10000, v198\nMvaa 64,188,68\nTa 216,202,32768\nv_add_u32 v197, 0x10000, v197\nMvaa 96,184,52\nTa 218,202,33280\n"
"v_add_u32 v199, 0x10000, v199\nMvaa 96,188,72\nDL 243,40,76,1024\nMvaa 128,184,56\nTa 220,202,34816\nMvaa 128,188,76\nTa 222,202,35328\n"
"Mvaa 4,192,40\nTa 224,202,32776\nMvaa 4,196,60\nDL 244,40,76,2048\nMvaa 36,192,44\nTa 226,202,33288\nMvaa 36,196,64\nTa 228,202,34824\n"
"Mvaa 68,192,48\nTa 230,202,35336\nMvaa 68,196,68\nDL 245,40,76,3072\nMvaa 100,192,52\ns_add_u32 s77, s91, 0x280\nTa 232,202,33024\nMvaa 100,196,72\n"
"s_and_b32 s77, s77, 0x3ff\nTa 234,202,33536\nMvaa 132,192,56\ns_add_u32 m0, s74, 0x18000\nTa 236,202,35072\nMvaa 132,196,76\nDL 249,36,77,0\n"
"Mvaa 8,200,40\nTa 238,202,35584\nMvaa 8,204,60\nTa 240,202,33032\nMvaa 40,200,44\nTa 242,202,33544\nMvaa 40,204,64\nDL 250,36,77,1024\n"
"Mvaa 72,200,48\nTa 244,202,35080\nMvaa 72,204,68\nTa 246,202,35592\nMvaa 104,200,52\nMvaa 104,204,72\nv_add_u32 v202, 0x10000, v202\nMvaa 136,200,56\n"
"Mvaa 136,204,76\nMvaa 12,208,40\nMvaa 12,212,60\nMvaa 44,208,44\nMvaa 44,212,64\ns_memrealtime s[98:99]\nMvaa 76,208,48\nMvaa 76,212,68\n"
"Mvaa 108,208,52\nMvaa 108,212,72\nMvaa 140,208,56\ns_waitcnt vmcnt(6) lgkmcnt(0)\nMvaa 140,212,76\ns_barrier\n"
"s_sub_u32 s95, s98, s93\ns_mov_b32 s93, s98\ns_sub_u32 s95, 43, s95\ns_cbranch_scc1 .Lfa_t65b\ns_add_u32 s93, s93, s95\n.Lfa_t65a:\ns_sub_u32 s95, s95, 4\ns_cbranch_scc1 .Lfa_t65b\ns_sleep 1\ns_branch .Lfa_t65a\n.Lfa_t65b:\n"
"Mvaa 16,216,40\ns_add_u32 s76, s90, 0x280200\nRa 0,196,2048\nMvaa 16,220,60\ns_and_b32 s76, s76, 0x3fffff\nRa 4,240,4096\nMvaa 48,216,44\n"
"s_add_u32 m0, s63, 0x8000\nRa 8,198,2048\nMvaa 48,220,64\nDL 242,40,76,0\nMvaa 80,216,48\nRa 20,197,2048\nMvaa 80,220,68\nRa 24,241,4096\n"
"Mvaa 112,216,52\nRa 28,199,2048\nMvaa 112,220,72\nDL 243,40,76,1024\nMvaa 144,216,56\nTa 248,202,0\nMvaa 144,220,76\nTa 250,202,512\nMvaa 20,224,40\n"
"Ta 252,202,2048\nMvaa 20,228,60\nDL 244,40,76,2048\nMvaa 52,224,44\nTa 254,202,2560\nMvaa 52,228,64\nTv 216,202,8\nMvaa 84,224,48\nTv 218,202,520\n"
"Mvaa 84,228,68\nDL 245,40,76,3072\nMvaa 116,224,52\ns_add_u32 s77, s91, 0x280\nTv 220,202,2056\nMvaa 116,228,72\ns_and_b32 s77, s77, 0x3ff\n"
"Tv 222,202,2568\nMvaa 148,224,56\ns_add_u32 m0, s73, 0x8000\nTv 224,202,256\nMvaa 148,228,76\nDL 246,36,77,0\nMvaa 24,232,40\nTv 226,202,768\n"
"Mvaa 24,236,60\nTv 228,202,2304\nMvaa 56,232,44\nTv 230,202,2816\nMvaa 56,236,64\nDL 247,36,77,1024\nMvaa 88,232,48\nTv 232,202,264\nMvaa 88,236,68\n"
"Tv 234,202,776\nMvaa 120,232,52\nTv 236,202,2312\nMvaa 120,236,72\nDL 248,36,77,2048\nMvaa 152,232,56\nTv 238,202,2824\nMvaa 152,236,76\n"
"Mvaa 28,240,40\nMvaa 28,244,60\nMvaa 60,240,44\nMvaa 60,244,64\ns_memrealtime s[98:99]\nMvaa 92,240,48\nMvaa 92,244,68\nMvaa 124,240,52\n"
"Mvaa 124,244,72\nMvaa 156,240,56\ns_waitcnt vmcnt(7) lgkmcnt(0)\nMvaa 156,244,76\ns_barrier\n"
"s_sub_u32 s95, s98, s93\ns_mov_b32 s93, s98\ns_sub_u32 s95, 43, s95\ns_cbranch_scc1 .Lfa_t66b\ns_add_u32 s93, s93, s95\n.Lfa_t66a:\ns_sub_u32 s95, s95, 4\ns_cbranch_scc1 .Lfa_t66b\ns_sleep 1\ns_branch .Lfa_t66a\n.Lfa_t66b:\n"
"Mvaa 0,248,0\ns_add_u32 s76, s90, 0x280280\nRa 52,196,34816\nv_add_u32 v240, 0xffff0000, v240\nMvaa 0,252,20\ns_and_b32 s76, s76, 0x3fffff\n"
"Ra 56,198,34816\nv_add_u32 v241, 0xffff0000, v241\nMvaa 32,248,4\ns_add_u32 m0, s63, 0x10000\nRa 72,197,34816\nv_add_u32 v196, 0xffff0000, v196\n"
"Mvaa 32,252,24\nDL 242,40,76,0\nMvaa 64,248,8\nRa 76,199,34816\nv_add_u32 v198, 0xffff0000, v198\nMvaa 64,252,28\nTa 184,202,32768\n"
"v_add_u32 v197, 0xffff0000, v197\nMvaa 96,248,12\nTa 186,202,33280\nv_add_u32 v199, 0xffff0000, v199\nMvaa 96,252,32\nDL 243,40,76,1024\n"
"Mvaa 128,248,16\nTa 188,202,34816\nMvaa 128,252,36\nTa 190,202,35328\nMvva 4,216,0\nTa 192,202,32776\nMvva 4,220,20\nDL 244,40,76,2048\n"
"Mvva 36,216,4\nTa 194,202,33288\nMvva 36,220,24\nTa 196,202,34824\nMvva 68,216,8\nTa 198,202,35336\nMvva 68,220,28\nDL 245,40,76,3072\n"
"Mvva 100,216,12\ns_add_u32 s77, s91, 0x300\nTa 200,202,33024\nMvva 100,220,32\ns_and_b32 s77, s77, 0x3ff\nTa 202,202,33536\nMvva 132,216,16\n"
"s_add_u32 m0, s74, 0x10000\nTa 204,202,35072\nMvva 132,220,36\nDL 249,36,77,0\nMvva 8,224,0\nTa 206,202,35584\nMvva 8,228,20\nTa 208,202,33032\n"
"Mvva 40,224,4\nTa 210,202,33544\nMvva 40,228,24\nDL 250,36,77,1024\nMvva 72,224,8\nTa 212,202,35080\nMvva 72,228,28\nTa 214,202,35592\n"
"Mvva 104,224,12\nMvva 104,228,32\nv_add_u32 v202, 0xffff0000, v202\nMvva 136,224,16\nMvva 136,228,36\nMvva 12,232,0\nMvva 12,236,20\nMvva 44,232,4\n"
"Mvva 44,236,24\ns_memrealtime s[98:99]\nMvva 76,232,8\nMvva 76,236,28\nMvva 108,232,12\nMvva 108,236,32\nMvva 140,232,16\n"
"s_waitcnt vmcnt(6) lgkmcnt(0)\nMvva 140,236,36\ns_barrier\n"
"s_sub_u32 s95, s98, s93\ns_mov_b32 s93, s98\ns_sub_u32 s95, 43, s95\ns_cbranch_scc1 .Lfa_t67b\ns_add_u32 s93, s93, s95\n.Lfa_t67a:\ns_sub_u32 s95, s95, 4\ns_cbranch_scc1 .Lfa_t67b\ns_sleep 1\ns_branch .Lfa_t67a\n.Lfa_t67b:\n"
"Mvaa 16,184,0\ns_add_u32 s76, s90, 0x300200\nRa 40,196,34816\nMvaa 16,188,20\ns_and_b32 s76, s76, 0x3fffff\nRa 44,240,36864\nMvaa 48,184,4\n"
"s_add_u32 m0, s63, 0x18000\nRa 48,198,34816\nv_add_u32 v196, 0x10000, v196\nMvaa 48,188,24\nDL 242,40,76,0\nMvaa 80,184,8\nRa 60,197,34816\n"
"v_add_u32 v198, 0x10000, v198\nMvaa 80,188,28\nRa 64,241,36864\nMvaa 112,184,12\nRa 68,199,34816\nv_add_u32 v197, 0x10000, v197\nMvaa 112,188,32\n"
"DL 243,40,76,1024\nMvaa 144,184,16\nTa 216,202,32768\nv_add_u32 v199, 0x10000, v199\nMvaa 144,188,36\nTa 218,202,33280\nMvaa 20,192,0\n"
"Ta 220,202,34816\nMvaa 20,196,20\nDL 244,40,76,2048\nMvaa 52,192,4\nTa 222,202,35328\nMvaa 52,196,24\nTa 224,202,32776\nMvaa 84,192,8\n"
"Ta 226,202,33288\nMvaa 84,196,28\nDL 245,40,76,3072\nMvaa 116,192,12\ns_add_u32 s77, s91, 0x300\nTa 228,202,34824\nMvaa 116,196,32\n"
"s_and_b32 s77, s77, 0x3ff\nTa 230,202,35336\nMvaa 148,192,16\ns_add_u32 m0, s73, 0x18000\nTa 232,202,33024\nMvaa 148,196,36\nDL 246,36,77,0\n"
"Mvaa 24,200,0\nTa 234,202,33536\nMvaa 24,204,20\nTa 236,202,35072\nMvaa 56,200,4\nTa 238,202,35584\nMvaa 56,204,24\nDL 247,36,77,1024\nMvaa 88,200,8\n"
"Ta 240,202,33032\nMvaa 88,204,28\nTa 242,202,33544\nMvaa 120,200,12\nTa 244,202,35080\nMvaa 120,204,32\nDL 248,36,77,2048\nMvaa 152,200,16\n"
"Ta 246,202,35592\nMvaa 152,204,36\nMvaa 28,208,0\nv_add_u32 v202, 0x10000, v202\nMvaa 28,212,20\nMvaa 60,208,4\nMvaa 60,212,24\n"
"s_memrealtime s[98:99]\nMvaa 92,208,8\nMvaa 92,212,28\nMvaa 124,208,12\nMvaa 124,212,32\nMvaa 156,208,16\ns_waitcnt vmcnt(7) lgkmcnt(0)\n"
"Mvaa 156,212,36\ns_barrier\n"
"s_sub_u32 s95, s98, s93\ns_mov_b32 s93, s98\ns_sub_u32 s95, 43, s95\ns_cbranch_scc1 .Lfa_t68b\ns_add_u32 s93, s93, s95\n.Lfa_t68a:\ns_sub_u32 s95, s95, 4\ns_cbranch_scc1 .Lfa_t68b\ns_sleep 1\ns_branch .Lfa_t68a\n.Lfa_t68b:\n"
"Mvaa 0,216,40\ns_add_u32 s76, s90, 0x300280\nRa 12,196,2048\nv_add_u32 v240, 0x10000, v240\nMvaa 0,220,60\ns_and_b32 s76, s76, 0x3fffff\n"
"Ra 16,198,2048\nv_add_u32 v241, 0x10000, v241\nMvaa 32,216,44\ns_add_u32 m0, s63, 0x8000\nRa 32,197,2048\nv_mbcnt_lo_u32_b32 v253, -1, 0\n"
"Mvaa 32,220,64\nDL 242,40,76,0\nMvaa 64,216,48\nRa 36,199,2048\nv_mbcnt_hi_u32_b32 v253, -1, v253\nMvaa 64,220,68\ns_lshl_b32 s88, s83, 14\n"
"Ta 248,202,0\nMvaa 96,216,52\ns_add_u32 s88, s88, 0x20000\nTa 250,202,512\nv_and_b32 v253, 48, v253\nMvaa 96,220,72\nDL 243,40,76,1024\n"
"Mvaa 128,216,56\nTa 252,202,2048\nv_add_u32 v253, s88, v253\nMvaa 128,220,76\nTa 254,202,2560\nMvaa 4,224,40\nTv 216,202,8\nMvaa 4,228,60\n"
"DL 244,40,76,2048\nMvaa 36,224,44\nTv 218,202,520\nMvaa 36,228,64\nTv 220,202,2056\nMvaa 68,224,48\nTv 222,202,2568\nMvaa 68,228,68\n"
"DL 245,40,76,3072\nMvaa 100,224,52\ns_add_u32 s77, s91, 0x380\nTv 224,202,256\nMvaa 100,228,72\ns_and_b32 s77, s77, 0x3ff\nTv 226,202,768\n"
"Mvaa 132,224,56\ns_add_u32 m0, s74, 0x8000\nTv 228,202,2304\nMvaa 132,228,76\nDL 249,36,77,0\nMvaa 8,232,40\nTv 230,202,2816\nMvaa 8,236,60\n"
"Tv 232,202,264\nMvaa 40,232,44\nTv 234,202,776\nMvaa 40,236,64\nDL 250,36,77,1024\nMvaa 72,232,48\nTv 236,202,2312\nMvaa 72,236,68\nTv 238,202,2824\n"
"Mvaa 104,232,52\nMvaa 104,236,72\nMvaa 136,232,56\nMvaa 136,236,76\nMvaa 12,240,40\nMvaa 12,244,60\nMvaa 44,240,44\nMvaa 44,244,64\n"
"s_memrealtime s[98:99]\nMvaa 76,240,48\nMvaa 76,244,68\nMvaa 108,240,52\nMvaa 108,244,72\nMvaa 140,240,56\ns_waitcnt vmcnt(6) lgkmcnt(0)\n"
"Mvaa 140,244,76\ns_barrier\n"
"s_sub_u32 s95, s98, s93\ns_mov_b32 s93, s98\ns_sub_u32 s95, 43, s95\ns_cbranch_scc1 .Lfa_t69b\ns_add_u32 s93, s93, s95\n.Lfa_t69a:\ns_sub_u32 s95, s95, 4\ns_cbranch_scc1 .Lfa_t69b\ns_sleep 1\ns_branch .Lfa_t69a\n.Lfa_t69b:\n"
"Mvaa 16,248,40\ns_add_u32 s76, s90, 0x380200\n.Lfa_pd16279:\ns_cbranch_execz .Lfa_wr8\nRa 0,196,34816\nMvaa 16,252,60\ns_and_b32 s76, s76, 0x3fffff\n"
"Ra 4,240,36864\nMvaa 48,248,44\ns_add_u32 m0, s63, 0x10000\nRa 8,198,34816\nv_add_u32 v196, 0xffff0000, v196\nMvaa 48,252,64\nDL 242,40,76,0\n"
"Mvaa 80,248,48\nRa 20,197,34816\nv_add_u32 v198, 0xffff0000, v198\nMvaa 80,252,68\nRa 24,241,36864\nMvaa 112,248,52\nRa 28,199,34816\n"
"v_add_u32 v197, 0xffff0000, v197\nMvaa 112,252,72\nDL 243,40,76,1024\nMvaa 144,248,56\nTa 184,202,32768\nv_add_u32 v199, 0xffff0000, v199\n"
"Mvaa 144,252,76\nTa 186,202,33280\nMvva 20,216,40\nTa 188,202,34816\nMvva 20,220,60\nDL 244,40,76,2048\nMvva 52,216,44\nTa 190,202,35328\n"
"Mvva 52,220,64\nTa 192,202,32776\nMvva 84,216,48\nTa 194,202,33288\nMvva 84,220,68\nDL 245,40,76,3072\nMvva 116,216,52\ns_add_u32 s77, s91, 0x380\n"
"Ta 196,202,34824\nMvva 116,220,72\ns_and_b32 s77, s77, 0x3ff\nTa 198,202,35336\nMvva 148,216,56\ns_add_u32 m0, s73, 0x10000\nTa 200,202,33024\n"
"Mvva 148,220,76\nDL 246,36,77,0\nMvva 24,224,40\nTa 202,202,33536\nMvva 24,228,60\nTa 204,202,35072\nMvva 56,224,44\nTa 206,202,35584\n"
"Mvva 56,228,64\nDL 247,36,77,1024\nMvva 88,224,48\nTa 208,202,33032\nMvva 88,228,68\nTa 210,202,33544\nMvva 120,224,52\nTa 212,202,35080\n"
"Mvva 120,228,72\nDL 248,36,77,2048\nMvva 152,224,56\nTa 214,202,35592\nMvva 152,228,76\nMvva 28,232,40\nv_add_u32 v202, 0xffff0000, v202\n"
"Mvva 28,236,60\nMvva 60,232,44\nMvva 60,236,64\ns_memrealtime s[98:99]\nMvva 92,232,48\nMvva 92,236,68\nMvva 124,232,52\nMvva 124,236,72\n"
"Mvva 156,232,56\ns_waitcnt vmcnt(7) lgkmcnt(0)\nMvva 156,236,76\ns_barrier\n"
"s_sub_u32 s95, s98, s93\ns_mov_b32 s93, s98\ns_sub_u32 s95, 43, s95\ns_cbranch_scc1 .Lfa_t70b\ns_add_u32 s93, s93, s95\n.Lfa_t70a:\ns_sub_u32 s95, s95, 4\ns_cbranch_scc1 .Lfa_t70b\ns_sleep 1\ns_branch .Lfa_t70a\n.Lfa_t70b:\n"
"Mvaa 0,184,0\ns_add_u32 s76, s90, 0x380280\nRa 52,196,34816\nMvaa 0,188,20\ns_and_b32 s76, s76, 0x3fffff\nRa 56,198,34816\nMvaa 32,184,4\n"
"s_add_u32 m0, s63, 0x18000\nRa 72,197,34816\nv_add_u32 v196, 0x10000, v196\nMvaa 32,188,24\nDL 242,40,76,0\nMvaa 64,184,8\nRa 76,199,34816\n"
"v_add_u32 v198, 0x10000, v198\nMvaa 64,188,28\nTa 216,202,32768\nv_add_u32 v197, 0x10000, v197\nMvaa 96,184,12\nTa 218,202,33280\n"
"v_add_u32 v199, 0x10000, v199\nMvaa 96,188,32\nDL 243,40,76,1024\nMvaa 128,184,16\nTa 220,202,34816\nMvaa 128,188,36\nTa 222,202,35328\nMvaa 4,192,0\n"
"Ta 224,202,32776\nMvaa 4,196,20\nDL 244,40,76,2048\nMvaa 36,192,4\nTa 226,202,33288\nMvaa 36,196,24\nTa 228,202,34824\nMvaa 68,192,8\n"
"Ta 230,202,35336\nMvaa 68,196,28\nDL 245,40,76,3072\nMvaa 100,192,12\nTa 232,202,33024\nMvaa 100,196,32\nTa 234,202,33536\nMvaa 132,192,16\n"
"Ta 236,202,35072\nMvaa 132,196,36\nTa 238,202,35584\nMvaa 8,200,0\nTa 240,202,33032\nMvaa 8,204,20\nTa 242,202,33544\nMvaa 40,200,4\n"
"Ta 244,202,35080\nMvaa 40,204,24\nTa 246,202,35592\nMvaa 72,200,8\nMvaa 72,204,28\nv_add_u32 v202, 0x10000, v202\nMvaa 104,200,12\nMvaa 104,204,32\n"
"Mvaa 136,200,16\nMvaa 136,204,36\nMvaa 12,208,0\nMvaa 12,212,20\nMvaa 44,208,4\nMvaa 44,212,24\ns_memrealtime s[98:99]\nMvaa 76,208,8\n"
"Mvaa 76,212,28\nMvaa 108,208,12\nMvaa 108,212,32\nMvaa 140,208,16\ns_waitcnt vmcnt(4) lgkmcnt(0)\nMvaa 140,212,36\ns_barrier\n"
"s_sub_u32 s95, s98, s93\ns_mov_b32 s93, s98\ns_sub_u32 s95, 43, s95\ns_cbranch_scc1 .Lfa_t71b\ns_add_u32 s93, s93, s95\n.Lfa_t71a:\ns_sub_u32 s95, s95, 4\ns_cbranch_scc1 .Lfa_t71b\ns_sleep 1\ns_branch .Lfa_t71a\n.Lfa_t71b:\n"
"Mvaa 16,216,0\ns_add_u32 s75, s97, 0x4000\nRa 40,196,2048\nMvaa 16,220,20\ns_and_b32 s75, s75, 0x1ffff\nRa 44,240,4096\nMvaa 48,216,4\n"
"s_add_u32 s75, s75, 0x40000\nRa 48,198,2048\nMvaa 48,220,24\ns_add_u32 m0, s72, 0x8000\nRa 60,197,2048\nMvaa 80,216,8\nDtL 210,44,75,0\n"
"Mvaa 80,220,28\nRa 64,241,4096\nMvaa 112,216,12\nRa 68,199,2048\nMvaa 112,220,32\nTa 248,202,0\nMvaa 144,216,16\nDtL 211,44,75,1024\nMvaa 144,220,36\n"
"Ta 250,202,512\nMvaa 20,224,0\nTa 252,202,2048\nMvaa 20,228,20\nTa 254,202,2560\nMvaa 52,224,4\nDtL 212,44,75,2048\nMvaa 52,228,24\nTv 216,202,8\n"
"Mvaa 84,224,8\nTv 218,202,520\nMvaa 84,228,28\nTv 220,202,2056\nMvaa 116,224,12\nDtL 213,44,75,3072\nMvaa 116,228,32\nTv 222,202,2568\n"
"Mvaa 148,224,16\nTv 224,202,256\nMvaa 148,228,36\nTv 226,202,768\nMvaa 24,232,0\nTv 228,202,2304\nMvaa 24,236,20\nTv 230,202,2816\nMvaa 56,232,4\n"
"Tv 232,202,264\nMvaa 56,236,24\nTv 234,202,776\nMvaa 88,232,8\nTv 236,202,2312\nMvaa 88,236,28\nTv 238,202,2824\nMvaa 120,232,12\nMvaa 120,236,32\n"
"Mvaa 152,232,16\nMvaa 152,236,36\nMvaa 28,240,0\nMvaa 28,244,20\nMvaa 60,240,4\nMvaa 60,244,24\ns_memrealtime s[98:99]\nMvaa 92,240,8\n"
"Mvaa 92,244,28\nMvaa 124,240,12\nMvaa 124,244,32\nMvaa 156,240,16\ns_waitcnt vmcnt(4) lgkmcnt(0)\nMvaa 156,244,36\ns_barrier\n"
"s_sub_u32 s95, s98, s93\ns_mov_b32 s93, s98\ns_sub_u32 s95, 43, s95\ns_cbranch_scc1 .Lfa_t72b\ns_add_u32 s93, s93, s95\n.Lfa_t72a:\ns_sub_u32 s95, s95, 4\ns_cbranch_scc1 .Lfa_t72b\ns_sleep 1\ns_branch .Lfa_t72a\n.Lfa_t72b:\n"
"Mvaa 0,248,40\nTa 184,202,32768\nv_add_u32 v204, 0x10000, v204\nMvaa 0,252,60\ns_add_u32 m0, s72, 0xa000\nTa 186,202,33280\n"
"v_add_u32 v205, 0x10000, v205\nMvaa 32,248,44\nDL 214,48,75,0\nMvaa 32,252,64\nTa 188,202,34816\nv_add_u32 v206, 0x10000, v206\nMvaa 64,248,48\n"
"Ta 190,202,35328\nv_add_u32 v207, 0x10000, v207\nMvaa 64,252,68\nTa 192,202,32776\nv_add_u32 v208, 0x10000, v208\nMvaa 96,248,52\nDL 215,48,75,1024\n"
"Mvaa 96,252,72\nTa 194,202,33288\nv_add_u32 v209, 0x10000, v209\nMvaa 128,248,56\nTa 196,202,34824\nv_accvgpr_write_b32 a160, 0\nMvaa 128,252,76\n"
"Ta 198,202,35336\nv_accvgpr_write_b32 a161, 0\nMvva 4,216,40\nDL 214,48,75,2048\nMvva 4,220,60\nTa 200,202,33024\nv_accvgpr_write_b32 a162, 0\n"
"Mvva 36,216,44\nTa 202,202,33536\nv_accvgpr_write_b32 a163, 0\nMvva 36,220,64\nTa 204,202,35072\nv_accvgpr_write_b32 a164, 0\nMvva 68,216,48\n"
"DL 215,48,75,3072\nMvva 68,220,68\nTa 206,202,35584\nv_accvgpr_write_b32 a165, 0\nMvva 100,216,52\nTa 208,202,33032\nv_accvgpr_write_b32 a166, 0\n"
"Mvva 100,220,72\nTa 210,202,33544\nv_accvgpr_write_b32 a167, 0\nMvva 132,216,56\nTa 212,202,35080\nv_accvgpr_write_b32 a168, 0\nMvva 132,220,76\n"
"Ta 214,202,35592\nv_accvgpr_write_b32 a169, 0\nMvva 8,224,40\nv_accvgpr_write_b32 a170, 0\nv_accvgpr_write_b32 a171, 0\nMvva 8,228,60\n"
"v_accvgpr_write_b32 a172, 0\nv_accvgpr_write_b32 a173, 0\nMvva 40,224,44\nv_accvgpr_write_b32 a174, 0\nv_accvgpr_write_b32 a175, 0\nMvva 40,228,64\n"
"v_accvgpr_write_b32 a176, 0\nv_accvgpr_write_b32 a177, 0\nMvva 72,224,48\nv_accvgpr_write_b32 a178, 0\nv_accvgpr_write_b32 a179, 0\nMvva 72,228,68\n"
"Mvva 104,224,52\nv_mul_f32 v0, s80, v0\nv_mul_f32 v1, s80, v1\nMvva 104,228,72\nv_mul_f32 v2, s80, v2\nv_mul_f32 v3, s80, v3\nMvva 136,224,56\n"
"v_mul_f32 v4, s80, v4\nv_mul_f32 v5, s80, v5\nMvva 136,228,76\nv_mul_f32 v6, s80, v6\nv_mul_f32 v7, s80, v7\nMvva 12,232,40\nCV 0,0\nCV 1,2\n"
"Mvva 12,236,60\nCV 2,4\nCV 3,6\nMvva 44,232,44\nv_mul_f32 v32, s80, v32\nv_mul_f32 v33, s80, v33\nMvva 44,236,64\ns_memrealtime s[98:99]\n"
"v_mul_f32 v34, s80, v34\nv_mul_f32 v35, s80, v35\nMvva 76,232,48\nv_mul_f32 v36, s80, v36\nv_mul_f32 v37, s80, v37\nMvva 76,236,68\n"
"v_mul_f32 v38, s80, v38\nv_mul_f32 v39, s80, v39\nMvva 108,232,52\nCV 32,32\nCV 33,34\nMvva 108,236,72\nCV 34,36\nCV 35,38\nMvva 140,232,56\n"
"s_waitcnt vmcnt(62) lgkmcnt(0)\nv_mul_f32 v64, s80, v64\nv_mul_f32 v65, s80, v65\nMvva 140,236,76\ns_barrier\n"
"s_sub_u32 s95, s98, s93\ns_mov_b32 s93, s98\ns_sub_u32 s95, 43, s95\ns_cbranch_scc1 .Lfa_t73b\ns_add_u32 s93, s93, s95\n.Lfa_t73a:\ns_sub_u32 s95, s95, 4\ns_cbranch_scc1 .Lfa_t73b\ns_sleep 1\ns_branch .Lfa_t73a\n.Lfa_t73b:\n"
"Mvaa 16,184,40\ns_add_u32 s75, s97, 0x6000\nRv 180,253,0\nMvaa 16,188,60\ns_and_b32 s75, s75, 0x1ffff\nRv 184,253,64\nMvaa 48,184,44\n"
"s_add_u32 s75, s75, 0x40000\nRv 188,253,128\nMvaa 48,188,64\ns_add_u32 m0, s72, 0x10000\nRv 192,253,192\nv_mul_f32 v66, s80, v66\nMvaa 80,184,48\n"
"DtL 210,44,75,0\nMvaa 80,188,68\nRa 216,204,0\nv_mul_f32 v67, s80, v67\nMvaa 112,184,52\nRa 220,205,0\nv_mul_f32 v68, s80, v68\nMvaa 112,188,72\n"
"Ra 224,206,0\nv_mul_f32 v69, s80, v69\nMvaa 144,184,56\nDtL 211,44,75,1024\nMvaa 144,188,76\nRa 228,207,0\nv_mul_f32 v70, s80, v70\nMvaa 20,192,40\n"
"Ra 232,204,4096\nv_mul_f32 v71, s80, v71\nMvaa 20,196,60\nRa 236,205,4096\nCV 64,64\nMvaa 52,192,44\nDtL 212,44,75,2048\nMvaa 52,196,64\n"
"Ra 240,206,4096\nv_add_u32 v204, 0xfffe0000, v204\nMvaa 84,192,48\nRa 244,207,4096\nv_add_u32 v205, 0xfffe0000, v205\nMvaa 84,196,68\n"
"Ta 248,208,8192\nv_add_u32 v206, 0xfffe0000, v206\nMvaa 116,192,52\nDtL 213,44,75,3072\nMvaa 116,196,72\nTa 250,208,12288\n"
"v_add_u32 v207, 0xfffe0000, v207\nMvaa 148,192,56\nTa 252,209,8192\nCV 65,66\nMvaa 148,196,76\nTa 254,209,12288\nCV 66,68\nMvaa 24,200,40\n"
"Tv 216,208,8448\nCV 67,70\nMvaa 24,204,60\nTv 218,208,12544\nv_mul_f32 v96, s80, v96\nMvaa 56,200,44\nTv 220,209,8448\nv_mul_f32 v97, s80, v97\n"
"Mvaa 56,204,64\nTv 222,209,12544\nv_mul_f32 v98, s80, v98\nMvaa 88,200,48\nTv 224,208,8704\nv_mul_f32 v99, s80, v99\nMvaa 88,204,68\n"
"Tv 226,208,12800\nv_mul_f32 v100, s80, v100\nMvaa 120,200,52\nTv 228,209,8704\nv_mul_f32 v101, s80, v101\nMvaa 120,204,72\nTv 230,209,12800\n"
"v_mul_f32 v102, s80, v102\nMvaa 152,200,56\nTv 232,208,8960\nv_mul_f32 v103, s80, v103\nMvaa 152,204,76\nTv 234,208,13056\nCV 96,96\nMvaa 28,208,40\n"
"Tv 236,209,8960\nCV 97,98\nMvaa 28,212,60\nTv 238,209,13056\nv_add_u32 v208, 0xfffe0000, v208\nMvaa 60,208,44\nCV 98,100\nCV 99,102\nMvaa 60,212,64\n"
"v_add_u32 v209, 0xfffe0000, v209\nv_mul_f32 v128, s80, v128\nMvaa 92,208,48\nv_mul_f32 v129, s80, v129\nv_mul_f32 v130, s80, v130\nMvaa 92,212,68\n"
"v_mul_f32 v131, s80, v131\nv_mul_f32 v132, s80, v132\nMvaa 124,208,52\nv_mul_f32 v133, s80, v133\nv_mul_f32 v134, s80, v134\nMvaa 124,212,72\n"
"v_mul_f32 v135, s80, v135\nCV 128,128\nMvaa 156,208,56\nCV 129,130\nCV 130,132\nMvaa 156,212,76\ns_waitcnt lgkmcnt(0)\nCV 131,134\n"
"v_xor_b32 v180, s81, v180\nv_xor_b32 v181, s81, v181\nv_xor_b32 v182, s81, v182\nv_xor_b32 v183, s81, v183\nv_xor_b32 v184, s81, v184\n"
"v_xor_b32 v185, s81, v185\nv_xor_b32 v186, s81, v186\nv_xor_b32 v187, s81, v187\nv_xor_b32 v188, s81, v188\nv_xor_b32 v189, s81, v189\n"
"v_xor_b32 v190, s81, v190\nv_xor_b32 v191, s81, v191\nv_xor_b32 v192, s81, v192\nv_xor_b32 v193, s81, v193\nv_xor_b32 v194, s81, v194\n"
"v_xor_b32 v195, s81, v195\ns_nop 7\nPS 8,80\nPS 10,80\nPS 12,80\nPS 14,80\nPS 16,80\nPS 18,80\nPS 20,80\nPS 22,80\nPS 24,80\nPS 26,80\nPS 28,80\n"
"PS 30,80\nPS 40,80\nPS 42,80\nPS 44,80\nPS 46,80\nPS 48,80\nPS 50,80\nPS 52,80\nPS 54,80\nPS 56,80\nPS 58,80\nPS 60,80\nPS 62,80\nPS 72,80\nPS 74,80\n"
"PS 76,80\nPS 78,80\nPS 80,80\nPS 82,80\nPS 84,80\nPS 86,80\nPS 88,80\nPS 90,80\nPS 92,80\nPS 94,80\nPS 104,80\nPS 106,80\nPS 108,80\nPS 110,80\n"
"PS 112,80\nPS 114,80\nPS 116,80\nPS 118,80\nPS 120,80\nPS 122,80\nPS 124,80\nPS 126,80\n.Lfa_pd16811:\ns_cbranch_execz .Lfa_wr8\nPS 136,80\n"
"PS 138,80\nPS 140,80\nPS 142,80\nPS 144,80\nPS 146,80\nPS 148,80\nPS 150,80\nPS 152,80\nPS 154,80\nPS 156,80\nPS 158,80\nCV 8,8\nCV 9,10\nCV 10,12\n"
"CV 11,14\nCV 16,16\nCV 17,18\nCV 18,20\nCV 19,22\nCV 24,24\nCV 25,26\nCV 26,28\nCV 27,30\nAW 0,0\nAW 1,1\nAW 2,2\nAW 3,3\nAW 4,8\nAW 5,9\nAW 6,10\n"
"AW 7,11\nAW 8,16\nAW 9,17\nAW 10,18\nAW 11,19\nAW 12,24\nAW 13,25\nAW 14,26\nAW 15,27\nCV 40,40\nCV 41,42\nZvva 160,180,0\nCV 42,44\nCV 43,46\n"
"CV 48,48\nCV 49,50\nCV 50,52\nCV 51,54\nCV 56,56\nMvva 160,184,4\nCV 57,58\nCV 58,60\nCV 59,62\nAW 16,32\nAW 17,33\nAW 18,34\nAW 19,35\n"
"Mvva 160,188,8\nAW 20,40\nAW 21,41\nAW 22,42\nAW 23,43\nAW 24,48\nAW 25,49\nAW 26,50\nMvva 160,192,12\nAW 27,51\nAW 28,56\nAW 29,57\nAW 30,58\n"
"AW 31,59\nCV 72,72\nCV 73,74\nZvva 164,180,16\nCV 74,76\nCV 75,78\nCV 80,80\nCV 81,82\nCV 82,84\nCV 83,86\nCV 88,88\nMvva 164,184,20\nCV 89,90\n"
"CV 90,92\nCV 91,94\nAW 32,64\nAW 33,65\nAW 34,66\nAW 35,67\nMvva 164,188,24\nAW 36,72\nAW 37,73\nAW 38,74\nAW 39,75\nAW 40,80\nAW 41,81\nAW 42,82\n"
"Mvva 164,192,28\nAW 43,83\nAW 44,88\nAW 45,89\nAW 46,90\nAW 47,91\nCV 104,104\nCV 105,106\nZvva 168,180,32\nCV 106,108\nCV 107,110\nCV 112,112\n"
"CV 113,114\nCV 114,116\nCV 115,118\nCV 120,120\nMvva 168,184,36\nCV 121,122\nCV 122,124\nCV 123,126\nAW 48,96\nAW 49,97\nAW 50,98\nAW 51,99\n"
"Mvva 168,188,40\nAW 52,104\nAW 53,105\nAW 54,106\nAW 55,107\nAW 56,112\nAW 57,113\nAW 58,114\nMvva 168,192,44\nAW 59,115\nAW 60,120\nAW 61,121\n"
"AW 62,122\nAW 63,123\nCV 136,136\nCV 137,138\nZvva 172,180,48\nCV 138,140\nCV 139,142\nCV 144,144\nCV 145,146\nCV 146,148\nCV 147,150\nCV 152,152\n"
"Mvva 172,184,52\nCV 153,154\nCV 154,156\nCV 155,158\nAW 64,128\nAW 65,129\nAW 66,130\nAW 67,131\nMvva 172,188,56\nAW 68,136\nAW 69,137\nAW 70,138\n"
"AW 71,139\nAW 72,144\nAW 73,145\nAW 74,146\nMvva 172,192,60\nAW 75,147\nAW 76,152\nAW 77,153\nAW 78,154\nAW 79,155\ns_nop 1\nZvva 176,180,64\n"
"Mvva 176,184,68\nMvva 176,188,72\nMvva 176,192,76\nCvaa 180,216,0,160\nMvaa 180,220,4\nMvaa 180,224,8\nMvaa 180,228,12\nCvaa 184,232,0,160\n"
"Mvaa 184,236,4\nMvaa 184,240,8\nMvaa 184,244,12\ns_nop 3\ns_memrealtime s[98:99]\ns_waitcnt vmcnt(62) lgkmcnt(0)\ns_barrier\ns_mov_b32 s93, s98\n"
"Cvaa 188,216,16,164\ns_add_u32 m0, s72, 0x12000\nEX 180\nMvaa 188,220,20\ns_waitcnt vmcnt(8)\nEX 181\nMvaa 188,224,24\nEX 182\nCV 196,180\n"
"Mvaa 188,228,28\nEX 183\nCvaa 192,232,16,164\nEX 184\nCV 197,182\nMvaa 192,236,20\nEX 185\nMvaa 192,240,24\nEX 186\nCV 198,184\nMvaa 192,244,28\n"
"EX 187\ns_nop 0\nCV 199,186\nCvaa 180,216,32,168\nEX 188\nMvaa 180,220,36\nDL 214,48,75,0\nMvaa 180,224,40\nEX 189\nMvaa 180,228,44\nRa 184,204,0\n"
"CV 200,188\nCvaa 184,232,32,168\nEX 190\nMvaa 184,236,36\nRa 188,205,0\nMvaa 184,240,40\nEX 191\nMvaa 184,244,44\nRa 192,206,0\nCV 201,190\n"
"Zvav 0,248,196\nEX 192\nZvav 4,252,196\nDL 215,48,75,1024\nZvvv 8,216,196\nEX 193\nZvvv 12,220,196\nRa 196,207,0\nCV 202,192\nZvvv 16,224,196\n"
"EX 194\nZvvv 20,228,196\nRa 200,204,4096\nZvvv 24,232,196\nEX 195\nZvvv 28,236,196\nRa 204,205,4096\nCV 203,194\nCvaa 188,216,48,172\n"
"s_waitcnt vmcnt(8)\nRa 208,206,4096\nMvaa 188,220,52\nEX 180\nMvaa 188,224,56\nDL 214,48,75,2048\nMvaa 188,228,60\nEX 181\n"
"v_smfmac_f32_16x16x64_bf16 a[160:163], a[180:183], v[196:203], v251\nRa 212,207,4096\nCV 196,180\nCvaa 192,232,48,172\nEX 182\nMvaa 192,236,52\n"
"Mvaa 192,240,56\nEX 183\nMvaa 192,244,60\nCV 197,182\nZvav 32,248,200\nEX 184\nZvav 36,252,200\nDL 215,48,75,3072\nZvvv 40,216,200\nEX 185\n"
"Zvvv 44,220,200\nCV 198,184\nZvvv 48,224,200\ns_add_u32 s75, s97, 0x8000\nEX 186\nZvvv 52,228,200\ns_and_b32 s75, s75, 0x1ffff\nZvvv 56,232,200\n"
"s_add_u32 s75, s75, 0x40000\nEX 187\nZvvv 60,236,200\ns_add_u32 m0, s72, 0x18000\nCV 199,186\nCvaa 180,216,64,176\ns_waitcnt vmcnt(8)\nEX 188\n"
"Mvaa 180,220,68\nDtL 210,44,75,0\nMvaa 180,224,72\nEX 189\nMvaa 180,228,76\nTa 216,208,8192\nCV 200,188\nCvaa 184,232,64,176\nEX 190\n"
"Mvaa 184,236,68\nTa 218,208,12288\nMvaa 184,240,72\nEX 191\nMvaa 184,244,76\nTa 220,209,8192\nCV 201,190\nZvav 64,248,196\nEX 192\nZvav 68,252,196\n"
"DtL 211,44,75,1024\nZvvv 72,216,196\nEX 193\nZvvv 76,220,196\nTa 222,209,12288\nCV 202,192\nZvvv 80,224,196\nEX 194\nZvvv 84,228,196\n"
"Ta 224,208,8448\nZvvv 88,232,196\nEX 195\nZvvv 92,236,196\ns_waitcnt lgkmcnt(2)\nTa 226,208,12544\nCV 203,194\nCvaa 188,184,0,160\n"
"s_waitcnt vmcnt(8)\nTa 228,209,8448\nMvaa 188,188,4\nEX 180\nMvaa 188,192,8\nDtL 212,44,75,2048\nMvaa 188,196,12\nEX 181\n"
"v_smfmac_f32_16x16x64_bf16 a[164:167], a[180:183], v[196:203], v251\nTa 230,209,12544\nCV 196,180\nCvaa 192,200,0,160\nEX 182\nMvaa 192,204,4\n"
"Ta 232,208,8704\nMvaa 192,208,8\nEX 183\nMvaa 192,212,12\nTa 234,208,12800\nCV 197,182\nZvav 96,248,200\nEX 184\nZvav 100,252,200\n"
"DtL 213,44,75,3072\nZvvv 104,216,200\nEX 185\nZvvv 108,220,200\nTa 236,209,8704\nCV 198,184\nZvvv 112,224,200\nEX 186\nZvvv 116,228,200\n"
"Ta 238,209,12800\nZvvv 120,232,200\nEX 187\nZvvv 124,236,200\nTa 240,208,8960\nCV 199,186\nCvaa 180,184,16,164\nEX 188\nMvaa 180,188,20\n"
"Ta 242,208,13056\nMvaa 180,192,24\nEX 189\nMvaa 180,196,28\ns_memrealtime s[98:99]\nTa 244,209,8960\nCV 200,188\nCvaa 184,200,16,164\nEX 190\n"
"Mvaa 184,204,20\nTa 246,209,13056\nMvaa 184,208,24\nEX 191\nMvaa 184,212,28\nCV 201,190\nZvav 128,248,196\ns_waitcnt vmcnt(12) lgkmcnt(0)\nEX 192\n"
"Zvav 132,252,196\ns_barrier\n"
"s_sub_u32 s95, s98, s93\ns_mov_b32 s93, s98\ns_sub_u32 s95, 102, s95\ns_cbranch_scc1 .Lfa_t74b\ns_add_u32 s93, s93, s95\n.Lfa_t74a:\ns_sub_u32 s95, s95, 4\ns_cbranch_scc1 .Lfa_t74b\ns_sleep 1\ns_branch .Lfa_t74a\n.Lfa_t74b:\n"
"Zvvv 136,216,196\ns_add_u32 m0, s72, 0x1a000\nEX 193\nZvvv 140,220,196\ns_waitcnt vmcnt(8)\nRa 248,204,32768\nCV 202,192\nZvvv 144,224,196\nEX 194\n"
"Zvvv 148,228,196\nDL 214,48,75,0\nZvvv 152,232,196\nEX 195\nZvvv 156,236,196\nRa 252,205,32768\nCV 203,194\nCvaa 188,184,32,168\nRv 216,206,32768\n"
"Mvaa 188,188,36\nEX 180\nMvaa 188,192,40\nRv 220,207,32768\nMvaa 188,196,44\nEX 181\n"
"v_smfmac_f32_16x16x64_bf16 a[168:171], a[180:183], v[196:203], v251\nRv 224,204,36864\nCV 196,180\nCvaa 192,200,32,168\nEX 182\nMvaa 192,204,36\n"
"DL 215,48,75,1024\nMvaa 192,208,40\nEX 183\nMvaa 192,212,44\nRv 228,205,36864\nCV 197,182\nMvav 0,216,200\nEX 184\nMvav 4,220,200\nRv 232,206,36864\n"
"v_add_u32 v204, 0x10000, v204\nMvav 8,224,200\nEX 185\nMvav 12,228,200\nRv 236,207,36864\nCV 198,184\nMvav 16,232,200\ns_waitcnt vmcnt(8)\nEX 186\n"
"Mvav 20,236,200\nDL 214,48,75,2048\nMvav 24,240,200\nEX 187\nMvav 28,244,200\nCV 199,186\nv_add_u32 v205, 0x10000, v205\nCvaa 180,184,48,172\nEX 188\n"
"Mvaa 180,188,52\nv_add_u32 v206, 0x10000, v206\nv_add_u32 v207, 0x10000, v207\nMvaa 180,192,56\nEX 189\nMvaa 180,196,60\nCV 200,188\n"
"Cvaa 184,200,48,172\nEX 190\nMvaa 184,204,52\nDL 215,48,75,3072\nMvaa 184,208,56\nEX 191\nMvaa 184,212,60\nCV 201,190\nMvav 32,216,196\n"
"s_add_u32 s75, s97, 0xa000\nEX 192\nMvav 36,220,196\ns_and_b32 s75, s75, 0x1ffff\nMvav 40,224,196\ns_add_u32 s75, s75, 0x40000\nEX 193\n"
"Mvav 44,228,196\ns_add_u32 m0, s72, 0x20000\nCV 202,192\nMvav 48,232,196\ns_waitcnt vmcnt(8)\nEX 194\nMvav 52,236,196\nDtL 210,44,75,0\n"
"Mvav 56,240,196\nEX 195\nMvav 60,244,196\nCV 203,194\nCvaa 188,184,64,176\nTa 184,208,40960\nMvaa 188,188,68\nEX 180\nMvaa 188,192,72\n"
"Ta 186,208,45056\nMvaa 188,196,76\nEX 181\nv_smfmac_f32_16x16x64_bf16 a[172:175], a[180:183], v[196:203], v251\nTa 188,209,40960\nCV 196,180\n"
"Cvaa 192,200,64,176\nEX 182\nMvaa 192,204,68\nDtL 211,44,75,1024\nMvaa 192,208,72\nEX 183\nMvaa 192,212,76\nTa 190,209,45056\nCV 197,182\n"
"Mvav 64,216,200\nEX 184\nMvav 68,220,200\nTa 192,208,41216\nMvav 72,224,200\nEX 185\nMvav 76,228,200\nTa 194,208,45312\nCV 198,184\nMvav 80,232,200\n"
"s_waitcnt vmcnt(8)\nEX 186\nMvav 84,236,200\nDtL 212,44,75,2048\nMvav 88,240,200\nEX 187\nMvav 92,244,200\ns_waitcnt lgkmcnt(2)\nTa 196,209,41216\n"
"CV 199,186\nCvaa 180,248,0,160\nEX 188\nMvaa 180,252,4\nTa 198,209,45312\nMvva 180,216,8\nEX 189\nMvva 180,220,12\nTa 200,208,41472\nCV 200,188\n"
"Cvva 184,224,0,160\nEX 190\nMvva 184,228,4\nDtL 213,44,75,3072\nMvva 184,232,8\nEX 191\nMvva 184,236,12\nTa 202,208,45568\nCV 201,190\n"
"Mvav 96,216,196\nEX 192\nMvav 100,220,196\nTa 204,209,41472\nMvav 104,224,196\nEX 193\nMvav 108,228,196\nTa 206,209,45568\nCV 202,192\n"
"Mvav 112,232,196\nEX 194\nMvav 116,236,196\nTa 208,208,41728\nMvav 120,240,196\nEX 195\nMvav 124,244,196\nTa 210,208,45824\nCV 203,194\n"
"Cvaa 188,248,16,164\nTa 212,209,41728\nMvaa 188,252,20\n.Lfa_pd17368:\ns_cbranch_execz .Lfa_wr9\nEX 180\nMvva 188,216,24\nTa 214,209,45824\n"
"v_add_u32 v208, 0x10000, v208\nMvva 188,220,28\nEX 181\nv_smfmac_f32_16x16x64_bf16 a[176:179], a[180:183], v[196:203], v251\ns_memrealtime s[98:99]\n"
"CV 196,180\nv_add_u32 v209, 0x10000, v209\nCvva 192,224,16,164\nEX 182\nMvva 192,228,20\nMvva 192,232,24\nEX 183\nMvva 192,236,28\nCV 197,182\n"
"Mvav 128,216,200\ns_waitcnt vmcnt(12) lgkmcnt(0)\nEX 184\nMvav 132,220,200\ns_barrier\n"
"s_mov_b64 exec, 0\ns_cmp_eq_u32 s60, 0\ns_cbranch_scc1 .Lfa_w10_0\ns_cmp_eq_u32 s60, 1\ns_cbranch_scc1 .Lfa_w10_1\ns_cmp_eq_u32 s60, 2\ns_cbranch_scc1 .Lfa_w10_2\ns_branch .Lfa_w10_3\n.Lfa_wr10:\ns_mov_b64 exec, -1\ns_nop 4\n"
"s_sub_u32 s95, s98, s93\ns_mov_b32 s93, s98\ns_sub_u32 s95, 102, s95\ns_cbranch_scc1 .Lfa_t75b\ns_add_u32 s93, s93, s95\n.Lfa_t75a:\ns_sub_u32 s95, s95, 4\ns_cbranch_scc1 .Lfa_t75b\ns_sleep 1\ns_branch .Lfa_t75a\n.Lfa_t75b:\n"
"Mvav 136,224,200\ns_add_u32 m0, s72, 0x22000\nEX 185\nMvav 140,228,200\ns_waitcnt vmcnt(8)\nRa 216,204,0\nCV 198,184\nMvav 144,232,200\nEX 186\n"
"Mvav 148,236,200\nDL 214,48,75,0\nMvav 152,240,200\nEX 187\nMvav 156,244,200\nRa 220,205,0\nCV 199,186\nCvaa 180,248,32,168\nEX 188\nMvaa 180,252,36\n"
"DL 215,48,75,1024\nMvva 180,216,40\nEX 189\nMvva 180,220,44\nRa 224,206,0\nCV 200,188\nCvva 184,224,32,168\ns_waitcnt vmcnt(8)\nEX 190\n"
"Mvva 184,228,36\nDL 214,48,75,2048\nMvva 184,232,40\nEX 191\nMvva 184,236,44\nRa 228,207,0\nCV 201,190\nMvav 0,184,196\nEX 192\nMvav 4,188,196\n"
"DL 215,48,75,3072\nMvav 8,192,196\ns_add_u32 s75, s97, 0xc000\nEX 193\nMvav 12,196,196\ns_and_b32 s75, s75, 0x1ffff\nRa 232,204,4096\nCV 202,192\n"
"Mvav 16,200,196\ns_add_u32 s75, s75, 0x40000\nEX 194\nMvav 20,204,196\ns_add_u32 m0, s72, 0x0\nRa 236,205,4096\nMvav 24,208,196\ns_waitcnt vmcnt(8)\n"
"EX 195\nMvav 28,212,196\nRa 240,206,4096\nCV 203,194\nCvaa 188,248,48,172\nDtL 210,44,75,0\nMvaa 188,252,52\nEX 180\nMvva 188,216,56\n"
"Ra 244,207,4096\nMvva 188,220,60\nEX 181\nv_smfmac_f32_16x16x64_bf16 a[160:163], a[180:183], v[196:203], v251\nCV 196,180\nCvva 192,224,48,172\n"
"EX 182\nMvva 192,228,52\nDtL 211,44,75,1024\nMvva 192,232,56\nEX 183\nMvva 192,236,60\nCV 197,182\nMvav 32,184,200\ns_waitcnt vmcnt(8)\nEX 184\n"
"Mvav 36,188,200\nDtL 212,44,75,2048\nMvav 40,192,200\nEX 185\nMvav 44,196,200\nCV 198,184\nMvav 48,200,200\nEX 186\nMvav 52,204,200\n"
"DtL 213,44,75,3072\nMvav 56,208,200\nEX 187\nMvav 60,212,200\nCV 199,186\nCvaa 180,248,64,176\nEX 188\nMvaa 180,252,68\n"
"buffer_load_dword v252, v210, s[68:71], s96 offen offset:0\nMvva 180,216,72\nEX 189\nMvva 180,220,76\nTa 248,208,8192\nCV 200,188\n"
"Cvva 184,224,64,176\nEX 190\nMvva 184,228,68\nbuffer_load_dword v252, v210, s[68:71], s96 offen offset:1024\nMvva 184,232,72\nEX 191\n"
"Mvva 184,236,76\nTa 250,208,12288\nCV 201,190\nMvav 64,184,196\nEX 192\nMvav 68,188,196\nTa 252,209,8192\nMvav 72,192,196\nEX 193\nMvav 76,196,196\n"
"Ta 254,209,12288\nCV 202,192\nMvav 80,200,196\nEX 194\nMvav 84,204,196\nTv 216,208,8448\nMvav 88,208,196\nEX 195\nMvav 92,212,196\n"
"s_waitcnt lgkmcnt(3)\nTv 218,208,12544\nCV 203,194\nCvaa 188,216,0,160\nTv 220,209,8448\nMvaa 188,220,4\nEX 180\nMvaa 188,224,8\nTv 222,209,12544\n"
"Mvaa 188,228,12\nEX 181\nv_smfmac_f32_16x16x64_bf16 a[164:167], a[180:183], v[196:203], v251\nTv 224,208,8704\nCV 196,180\nCvaa 192,232,0,160\n"
"EX 182\nMvaa 192,236,4\nTv 226,208,12800\nMvaa 192,240,8\nEX 183\nMvaa 192,244,12\nTv 228,209,8704\nCV 197,182\nMvav 96,184,200\nEX 184\n"
"Mvav 100,188,200\nTv 230,209,12800\nMvav 104,192,200\nEX 185\nMvav 108,196,200\nTv 232,208,8960\nCV 198,184\nMvav 112,200,200\nEX 186\n"
"Mvav 116,204,200\nTv 234,208,13056\nMvav 120,208,200\nEX 187\nMvav 124,212,200\nTv 236,209,8960\nCV 199,186\nCvaa 180,216,16,164\nEX 188\n"
"Mvaa 180,220,20\nTv 238,209,13056\nMvaa 180,224,24\nEX 189\nMvaa 180,228,28\ns_memrealtime s[98:99]\nCV 200,188\nCvaa 184,232,16,164\nEX 190\n"
"Mvaa 184,236,20\nMvaa 184,240,24\nEX 191\nMvaa 184,244,28\nCV 201,190\nMvav 128,184,196\ns_waitcnt vmcnt(14) lgkmcnt(0)\nEX 192\nMvav 132,188,196\n"
"s_barrier\n"
"s_sub_u32 s95, s98, s93\ns_mov_b32 s93, s98\ns_sub_u32 s95, 102, s95\ns_cbranch_scc1 .Lfa_t76b\ns_add_u32 s93, s93, s95\n.Lfa_t76a:\ns_sub_u32 s95, s95, 4\ns_cbranch_scc1 .Lfa_t76b\ns_sleep 1\ns_branch .Lfa_t76a\n.Lfa_t76b:\n"
"Mvav 136,192,196\ns_add_u32 m0, s72, 0x2000\nEX 193\nMvav 140,196,196\ns_waitcnt vmcnt(8)\nRa 184,204,32768\nCV 202,192\nMvav 144,200,196\nEX 194\n"
"Mvav 148,204,196\nDL 214,48,75,0\nMvav 152,208,196\nEX 195\nMvav 156,212,196\nRa 188,205,32768\nCV 203,194\nCvaa 188,216,32,168\nRa 192,206,32768\n"
"Mvaa 188,220,36\nEX 180\nMvaa 188,224,40\nRa 196,207,32768\nMvaa 188,228,44\nEX 181\n"
"v_smfmac_f32_16x16x64_bf16 a[168:171], a[180:183], v[196:203], v251\nRa 200,204,36864\nCV 196,180\nCvaa 192,232,32,168\nEX 182\nMvaa 192,236,36\n"
"DL 215,48,75,1024\nMvaa 192,240,40\nEX 183\nMvaa 192,244,44\nRa 204,205,36864\nCV 197,182\nMvav 0,248,200\nEX 184\nMvav 4,252,200\nRa 208,206,36864\n"
"v_add_u32 v204, 0x10000, v204\nMvvv 8,216,200\nEX 185\nMvvv 12,220,200\nRa 212,207,36864\nCV 198,184\nMvvv 16,224,200\ns_waitcnt vmcnt(8)\nEX 186\n"
"Mvvv 20,228,200\nDL 214,48,75,2048\nMvvv 24,232,200\nEX 187\nMvvv 28,236,200\nCV 199,186\nv_add_u32 v205, 0x10000, v205\nCvaa 180,216,48,172\nEX 188\n"
"Mvaa 180,220,52\nv_add_u32 v206, 0x10000, v206\nv_add_u32 v207, 0x10000, v207\nMvaa 180,224,56\nEX 189\nMvaa 180,228,60\nCV 200,188\n"
"Cvaa 184,232,48,172\nEX 190\nMvaa 184,236,52\nDL 215,48,75,3072\nMvaa 184,240,56\nEX 191\nMvaa 184,244,60\nCV 201,190\nMvav 32,248,196\n"
"s_add_u32 s75, s97, 0xe000\nEX 192\nMvav 36,252,196\ns_and_b32 s75, s75, 0x1ffff\nMvvv 40,216,196\ns_add_u32 s75, s75, 0x40000\nEX 193\n"
"Mvvv 44,220,196\ns_add_u32 m0, s72, 0x8000\nCV 202,192\nMvvv 48,224,196\ns_waitcnt vmcnt(8)\nEX 194\nMvvv 52,228,196\nDtL 210,44,75,0\n"
"Mvvv 56,232,196\nEX 195\nMvvv 60,236,196\nCV 203,194\nCvaa 188,216,64,176\nTa 216,208,40960\nMvaa 188,220,68\nEX 180\nMvaa 188,224,72\n"
"Ta 218,208,45056\nMvaa 188,228,76\nEX 181\nv_smfmac_f32_16x16x64_bf16 a[172:175], a[180:183], v[196:203], v251\nTa 220,209,40960\nCV 196,180\n"
"Cvaa 192,232,64,176\nEX 182\nMvaa 192,236,68\nDtL 211,44,75,1024\nMvaa 192,240,72\nEX 183\nMvaa 192,244,76\nTa 222,209,45056\nCV 197,182\n"
"Mvav 64,248,200\nEX 184\nMvav 68,252,200\nTa 224,208,41216\nMvvv 72,216,200\nEX 185\nMvvv 76,220,200\nTa 226,208,45312\nCV 198,184\nMvvv 80,224,200\n"
"s_waitcnt vmcnt(8)\nEX 186\nMvvv 84,228,200\nDtL 212,44,75,2048\nMvvv 88,232,200\nEX 187\nMvvv 92,236,200\ns_waitcnt lgkmcnt(2)\nTa 228,209,41216\n"
"CV 199,186\nCvaa 180,184,0,160\nEX 188\nMvaa 180,188,4\nTa 230,209,45312\nMvaa 180,192,8\nEX 189\nMvaa 180,196,12\nTa 232,208,41472\nCV 200,188\n"
"Cvaa 184,200,0,160\nEX 190\nMvaa 184,204,4\nDtL 213,44,75,3072\nMvaa 184,208,8\nEX 191\nMvaa 184,212,12\nTa 234,208,45568\nCV 201,190\n"
"Mvav 96,248,196\nEX 192\nMvav 100,252,196\nTa 236,209,41472\nMvvv 104,216,196\nEX 193\nMvvv 108,220,196\nTa 238,209,45568\nCV 202,192\n"
"Mvvv 112,224,196\nEX 194\nMvvv 116,228,196\nTa 240,208,41728\nMvvv 120,232,196\nEX 195\nMvvv 124,236,196\nTa 242,208,45824\nCV 203,194\n"
"Cvaa 188,184,16,164\nTa 244,209,41728\nMvaa 188,188,20\nEX 180\nMvaa 188,192,24\nTa 246,209,45824\nv_add_u32 v208, 0x10000, v208\nMvaa 188,196,28\n"
"EX 181\nv_smfmac_f32_16x16x64_bf16 a[176:179], a[180:183], v[196:203], v251\ns_memrealtime s[98:99]\nCV 196,180\nv_add_u32 v209, 0x10000, v209\n"
"Cvaa 192,200,16,164\nEX 182\nMvaa 192,204,20\nMvaa 192,208,24\nEX 183\nMvaa 192,212,28\nCV 197,182\nMvav 128,248,200\ns_waitcnt vmcnt(14) lgkmcnt(0)\n"
"EX 184\nMvav 132,252,200\ns_barrier\n"
"s_sub_u32 s95, s98, s93\ns_mov_b32 s93, s98\ns_sub_u32 s95, 102, s95\ns_cbranch_scc1 .Lfa_t77b\ns_add_u32 s93, s93, s95\n.Lfa_t77a:\ns_sub_u32 s95, s95, 4\ns_cbranch_scc1 .Lfa_t77b\ns_sleep 1\ns_branch .Lfa_t77a\n.Lfa_t77b:\n"
"Mvvv 136,216,200\ns_add_u32 m0, s72, 0xa000\nEX 185\nMvvv 140,220,200\ns_waitcnt vmcnt(8)\nRa 248,204,0\nCV 198,184\nMvvv 144,224,200\nEX 186\n"
"Mvvv 148,228,200\nDL 214,48,75,0\nMvvv 152,232,200\nEX 187\nMvvv 156,236,200\nRa 252,205,0\nCV 199,186\nCvaa 180,184,32,168\nEX 188\nMvaa 180,188,36\n"
"Rv 216,206,0\nv_mbcnt_lo_u32_b32 v253, -1, 0\nMvaa 180,192,40\nEX 189\nMvaa 180,196,44\nRv 220,207,0\nCV 200,188\nCvaa 184,200,32,168\nEX 190\n"
"Mvaa 184,204,36\nDL 215,48,75,1024\nMvaa 184,208,40\nEX 191\nMvaa 184,212,44\nRv 224,204,4096\nCV 201,190\nMvav 0,216,196\nEX 192\nMvav 4,220,196\n"
"Rv 228,205,4096\nv_add_u32 v204, 0xfffe0000, v204\nMvav 8,224,196\nEX 193\nMvav 12,228,196\nRv 232,206,4096\nCV 202,192\nMvav 16,232,196\n"
"s_waitcnt vmcnt(8)\nEX 194\nMvav 20,236,196\nDL 214,48,75,2048\nMvav 24,240,196\nEX 195\nMvav 28,244,196\nRv 236,207,4096\nCV 203,194\n"
"Cvaa 188,184,48,172\nv_add_u32 v205, 0xfffe0000, v205\nv_add_u32 v206, 0xfffe0000, v206\nMvaa 188,188,52\nEX 180\nMvaa 188,192,56\n"
"v_add_u32 v207, 0xfffe0000, v207\nv_mbcnt_hi_u32_b32 v253, -1, v253\nMvaa 188,196,60\nEX 181\n"
"v_smfmac_f32_16x16x64_bf16 a[160:163], a[180:183], v[196:203], v251\nCV 196,180\nv_lshrrev_b32 v249, 5, v253\nCvaa 192,200,48,172\nEX 182\n"
"Mvaa 192,204,52\nDL 215,48,75,3072\nMvaa 192,208,56\nEX 183\nMvaa 192,212,60\nCV 197,182\nv_bfe_u32 v250, v253, 4, 1\nMvav 32,216,200\n"
"s_add_u32 s75, s97, 0x10000\nEX 184\nMvav 36,220,200\ns_and_b32 s75, s75, 0x1ffff\nv_bfe_u32 v248, v253, 2, 2\nv_and_b32 v243, 3, v253\n"
"Mvav 40,224,200\ns_add_u32 s75, s75, 0x40000\nEX 185\nMvav 44,228,200\ns_add_u32 m0, s72, 0x10000\nCV 198,184\nv_lshlrev_b32 v240, 1, v249\n"
"Mvav 48,232,200\ns_waitcnt vmcnt(8)\nEX 186\nMvav 52,236,200\nDtL 210,44,75,0\nMvav 56,240,200\nEX 187\nMvav 60,244,200\nCV 199,186\n"
"v_xor_b32 v240, v240, v243\nCvaa 180,184,64,176\nEX 188\nMvaa 180,188,68\nTa 184,208,8192\nv_lshlrev_b32 v240, 4, v240\nMvaa 180,192,72\nEX 189\n"
"Mvaa 180,196,76\nTa 186,208,12288\nCV 200,188\nCvaa 184,200,64,176\nEX 190\nMvaa 184,204,68\nDtL 211,44,75,1024\nMvaa 184,208,72\nEX 191\n"
"Mvaa 184,212,76\nTa 188,209,8192\nCV 201,190\nMvav 64,216,196\nEX 192\nMvav 68,220,196\nTa 190,209,12288\nv_lshl_add_u32 v240, v250, 6, v240\n"
"Mvav 72,224,196\nEX 193\nMvav 76,228,196\nTa 192,208,8448\nCV 202,192\nMvav 80,232,196\ns_waitcnt vmcnt(8)\nEX 194\nMvav 84,236,196\n"
"DtL 212,44,75,2048\nMvav 88,240,196\nEX 195\nMvav 92,244,196\ns_waitcnt lgkmcnt(2)\nTa 194,208,12544\nCV 203,194\nCvaa 188,248,0,160\n"
"Ta 196,209,8448\nv_lshl_add_u32 v240, v248, 10, v240\nMvaa 188,252,4\nEX 180\nMvva 188,216,8\nTa 198,209,12544\nv_lshl_add_u32 v240, v249, 12, v240\n"
"Mvva 188,220,12\ns_mul_i32 s88, s83, 8448\nEX 181\nv_smfmac_f32_16x16x64_bf16 a[164:167], a[180:183], v[196:203], v251\nTa 200,208,8704\nCV 196,180\n"
"Cvva 192,224,0,160\nEX 182\nMvva 192,228,4\n.Lfa_pd17936:\ns_cbranch_execz .Lfa_wr9\nDtL 213,44,75,3072\nMvva 192,232,8\nEX 183\nMvva 192,236,12\n"
"Ta 202,208,12800\nCV 197,182\nMvav 96,216,200\nEX 184\nMvav 100,220,200\nTa 204,209,8704\nv_mul_u32_u24 v244, 544, v250\nMvav 104,224,200\nEX 185\n"
"Mvav 108,228,200\nTa 206,209,12800\nCV 198,184\nMvav 112,232,200\nEX 186\nMvav 116,236,200\nTa 208,208,8960\nv_lshl_add_u32 v244, v249, 10, v244\n"
"Mvav 120,240,200\nEX 187\nMvav 124,244,200\nTa 210,208,13056\nCV 199,186\nCvaa 180,248,16,164\nEX 188\nMvaa 180,252,20\nTa 212,209,8960\n"
"v_add_u32 v208, 0xfffe0000, v208\nMvva 180,216,24\nEX 189\nMvva 180,220,28\ns_memrealtime s[98:99]\nTa 214,209,13056\nCV 200,188\n"
"Cvva 184,224,16,164\nEX 190\nMvva 184,228,20\nv_add_u32 v209, 0xfffe0000, v209\nv_lshl_add_u32 v244, v248, 6, v244\nMvva 184,232,24\nEX 191\n"
"Mvva 184,236,28\nCV 201,190\nv_lshl_add_u32 v244, v243, 3, v244\nMvav 128,216,196\ns_waitcnt vmcnt(12) lgkmcnt(0)\nEX 192\nMvav 132,220,196\n"
"s_barrier\n"
"s_sub_u32 s95, s98, s93\ns_mov_b32 s93, s98\ns_sub_u32 s95, 102, s95\ns_cbranch_scc1 .Lfa_t78b\ns_add_u32 s93, s93, s95\n.Lfa_t78a:\ns_sub_u32 s95, s95, 4\ns_cbranch_scc1 .Lfa_t78b\ns_sleep 1\ns_branch .Lfa_t78a\n.Lfa_t78b:\n"
"Mvav 136,224,196\ns_add_u32 m0, s72, 0x12000\nEX 193\nMvav 140,228,196\ns_waitcnt vmcnt(8)\nRa 216,204,0\nCV 202,192\nMvav 144,232,196\nEX 194\n"
"Mvav 148,236,196\nDL 214,48,75,0\nMvav 152,240,196\nEX 195\nMvav 156,244,196\nRa 220,205,0\nCV 203,194\nCvaa 188,248,32,168\nRa 224,206,0\n"
"v_add_u32 v244, s88, v244\nMvaa 188,252,36\nEX 180\nMvva 188,216,40\nRa 228,207,0\nv_xor_b32 v245, 32, v244\nMvva 188,220,44\nEX 181\n"
"v_smfmac_f32_16x16x64_bf16 a[168:171], a[180:183], v[196:203], v251\nRa 232,204,4096\nCV 196,180\nCvva 192,224,32,168\nEX 182\nMvva 192,228,36\n"
"DL 215,48,75,1024\nMvva 192,232,40\nEX 183\nMvva 192,236,44\nRa 236,205,4096\nCV 197,182\nMvav 0,184,200\nEX 184\nMvav 4,188,200\nRa 240,206,4096\n"
"v_xor_b32 v246, 0x100, v244\nMvav 8,192,200\nEX 185\nMvav 12,196,200\nRa 244,207,4096\nCV 198,184\nMvav 16,200,200\ns_waitcnt vmcnt(8)\nEX 186\n"
"Mvav 20,204,200\nDL 214,48,75,2048\nMvav 24,208,200\nEX 187\nMvav 28,212,200\nCV 199,186\nv_xor_b32 v247, 0x120, v244\nCvaa 180,248,48,172\nEX 188\n"
"Mvaa 180,252,52\nv_add_u32 v241, 0x1c00, v240\nv_add_u32 v242, 0x3800, v240\nMvva 180,216,56\nEX 189\nMvva 180,220,60\nCV 200,188\n"
"v_add_u32 v243, 0x5400, v240\nCvva 184,224,48,172\ns_and_b32 s88, s62, 7\nEX 190\nMvva 184,228,52\ns_lshl_b32 s88, s88, 12\nDL 215,48,75,3072\n"
"Mvva 184,232,56\nEX 191\nMvva 184,236,60\nCV 201,190\nv_mul_u32_u24 v249, 61440, v249\nMvav 32,184,196\ns_add_u32 s75, s97, 0x12000\nEX 192\n"
"Mvav 36,188,196\ns_and_b32 s75, s75, 0x1ffff\nv_lshl_add_u32 v249, v253, 7, v249\nv_add_u32 v249, s88, v249\nMvav 40,192,196\n"
"s_add_u32 s75, s75, 0x40000\nEX 193\nMvav 44,196,196\ns_add_u32 m0, s72, 0x18000\nCV 202,192\nv_add_u32 v250, 0x40000, v249\nMvav 48,200,196\n"
"s_waitcnt vmcnt(8)\nEX 194\nMvav 52,204,196\ns_mul_i32 s88, s60, 12288\nDtL 210,44,75,0\nMvav 56,208,196\ns_add_u32 s88, s88, 0x10000\nEX 195\n"
"Mvav 60,212,196\nCV 203,194\nv_lshlrev_b32 v248, 4, v253\nCvaa 188,248,64,176\nTa 248,208,8192\nv_add_u32 v248, s88, v248\nMvaa 188,252,68\nEX 180\n"
"Mvva 188,216,72\nTa 250,208,12288\nMvva 188,220,76\nEX 181\nv_smfmac_f32_16x16x64_bf16 a[172:175], a[180:183], v[196:203], v251\nTa 252,209,8192\n"
"CV 196,180\nCvva 192,224,64,176\nEX 182\nMvva 192,228,68\nDtL 211,44,75,1024\nMvva 192,232,72\nEX 183\nMvva 192,236,76\nTa 254,209,12288\nCV 197,182\n"
"Mvav 64,184,200\nEX 184\nMvav 68,188,200\nTv 216,208,8448\nMvav 72,192,200\nEX 185\nMvav 76,196,200\nTv 218,208,12544\nCV 198,184\nMvav 80,200,200\n"
"s_waitcnt vmcnt(8)\nEX 186\nMvav 84,204,200\nDtL 212,44,75,2048\nMvav 88,208,200\nEX 187\nMvav 92,212,200\ns_waitcnt lgkmcnt(2)\nTv 220,209,8448\n"
"CV 199,186\nCvaa 180,216,0,160\nEX 188\nMvaa 180,220,4\nTv 222,209,12544\nMvaa 180,224,8\nEX 189\nMvaa 180,228,12\nTv 224,208,8704\nCV 200,188\n"
"Cvaa 184,232,0,160\nEX 190\nMvaa 184,236,4\nDtL 213,44,75,3072\nMvaa 184,240,8\nEX 191\nMvaa 184,244,12\nTv 226,208,12800\nCV 201,190\n"
"Mvav 96,184,196\nEX 192\nMvav 100,188,196\nTv 228,209,8704\nMvav 104,192,196\nEX 193\nMvav 108,196,196\nTv 230,209,12800\nCV 202,192\n"
"Mvav 112,200,196\nEX 194\nMvav 116,204,196\nTv 232,208,8960\nMvav 120,208,196\nEX 195\nMvav 124,212,196\nTv 234,208,13056\nCV 203,194\n"
"Cvaa 188,216,16,164\nTv 236,209,8960\nMvaa 188,220,20\nEX 180\nMvaa 188,224,24\nTv 238,209,13056\nMvaa 188,228,28\nEX 181\n"
"v_smfmac_f32_16x16x64_bf16 a[176:179], a[180:183], v[196:203], v251\ns_memrealtime s[98:99]\nCV 196,180\nCvaa 192,232,16,164\nEX 182\n"
"Mvaa 192,236,20\nMvaa 192,240,24\nEX 183\nMvaa 192,244,28\nCV 197,182\nMvav 128,184,200\ns_waitcnt vmcnt(12) lgkmcnt(0)\nEX 184\nMvav 132,188,200\n"
"s_barrier\n"
"s_sub_u32 s95, s98, s93\ns_mov_b32 s93, s98\ns_sub_u32 s95, 102, s95\ns_cbranch_scc1 .Lfa_t79b\ns_add_u32 s93, s93, s95\n.Lfa_t79a:\ns_sub_u32 s95, s95, 4\ns_cbranch_scc1 .Lfa_t79b\ns_sleep 1\ns_branch .Lfa_t79a\n.Lfa_t79b:\n"
"Mvav 136,192,200\ns_add_u32 m0, s72, 0x1a000\nEX 185\nMvav 140,196,200\ns_waitcnt vmcnt(8)\nRa 184,204,32768\nCV 198,184\nMvav 144,200,200\nEX 186\n"
"Mvav 148,204,200\nDL 214,48,75,0\nMvav 152,208,200\nEX 187\nMvav 156,212,200\nRa 188,205,32768\nCV 199,186\nCvaa 180,216,32,168\nEX 188\n"
"Mvaa 180,220,36\nRa 192,206,32768\nMvaa 180,224,40\nEX 189\nMvaa 180,228,44\nRa 196,207,32768\nCV 200,188\nCvaa 184,232,32,168\nEX 190\n"
"Mvaa 184,236,36\nDL 215,48,75,1024\nMvaa 184,240,40\nEX 191\nMvaa 184,244,44\nRa 200,204,36864\nCV 201,190\nMvav 0,248,196\nEX 192\nMvav 4,252,196\n"
"Ra 204,205,36864\nv_add_u32 v204, 0x10000, v204\nMvvv 8,216,196\nEX 193\nMvvv 12,220,196\nRa 208,206,36864\nCV 202,192\nMvvv 16,224,196\n"
"s_waitcnt vmcnt(8)\nEX 194\nMvvv 20,228,196\nDL 214,48,75,2048\nMvvv 24,232,196\nEX 195\nMvvv 28,236,196\nRa 212,207,36864\nCV 203,194\n"
"Cvaa 188,216,48,172\nv_add_u32 v205, 0x10000, v205\nv_add_u32 v206, 0x10000, v206\nMvaa 188,220,52\nEX 180\nMvaa 188,224,56\n"
"v_add_u32 v207, 0x10000, v207\nMvaa 188,228,60\nEX 181\nv_smfmac_f32_16x16x64_bf16 a[160:163], a[180:183], v[196:203], v251\nCV 196,180\n"
"Cvaa 192,232,48,172\nEX 182\nMvaa 192,236,52\nDL 215,48,75,3072\nMvaa 192,240,56\nEX 183\nMvaa 192,244,60\nCV 197,182\nMvav 32,248,200\n"
"s_add_u32 s75, s97, 0x14000\nEX 184\nMvav 36,252,200\ns_and_b32 s75, s75, 0x1ffff\nMvvv 40,216,200\ns_add_u32 s75, s75, 0x40000\nEX 185\n"
"Mvvv 44,220,200\ns_add_u32 m0, s72, 0x20000\nCV 198,184\nMvvv 48,224,200\ns_waitcnt vmcnt(8)\nEX 186\nMvvv 52,228,200\nDtL 210,44,75,0\n"
"Mvvv 56,232,200\nEX 187\nMvvv 60,236,200\nCV 199,186\nCvaa 180,216,64,176\nEX 188\nMvaa 180,220,68\nTa 216,208,40960\nMvaa 180,224,72\nEX 189\n"
"Mvaa 180,228,76\nTa 218,208,45056\nCV 200,188\nCvaa 184,232,64,176\nEX 190\nMvaa 184,236,68\nDtL 211,44,75,1024\nMvaa 184,240,72\nEX 191\n"
"Mvaa 184,244,76\nTa 220,209,40960\nCV 201,190\nMvav 64,248,196\nEX 192\nMvav 68,252,196\nTa 222,209,45056\nMvvv 72,216,196\nEX 193\nMvvv 76,220,196\n"
"Ta 224,208,41216\nCV 202,192\nMvvv 80,224,196\ns_waitcnt vmcnt(8)\nEX 194\nMvvv 84,228,196\nDtL 212,44,75,2048\nMvvv 88,232,196\nEX 195\n"
"Mvvv 92,236,196\ns_waitcnt lgkmcnt(2)\nTa 226,208,45312\nCV 203,194\nCvaa 188,184,0,160\nTa 228,209,41216\nMvaa 188,188,4\nEX 180\nMvaa 188,192,8\n"
"Ta 230,209,45312\nMvaa 188,196,12\nEX 181\nv_smfmac_f32_16x16x64_bf16 a[164:167], a[180:183], v[196:203], v251\nTa 232,208,41472\nCV 196,180\n"
"Cvaa 192,200,0,160\nEX 182\nMvaa 192,204,4\nDtL 213,44,75,3072\nMvaa 192,208,8\nEX 183\nMvaa 192,212,12\nTa 234,208,45568\nCV 197,182\n"
"Mvav 96,248,200\nEX 184\nMvav 100,252,200\nTa 236,209,41472\nMvvv 104,216,200\nEX 185\nMvvv 108,220,200\nTa 238,209,45568\nCV 198,184\n"
"Mvvv 112,224,200\nEX 186\nMvvv 116,228,200\nTa 240,208,41728\nMvvv 120,232,200\nEX 187\nMvvv 124,236,200\nTa 242,208,45824\nCV 199,186\n"
"Cvaa 180,184,16,164\nEX 188\nMvaa 180,188,20\nTa 244,209,41728\nv_add_u32 v208, 0x10000, v208\nMvaa 180,192,24\nEX 189\nMvaa 180,196,28\n"
"s_memrealtime s[98:99]\nTa 246,209,45824\nCV 200,188\nCvaa 184,200,16,164\nEX 190\nMvaa 184,204,20\nv_add_u32 v209, 0x10000, v209\nMvaa 184,208,24\n"
"EX 191\nMvaa 184,212,28\nCV 201,190\nMvav 128,248,196\ns_waitcnt vmcnt(12) lgkmcnt(0)\nEX 192\nMvav 132,252,196\ns_barrier\n"
"s_sub_u32 s95, s98, s93\ns_mov_b32 s93, s98\ns_sub_u32 s95, 102, s95\ns_cbranch_scc1 .Lfa_t80b\ns_add_u32 s93, s93, s95\n.Lfa_t80a:\ns_sub_u32 s95, s95, 4\ns_cbranch_scc1 .Lfa_t80b\ns_sleep 1\ns_branch .Lfa_t80a\n.Lfa_t80b:\n"
"Mvvv 136,216,196\ns_add_u32 m0, s72, 0x22000\nEX 193\nMvvv 140,220,196\ns_waitcnt vmcnt(8)\nRa 248,204,0\nCV 202,192\nMvvv 144,224,196\nEX 194\n"
"Mvvv 148,228,196\nDL 214,48,75,0\nMvvv 152,232,196\nEX 195\nMvvv 156,236,196\nRa 252,205,0\nCV 203,194\nCvaa 188,184,32,168\nRv 216,206,0\n"
"Mvaa 188,188,36\nEX 180\nMvaa 188,192,40\nRv 220,207,0\nMvaa 188,196,44\nEX 181\nv_smfmac_f32_16x16x64_bf16 a[168:171], a[180:183], v[196:203], v251\n"
"Rv 224,204,4096\nCV 196,180\nCvaa 192,200,32,168\nEX 182\nMvaa 192,204,36\nDL 215,48,75,1024\nMvaa 192,208,40\nEX 183\nMvaa 192,212,44\n"
"Rv 228,205,4096\nCV 197,182\nMvav 0,216,200\nEX 184\nMvav 4,220,200\nRv 232,206,4096\nMvav 8,224,200\nEX 185\nMvav 12,228,200\nRv 236,207,4096\n"
"CV 198,184\nMvav 16,232,200\ns_waitcnt vmcnt(8)\nEX 186\nMvav 20,236,200\nDL 214,48,75,2048\nMvav 24,240,200\nEX 187\nMvav 28,244,200\nCV 199,186\n"
"Cvaa 180,184,48,172\nEX 188\nMvaa 180,188,52\nMvaa 180,192,56\nEX 189\nMvaa 180,196,60\nCV 200,188\nCvaa 184,200,48,172\nEX 190\nMvaa 184,204,52\n"
"DL 215,48,75,3072\nMvaa 184,208,56\nEX 191\nMvaa 184,212,60\nCV 201,190\nMvav 32,216,196\ns_add_u32 s75, s97, 0x16000\nEX 192\nMvav 36,220,196\n"
"s_and_b32 s75, s75, 0x1ffff\nMvav 40,224,196\ns_add_u32 s75, s75, 0x40000\nEX 193\nMvav 44,228,196\ns_add_u32 m0, s72, 0x0\nCV 202,192\n"
"Mvav 48,232,196\ns_waitcnt vmcnt(8)\nEX 194\nMvav 52,236,196\nDtL 210,44,75,0\nMvav 56,240,196\nEX 195\nMvav 60,244,196\nCV 203,194\n"
"Cvaa 188,184,64,176\nTa 184,208,8192\nMvaa 188,188,68\nEX 180\nMvaa 188,192,72\nTa 186,208,12288\nMvaa 188,196,76\nEX 181\n"
"v_smfmac_f32_16x16x64_bf16 a[172:175], a[180:183], v[196:203], v251\nTa 188,209,8192\nCV 196,180\nCvaa 192,200,64,176\nEX 182\nMvaa 192,204,68\n"
"DtL 211,44,75,1024\nMvaa 192,208,72\nEX 183\nMvaa 192,212,76\nTa 190,209,12288\nCV 197,182\nMvav 64,216,200\nEX 184\nMvav 68,220,200\n"
"Ta 192,208,8448\nMvav 72,224,200\nEX 185\nMvav 76,228,200\nTa 194,208,12544\nCV 198,184\nMvav 80,232,200\ns_waitcnt vmcnt(8)\nEX 186\n"
"Mvav 84,236,200\n.Lfa_pd18512:\ns_cbranch_execz .Lfa_wr9\nDtL 212,44,75,2048\nMvav 88,240,200\nEX 187\nMvav 92,244,200\ns_waitcnt lgkmcnt(2)\n"
"Ta 196,209,8448\nCV 199,186\nCvaa 180,248,0,160\nEX 188\nMvaa 180,252,4\nTa 198,209,12544\nMvva 180,216,8\nEX 189\nMvva 180,220,12\nTa 200,208,8704\n"
"CV 200,188\nCvva 184,224,0,160\nEX 190\nMvva 184,228,4\nDtL 213,44,75,3072\nMvva 184,232,8\nEX 191\nMvva 184,236,12\nTa 202,208,12800\nCV 201,190\n"
"Mvav 96,216,196\nEX 192\nMvav 100,220,196\nTa 204,209,8704\nMvav 104,224,196\nEX 193\nMvav 108,228,196\nTa 206,209,12800\nCV 202,192\n"
"Mvav 112,232,196\nEX 194\nMvav 116,236,196\nTa 208,208,8960\nMvav 120,240,196\nEX 195\nMvav 124,244,196\nTa 210,208,13056\nCV 203,194\n"
"Cvaa 188,248,16,164\nTa 212,209,8960\nMvaa 188,252,20\nEX 180\nMvva 188,216,24\nTa 214,209,13056\nMvva 188,220,28\nEX 181\n"
"v_smfmac_f32_16x16x64_bf16 a[176:179], a[180:183], v[196:203], v251\ns_memrealtime s[98:99]\nCV 196,180\nCvva 192,224,16,164\nEX 182\n"
"Mvva 192,228,20\nMvva 192,232,24\nEX 183\nMvva 192,236,28\nCV 197,182\nMvav 128,216,200\ns_waitcnt vmcnt(12) lgkmcnt(0)\nEX 184\nMvav 132,220,200\n"
"s_barrier\n"
"s_mov_b64 exec, 0\ns_cmp_eq_u32 s60, 0\ns_cbranch_scc1 .Lfa_w11_0\ns_cmp_eq_u32 s60, 1\ns_cbranch_scc1 .Lfa_w11_1\ns_cmp_eq_u32 s60, 2\ns_cbranch_scc1 .Lfa_w11_2\ns_branch .Lfa_w11_3\n.Lfa_wr11:\ns_bitcmp1_b32 %13, 0\ns_cbranch_scc1 .Lfa_wr5\ns_mov_b64 exec, -1\ns_nop 4\n"
"s_sub_u32 s95, s98, s93\ns_mov_b32 s93, s98\ns_sub_u32 s95, 102, s95\ns_cbranch_scc1 .Lfa_t81b\ns_add_u32 s93, s93, s95\n.Lfa_t81a:\ns_sub_u32 s95, s95, 4\ns_cbranch_scc1 .Lfa_t81b\ns_sleep 1\ns_branch .Lfa_t81a\n.Lfa_t81b:\n"
"Mvav 136,224,200\ns_add_u32 m0, s72, 0x2000\nEX 185\nMvav 140,228,200\ns_waitcnt vmcnt(8)\nRa 216,204,32768\nCV 198,184\nMvav 144,232,200\nEX 186\n"
"Mvav 148,236,200\nDL 214,48,75,0\nMvav 152,240,200\nEX 187\nMvav 156,244,200\nRa 220,205,32768\nCV 199,186\nCvaa 180,248,32,168\nEX 188\n"
"Mvaa 180,252,36\nRa 224,206,32768\nMvva 180,216,40\nEX 189\nMvva 180,220,44\nRa 228,207,32768\nCV 200,188\nCvva 184,224,32,168\nEX 190\n"
"Mvva 184,228,36\nDL 215,48,75,1024\nMvva 184,232,40\nEX 191\nMvva 184,236,44\nRa 232,204,36864\nCV 201,190\nMvav 0,184,196\nEX 192\nMvav 4,188,196\n"
"Ra 236,205,36864\nv_add_u32 v204, 0x10000, v204\nMvav 8,192,196\nEX 193\nMvav 12,196,196\nRa 240,206,36864\nCV 202,192\nMvav 16,200,196\n"
"s_waitcnt vmcnt(8)\nEX 194\nMvav 20,204,196\nDL 214,48,75,2048\nMvav 24,208,196\nEX 195\nMvav 28,212,196\nRa 244,207,36864\nCV 203,194\n"
"Cvaa 188,248,48,172\nv_add_u32 v205, 0x10000, v205\nv_add_u32 v206, 0x10000, v206\nMvaa 188,252,52\nEX 180\nMvva 188,216,56\n"
"v_add_u32 v207, 0x10000, v207\nMvva 188,220,60\nEX 181\nv_smfmac_f32_16x16x64_bf16 a[160:163], a[180:183], v[196:203], v251\nCV 196,180\n"
"Cvva 192,224,48,172\nEX 182\nMvva 192,228,52\nDL 215,48,75,3072\nMvva 192,232,56\nEX 183\nMvva 192,236,60\nCV 197,182\nMvav 32,184,200\n"
"s_add_u32 s75, s97, 0x18000\nEX 184\nMvav 36,188,200\ns_and_b32 s75, s75, 0x1ffff\nMvav 40,192,200\ns_add_u32 s75, s75, 0x40000\nEX 185\n"
"Mvav 44,196,200\ns_add_u32 m0, s72, 0x8000\nCV 198,184\nMvav 48,200,200\ns_waitcnt vmcnt(8)\nEX 186\nMvav 52,204,200\nDtL 210,44,75,0\n"
"Mvav 56,208,200\nEX 187\nMvav 60,212,200\nCV 199,186\nCvaa 180,248,64,176\nEX 188\nMvaa 180,252,68\nTa 248,208,40960\nMvva 180,216,72\nEX 189\n"
"Mvva 180,220,76\nTa 250,208,45056\nCV 200,188\nCvva 184,224,64,176\nEX 190\nMvva 184,228,68\nDtL 211,44,75,1024\nMvva 184,232,72\nEX 191\n"
"Mvva 184,236,76\nTa 252,209,40960\nCV 201,190\nMvav 64,184,196\nEX 192\nMvav 68,188,196\nTa 254,209,45056\nMvav 72,192,196\nEX 193\nMvav 76,196,196\n"
"Tv 216,208,41216\nCV 202,192\nMvav 80,200,196\ns_waitcnt vmcnt(8)\nEX 194\nMvav 84,204,196\nDtL 212,44,75,2048\nMvav 88,208,196\nEX 195\n"
"Mvav 92,212,196\ns_waitcnt lgkmcnt(2)\nTv 218,208,45312\nCV 203,194\nCvaa 188,216,0,160\nTv 220,209,41216\nMvaa 188,220,4\nEX 180\nMvaa 188,224,8\n"
"Tv 222,209,45312\nMvaa 188,228,12\nEX 181\nv_smfmac_f32_16x16x64_bf16 a[164:167], a[180:183], v[196:203], v251\nTv 224,208,41472\nCV 196,180\n"
"Cvaa 192,232,0,160\nEX 182\nMvaa 192,236,4\nDtL 213,44,75,3072\nMvaa 192,240,8\nEX 183\nMvaa 192,244,12\nTv 226,208,45568\nCV 197,182\n"
"Mvav 96,184,200\nEX 184\nMvav 100,188,200\nTv 228,209,41472\nMvav 104,192,200\nEX 185\nMvav 108,196,200\nTv 230,209,45568\nCV 198,184\n"
"Mvav 112,200,200\nEX 186\nMvav 116,204,200\nTv 232,208,41728\nMvav 120,208,200\nEX 187\nMvav 124,212,200\nTv 234,208,45824\nCV 199,186\n"
"Cvaa 180,216,16,164\nEX 188\nMvaa 180,220,20\nTv 236,209,41728\nv_add_u32 v208, 0x10000, v208\nMvaa 180,224,24\nEX 189\nMvaa 180,228,28\n"
"s_memrealtime s[98:99]\nTv 238,209,45824\nCV 200,188\nCvaa 184,232,16,164\nEX 190\nMvaa 184,236,20\nv_add_u32 v209, 0x10000, v209\nMvaa 184,240,24\n"
"EX 191\nMvaa 184,244,28\nCV 201,190\nMvav 128,184,196\ns_waitcnt vmcnt(12) lgkmcnt(0)\nEX 192\nMvav 132,188,196\ns_barrier\n"
"s_sub_u32 s95, s98, s93\ns_mov_b32 s93, s98\ns_sub_u32 s95, 102, s95\ns_cbranch_scc1 .Lfa_t82b\ns_add_u32 s93, s93, s95\n.Lfa_t82a:\ns_sub_u32 s95, s95, 4\ns_cbranch_scc1 .Lfa_t82b\ns_sleep 1\ns_branch .Lfa_t82a\n.Lfa_t82b:\n"
"Mvav 136,192,196\ns_add_u32 m0, s72, 0xa000\nEX 193\nMvav 140,196,196\ns_waitcnt vmcnt(8)\nRa 184,204,0\nCV 202,192\nMvav 144,200,196\nEX 194\n"
"Mvav 148,204,196\nDL 214,48,75,0\nMvav 152,208,196\nEX 195\nMvav 156,212,196\nRa 188,205,0\nCV 203,194\nCvaa 188,216,32,168\nRa 192,206,0\n"
"Mvaa 188,220,36\nEX 180\nMvaa 188,224,40\nRa 196,207,0\nMvaa 188,228,44\nEX 181\nv_smfmac_f32_16x16x64_bf16 a[168:171], a[180:183], v[196:203], v251\n"
"Ra 200,204,4096\nCV 196,180\nCvaa 192,232,32,168\nEX 182\nMvaa 192,236,36\nDL 215,48,75,1024\nMvaa 192,240,40\nEX 183\nMvaa 192,244,44\n"
"Ra 204,205,4096\nCV 197,182\nMvav 0,248,200\nEX 184\nMvav 4,252,200\nRa 208,206,4096\nv_add_u32 v204, 0xfffe0000, v204\nMvvv 8,216,200\nEX 185\n"
"Mvvv 12,220,200\nRa 212,207,4096\nCV 198,184\nMvvv 16,224,200\ns_waitcnt vmcnt(8)\nEX 186\nMvvv 20,228,200\nDL 214,48,75,2048\nMvvv 24,232,200\n"
"EX 187\nMvvv 28,236,200\nCV 199,186\nv_add_u32 v205, 0xfffe0000, v205\nCvaa 180,216,48,172\nEX 188\nMvaa 180,220,52\n"
"v_add_u32 v206, 0xfffe0000, v206\nv_add_u32 v207, 0xfffe0000, v207\nMvaa 180,224,56\nEX 189\nMvaa 180,228,60\nCV 200,188\nCvaa 184,232,48,172\n"
"EX 190\nMvaa 184,236,52\nDL 215,48,75,3072\nMvaa 184,240,56\nEX 191\nMvaa 184,244,60\nCV 201,190\nMvav 32,248,196\ns_add_u32 s75, s97, 0x1a000\n"
"EX 192\nMvav 36,252,196\ns_and_b32 s75, s75, 0x1ffff\nMvvv 40,216,196\ns_add_u32 s75, s75, 0x40000\nEX 193\nMvvv 44,220,196\n"
"s_add_u32 m0, s72, 0x10000\nCV 202,192\nMvvv 48,224,196\ns_waitcnt vmcnt(8)\nEX 194\nMvvv 52,228,196\nDtL 210,44,75,0\nMvvv 56,232,196\nEX 195\n"
"Mvvv 60,236,196\nCV 203,194\nCvaa 188,216,64,176\nTa 216,208,8192\nMvaa 188,220,68\nEX 180\nMvaa 188,224,72\nTa 218,208,12288\nMvaa 188,228,76\n"
"EX 181\nv_smfmac_f32_16x16x64_bf16 a[172:175], a[180:183], v[196:203], v251\nTa 220,209,8192\nCV 196,180\nCvaa 192,232,64,176\nEX 182\n"
"Mvaa 192,236,68\nDtL 211,44,75,1024\nMvaa 192,240,72\nEX 183\nMvaa 192,244,76\nTa 222,209,12288\nCV 197,182\nMvav 64,248,200\nEX 184\n"
"Mvav 68,252,200\nTa 224,208,8448\nMvvv 72,216,200\nEX 185\nMvvv 76,220,200\nTa 226,208,12544\nCV 198,184\nMvvv 80,224,200\ns_waitcnt vmcnt(8)\n"
"EX 186\nMvvv 84,228,200\nDtL 212,44,75,2048\nMvvv 88,232,200\nEX 187\nMvvv 92,236,200\ns_waitcnt lgkmcnt(2)\nTa 228,209,8448\nCV 199,186\n"
"Cvaa 180,184,0,160\nEX 188\nMvaa 180,188,4\nTa 230,209,12544\nMvaa 180,192,8\nEX 189\nMvaa 180,196,12\nTa 232,208,8704\nCV 200,188\n"
"Cvaa 184,200,0,160\nEX 190\nMvaa 184,204,4\nDtL 213,44,75,3072\nMvaa 184,208,8\nEX 191\nMvaa 184,212,12\nTa 234,208,12800\nCV 201,190\n"
"Mvav 96,248,196\nEX 192\nMvav 100,252,196\nTa 236,209,8704\nMvvv 104,216,196\nEX 193\nMvvv 108,220,196\nTa 238,209,12800\nCV 202,192\n"
"Mvvv 112,224,196\nEX 194\nMvvv 116,228,196\nTa 240,208,8960\nMvvv 120,232,196\nEX 195\nMvvv 124,236,196\nTa 242,208,13056\nCV 203,194\n"
"Cvaa 188,184,16,164\nTa 244,209,8960\nMvaa 188,188,20\nEX 180\nMvaa 188,192,24\nTa 246,209,13056\nv_add_u32 v208, 0xfffe0000, v208\nMvaa 188,196,28\n"
"EX 181\nv_smfmac_f32_16x16x64_bf16 a[176:179], a[180:183], v[196:203], v251\ns_memrealtime s[98:99]\nCV 196,180\nv_add_u32 v209, 0xfffe0000, v209\n"
"Cvaa 192,200,16,164\nEX 182\nMvaa 192,204,20\nMvaa 192,208,24\nEX 183\nMvaa 192,212,28\nCV 197,182\nMvav 128,248,200\ns_waitcnt vmcnt(12) lgkmcnt(0)\n"
"EX 184\nMvav 132,252,200\ns_barrier\n"
"s_sub_u32 s95, s98, s93\ns_mov_b32 s93, s98\ns_sub_u32 s95, 102, s95\ns_cbranch_scc1 .Lfa_t83b\ns_add_u32 s93, s93, s95\n.Lfa_t83a:\ns_sub_u32 s95, s95, 4\ns_cbranch_scc1 .Lfa_t83b\ns_sleep 1\ns_branch .Lfa_t83a\n.Lfa_t83b:\n"
"Mvvv 136,216,200\ns_add_u32 m0, s72, 0x12000\nEX 185\nMvvv 140,220,200\ns_waitcnt vmcnt(8)\nRa 248,204,0\nCV 198,184\nMvvv 144,224,200\nEX 186\n"
"Mvvv 148,228,200\nDL 214,48,75,0\nMvvv 152,232,200\nEX 187\nMvvv 156,236,200\nRa 252,205,0\nCV 199,186\nCvaa 180,184,32,168\nEX 188\nMvaa 180,188,36\n"
"Rv 216,206,0\nMvaa 180,192,40\nEX 189\nMvaa 180,196,44\nRv 220,207,0\nCV 200,188\nCvaa 184,200,32,168\nEX 190\nMvaa 184,204,36\nDL 215,48,75,1024\n"
"Mvaa 184,208,40\nEX 191\nMvaa 184,212,44\nRv 224,204,4096\nCV 201,190\nMvav 0,216,196\nEX 192\nMvav 4,220,196\nRv 228,205,4096\nMvav 8,224,196\n"
"EX 193\nMvav 12,228,196\nRv 232,206,4096\nCV 202,192\nMvav 16,232,196\ns_waitcnt vmcnt(8)\nEX 194\nMvav 20,236,196\nDL 214,48,75,2048\n"
"Mvav 24,240,196\nEX 195\nMvav 28,244,196\nRv 236,207,4096\nCV 203,194\nCvaa 188,184,48,172\nMvaa 188,188,52\nEX 180\nMvaa 188,192,56\n"
"Mvaa 188,196,60\nEX 181\nv_smfmac_f32_16x16x64_bf16 a[160:163], a[180:183], v[196:203], v251\nCV 196,180\nCvaa 192,200,48,172\nEX 182\n"
"Mvaa 192,204,52\nDL 215,48,75,3072\nMvaa 192,208,56\nEX 183\nMvaa 192,212,60\nCV 197,182\nMvav 32,216,200\ns_add_u32 s75, s97, 0x1c000\nEX 184\n"
"Mvav 36,220,200\ns_and_b32 s75, s75, 0x1ffff\nMvav 40,224,200\ns_add_u32 s75, s75, 0x40000\nEX 185\nMvav 44,228,200\ns_add_u32 m0, s72, 0x18000\n"
"CV 198,184\nMvav 48,232,200\ns_waitcnt vmcnt(8)\nEX 186\nMvav 52,236,200\nDtL 210,44,75,0\nMvav 56,240,200\nEX 187\nMvav 60,244,200\nCV 199,186\n"
"Cvaa 180,184,64,176\nEX 188\nMvaa 180,188,68\nTa 184,208,8192\nMvaa 180,192,72\nEX 189\nMvaa 180,196,76\nTa 186,208,12288\nCV 200,188\n"
"Cvaa 184,200,64,176\nEX 190\nMvaa 184,204,68\nDtL 211,44,75,1024\nMvaa 184,208,72\n.Lfa_pd19073:\ns_cbranch_execz .Lfa_wr9\nEX 191\nMvaa 184,212,76\n"
"Ta 188,209,8192\nCV 201,190\nMvav 64,216,196\nEX 192\nMvav 68,220,196\nTa 190,209,12288\nMvav 72,224,196\nEX 193\nMvav 76,228,196\nTa 192,208,8448\n"
"CV 202,192\nMvav 80,232,196\ns_waitcnt vmcnt(8)\nEX 194\nMvav 84,236,196\nDtL 212,44,75,2048\nMvav 88,240,196\nEX 195\nMvav 92,244,196\n"
"s_waitcnt lgkmcnt(2)\nTa 194,208,12544\nCV 203,194\nCvaa 188,248,0,160\nTa 196,209,8448\nMvaa 188,252,4\nEX 180\nMvva 188,216,8\nTa 198,209,12544\n"
"Mvva 188,220,12\nEX 181\nv_smfmac_f32_16x16x64_bf16 a[164:167], a[180:183], v[196:203], v251\nTa 200,208,8704\nCV 196,180\nCvva 192,224,0,160\n"
"EX 182\nMvva 192,228,4\nDtL 213,44,75,3072\nMvva 192,232,8\nEX 183\nMvva 192,236,12\nTa 202,208,12800\nCV 197,182\nMvav 96,216,200\nEX 184\n"
"Mvav 100,220,200\nTa 204,209,8704\nMvav 104,224,200\nEX 185\nMvav 108,228,200\nTa 206,209,12800\nCV 198,184\nMvav 112,232,200\nEX 186\n"
"Mvav 116,236,200\nTa 208,208,8960\nMvav 120,240,200\nEX 187\nMvav 124,244,200\nTa 210,208,13056\nCV 199,186\nCvaa 180,248,16,164\nEX 188\n"
"Mvaa 180,252,20\nTa 212,209,8960\nMvva 180,216,24\nEX 189\nMvva 180,220,28\ns_memrealtime s[98:99]\nTa 214,209,13056\nCV 200,188\n"
"Cvva 184,224,16,164\nEX 190\nMvva 184,228,20\nMvva 184,232,24\nEX 191\nMvva 184,236,28\nCV 201,190\nMvav 128,216,196\ns_waitcnt vmcnt(12) lgkmcnt(0)\n"
"EX 192\nMvav 132,220,196\ns_barrier\n"
"s_sub_u32 s95, s98, s93\ns_mov_b32 s93, s98\ns_sub_u32 s95, 102, s95\ns_cbranch_scc1 .Lfa_t84b\ns_add_u32 s93, s93, s95\n.Lfa_t84a:\ns_sub_u32 s95, s95, 4\ns_cbranch_scc1 .Lfa_t84b\ns_sleep 1\ns_branch .Lfa_t84a\n.Lfa_t84b:\n"
"Mvav 136,224,196\ns_add_u32 m0, s72, 0x1a000\nEX 193\nMvav 140,228,196\ns_waitcnt vmcnt(8)\nRa 216,204,32768\nCV 202,192\nMvav 144,232,196\nEX 194\n"
"Mvav 148,236,196\nDL 214,48,75,0\nMvav 152,240,196\nEX 195\nMvav 156,244,196\nRa 220,205,32768\nCV 203,194\nCvaa 188,248,32,168\nRa 224,206,32768\n"
"Mvaa 188,252,36\nEX 180\nMvva 188,216,40\nRa 228,207,32768\nMvva 188,220,44\nEX 181\n"
"v_smfmac_f32_16x16x64_bf16 a[168:171], a[180:183], v[196:203], v251\nRa 232,204,36864\nCV 196,180\nCvva 192,224,32,168\nEX 182\nMvva 192,228,36\n"
"DL 215,48,75,1024\nMvva 192,232,40\nEX 183\nMvva 192,236,44\nRa 236,205,36864\nCV 197,182\nMvav 0,184,200\nEX 184\nMvav 4,188,200\nRa 240,206,36864\n"
"v_add_u32 v204, 0x10000, v204\nMvav 8,192,200\nEX 185\nMvav 12,196,200\nRa 244,207,36864\nCV 198,184\nMvav 16,200,200\ns_waitcnt vmcnt(8)\nEX 186\n"
"Mvav 20,204,200\nDL 214,48,75,2048\nMvav 24,208,200\nEX 187\nMvav 28,212,200\nCV 199,186\nv_add_u32 v205, 0x10000, v205\nCvaa 180,248,48,172\nEX 188\n"
"Mvaa 180,252,52\nv_add_u32 v206, 0x10000, v206\nv_add_u32 v207, 0x10000, v207\nMvva 180,216,56\nEX 189\nMvva 180,220,60\nCV 200,188\n"
"Cvva 184,224,48,172\nEX 190\nMvva 184,228,52\nDL 215,48,75,3072\nMvva 184,232,56\nEX 191\nMvva 184,236,60\nCV 201,190\nMvav 32,184,196\n"
"s_add_u32 s75, s97, 0x1e000\nEX 192\nMvav 36,188,196\ns_and_b32 s75, s75, 0x1ffff\nMvav 40,192,196\ns_add_u32 s75, s75, 0x40000\nEX 193\n"
"Mvav 44,196,196\ns_add_u32 m0, s72, 0x20000\nCV 202,192\nMvav 48,200,196\ns_waitcnt vmcnt(8)\nEX 194\nMvav 52,204,196\nDtL 210,44,75,0\n"
"Mvav 56,208,196\nEX 195\nMvav 60,212,196\nCV 203,194\nCvaa 188,248,64,176\nTa 248,208,40960\nMvaa 188,252,68\nEX 180\nMvva 188,216,72\n"
"Ta 250,208,45056\nMvva 188,220,76\nEX 181\nv_smfmac_f32_16x16x64_bf16 a[172:175], a[180:183], v[196:203], v251\nTa 252,209,40960\nCV 196,180\n"
"Cvva 192,224,64,176\nEX 182\nMvva 192,228,68\nDtL 211,44,75,1024\nMvva 192,232,72\nEX 183\nMvva 192,236,76\nTa 254,209,45056\nCV 197,182\n"
"Mvav 64,184,200\nEX 184\nMvav 68,188,200\nTv 216,208,41216\nMvav 72,192,200\nEX 185\nMvav 76,196,200\nTv 218,208,45312\nCV 198,184\nMvav 80,200,200\n"
"s_waitcnt vmcnt(8)\nEX 186\nMvav 84,204,200\nDtL 212,44,75,2048\nMvav 88,208,200\nEX 187\nMvav 92,212,200\ns_waitcnt lgkmcnt(2)\nTv 220,209,41216\n"
"CV 199,186\nCvaa 180,216,0,160\nEX 188\nMvaa 180,220,4\nTv 222,209,45312\nMvaa 180,224,8\nEX 189\nMvaa 180,228,12\nTv 224,208,41472\nCV 200,188\n"
"Cvaa 184,232,0,160\nEX 190\nMvaa 184,236,4\nDtL 213,44,75,3072\nMvaa 184,240,8\nEX 191\nMvaa 184,244,12\nTv 226,208,45568\nCV 201,190\n"
"Mvav 96,184,196\nEX 192\nMvav 100,188,196\nTv 228,209,41472\nMvav 104,192,196\nEX 193\nMvav 108,196,196\nTv 230,209,45568\nCV 202,192\n"
"Mvav 112,200,196\nEX 194\nMvav 116,204,196\nTv 232,208,41728\nMvav 120,208,196\nEX 195\nMvav 124,212,196\nTv 234,208,45824\nCV 203,194\n"
"Cvaa 188,216,16,164\nTv 236,209,41728\nMvaa 188,220,20\nEX 180\nMvaa 188,224,24\nTv 238,209,45824\nv_add_u32 v208, 0x10000, v208\nMvaa 188,228,28\n"
"EX 181\nv_smfmac_f32_16x16x64_bf16 a[176:179], a[180:183], v[196:203], v251\ns_memrealtime s[98:99]\nCV 196,180\nv_add_u32 v209, 0x10000, v209\n"
"Cvaa 192,232,16,164\nEX 182\nMvaa 192,236,20\nMvaa 192,240,24\nEX 183\nMvaa 192,244,28\nCV 197,182\nMvav 128,184,200\ns_waitcnt vmcnt(12) lgkmcnt(0)\n"
"EX 184\nMvav 132,188,200\ns_barrier\n"
"s_sub_u32 s95, s98, s93\ns_mov_b32 s93, s98\ns_sub_u32 s95, 102, s95\ns_cbranch_scc1 .Lfa_t85b\ns_add_u32 s93, s93, s95\n.Lfa_t85a:\ns_sub_u32 s95, s95, 4\ns_cbranch_scc1 .Lfa_t85b\ns_sleep 1\ns_branch .Lfa_t85a\n.Lfa_t85b:\n"
"Mvav 136,192,200\ns_add_u32 m0, s72, 0x22000\nEX 185\nMvav 140,196,200\ns_waitcnt vmcnt(8)\nRa 184,204,0\nCV 198,184\nMvav 144,200,200\nEX 186\n"
"Mvav 148,204,200\nDL 214,48,75,0\nMvav 152,208,200\nEX 187\nMvav 156,212,200\nRa 188,205,0\nCV 199,186\nCvaa 180,216,32,168\nEX 188\nMvaa 180,220,36\n"
"Ra 192,206,0\nMvaa 180,224,40\nEX 189\nMvaa 180,228,44\nRa 196,207,0\nCV 200,188\nCvaa 184,232,32,168\nEX 190\nMvaa 184,236,36\nDL 215,48,75,1024\n"
"Mvaa 184,240,40\nEX 191\nMvaa 184,244,44\nRa 200,204,4096\nCV 201,190\nMvav 0,248,196\nEX 192\nMvav 4,252,196\nRa 204,205,4096\nMvvv 8,216,196\n"
"EX 193\nMvvv 12,220,196\nRa 208,206,4096\nCV 202,192\nMvvv 16,224,196\ns_waitcnt vmcnt(8)\nEX 194\nMvvv 20,228,196\nDL 214,48,75,2048\n"
"Mvvv 24,232,196\nEX 195\nMvvv 28,236,196\nRa 212,207,4096\nCV 203,194\nCvaa 188,216,48,172\nMvaa 188,220,52\nEX 180\nMvaa 188,224,56\n"
"Mvaa 188,228,60\nEX 181\nv_smfmac_f32_16x16x64_bf16 a[160:163], a[180:183], v[196:203], v251\nCV 196,180\nCvaa 192,232,48,172\nEX 182\n"
"Mvaa 192,236,52\nDL 215,48,75,3072\nMvaa 192,240,56\nEX 183\nMvaa 192,244,60\nCV 197,182\nMvav 32,248,200\nEX 184\nMvav 36,252,200\nMvvv 40,216,200\n"
"EX 185\nMvvv 44,220,200\nCV 198,184\nMvvv 48,224,200\nEX 186\nMvvv 52,228,200\nMvvv 56,232,200\nEX 187\nMvvv 60,236,200\nCV 199,186\n"
"Cvaa 180,216,64,176\nEX 188\nMvaa 180,220,68\nTa 216,208,8192\nMvaa 180,224,72\nEX 189\nMvaa 180,228,76\nTa 218,208,12288\nCV 200,188\n"
"Cvaa 184,232,64,176\nEX 190\nMvaa 184,236,68\nTa 220,209,8192\nMvaa 184,240,72\nEX 191\nMvaa 184,244,76\nTa 222,209,12288\nCV 201,190\n"
"Mvav 64,248,196\nEX 192\nMvav 68,252,196\nTa 224,208,8448\nMvvv 72,216,196\nEX 193\nMvvv 76,220,196\nTa 226,208,12544\nCV 202,192\nMvvv 80,224,196\n"
"EX 194\nMvvv 84,228,196\nTa 228,209,8448\nMvvv 88,232,196\nEX 195\nMvvv 92,236,196\ns_waitcnt lgkmcnt(3)\nTa 230,209,12544\nCV 203,194\n"
"Cvaa 188,184,0,160\nTa 232,208,8704\nMvaa 188,188,4\nEX 180\nMvaa 188,192,8\nTa 234,208,12800\nMvaa 188,196,12\nEX 181\n"
"v_smfmac_f32_16x16x64_bf16 a[164:167], a[180:183], v[196:203], v251\nTa 236,209,8704\nCV 196,180\nCvaa 192,200,0,160\nEX 182\nMvaa 192,204,4\n"
"Ta 238,209,12800\nMvaa 192,208,8\nEX 183\nMvaa 192,212,12\nTa 240,208,8960\nCV 197,182\nMvav 96,248,200\nEX 184\nMvav 100,252,200\nTa 242,208,13056\n"
"Mvvv 104,216,200\nEX 185\nMvvv 108,220,200\nTa 244,209,8960\nCV 198,184\nMvvv 112,224,200\nEX 186\nMvvv 116,228,200\nTa 246,209,13056\n"
"Mvvv 120,232,200\nEX 187\nMvvv 124,236,200\nCV 199,186\nCvaa 180,184,16,164\nEX 188\nMvaa 180,188,20\nMvaa 180,192,24\nEX 189\nMvaa 180,196,28\n"
"CV 200,188\nCvaa 184,200,16,164\nEX 190\nMvaa 184,204,20\nMvaa 184,208,24\nEX 191\nMvaa 184,212,28\nCV 201,190\nMvav 128,248,196\n"
"s_waitcnt vmcnt(8) lgkmcnt(0)\nEX 192\nMvav 132,252,196\ns_barrier\nMvvv 136,216,196\ns_add_u32 s76, s85, 0x80\nEX 193\nMvvv 140,220,196\n"
"s_and_b32 s76, s76, 0x3ff\nRa 248,204,32768\nCV 202,192\nMvvv 144,224,196\ns_add_u32 s76, s76, 0x0\nEX 194\nMvvv 148,228,196\ns_add_u32 m0, s94, 0x0\n"
"Ra 252,205,32768\nMvvv 152,232,196\nEX 195\nMvvv 156,236,196\nRv 216,206,32768\nCV 203,194\nCvaa 188,184,32,168\nDL 240,52,76,0\nMvaa 188,188,36\n"
"EX 180\nMvaa 188,192,40\nRv 220,207,32768\nMvaa 188,196,44\nEX 181\nv_smfmac_f32_16x16x64_bf16 a[168:171], a[180:183], v[196:203], v251\n"
"Rv 224,204,36864\nCV 196,180\nCvaa 192,200,32,168\nEX 182\nMvaa 192,204,36\nRv 228,205,36864\nv_add_u32 v204, 0x10000, v204\nMvaa 192,208,40\nEX 183\n"
"Mvaa 192,212,44\nRv 232,206,36864\nCV 197,182\nMvav 0,216,200\nEX 184\nMvav 4,220,200\nDL 241,52,76,1024\nMvav 8,224,200\nEX 185\nMvav 12,228,200\n"
"Rv 236,207,36864\nCV 198,184\nMvav 16,232,200\nEX 186\nMvav 20,236,200\nv_add_u32 v205, 0x10000, v205\nv_add_u32 v206, 0x10000, v206\n"
"Mvav 24,240,200\nEX 187\nMvav 28,244,200\nCV 199,186\nv_add_u32 v207, 0x10000, v207\nCvaa 180,184,48,172\nEX 188\nMvaa 180,188,52\nDL 242,52,76,2048\n"
"Mvaa 180,192,56\nEX 189\nMvaa 180,196,60\nCV 200,188\nCvaa 184,200,48,172\nEX 190\nMvaa 184,204,52\nMvaa 184,208,56\nEX 191\nMvaa 184,212,60\n"
"CV 201,190\nMvav 32,216,196\nEX 192\nMvav 36,220,196\nDL 243,52,76,3072\nMvav 40,224,196\nEX 193\nMvav 44,228,196\nCV 202,192\nMvav 48,232,196\n"
"EX 194\nMvav 52,236,196\ns_add_u32 s76, s85, 0x80\nMvav 56,240,196\ns_and_b32 s76, s76, 0x3ff\nEX 195\nMvav 60,244,196\ns_add_u32 s76, s76, 0x10000\n"
"CV 203,194\nCvaa 188,184,64,176\ns_add_u32 m0, s94, 0x4000\nTa 184,208,40960\nMvaa 188,188,68\nEX 180\nMvaa 188,192,72\nDL 240,52,76,0\n"
"Mvaa 188,196,76\nEX 181\nv_smfmac_f32_16x16x64_bf16 a[172:175], a[180:183], v[196:203], v251\nTa 186,208,45056\nCV 196,180\nCvaa 192,200,64,176\n"
"EX 182\nMvaa 192,204,68\nTa 188,209,40960\nMvaa 192,208,72\nEX 183\nMvaa 192,212,76\nTa 190,209,45056\nCV 197,182\nMvav 64,216,200\nEX 184\n"
"Mvav 68,220,200\nDL 241,52,76,1024\nMvav 72,224,200\n.Lfa_pd19647:\ns_cbranch_execz .Lfa_wr10\nEX 185\nMvav 76,228,200\nTa 192,208,41216\nCV 198,184\n"
"Mvav 80,232,200\nEX 186\nMvav 84,236,200\nTa 194,208,45312\nMvav 88,240,200\nEX 187\nMvav 92,244,200\ns_waitcnt lgkmcnt(2)\nTa 196,209,41216\n"
"CV 199,186\nCvaa 180,248,0,160\nEX 188\nMvaa 180,252,4\nDL 242,52,76,2048\nMvva 180,216,8\nEX 189\nMvva 180,220,12\nTa 198,209,45312\nCV 200,188\n"
"Cvva 184,224,0,160\nEX 190\nMvva 184,228,4\nTa 200,208,41472\nMvva 184,232,8\nEX 191\nMvva 184,236,12\nTa 202,208,45568\nCV 201,190\nMvav 96,216,196\n"
"EX 192\nMvav 100,220,196\nDL 243,52,76,3072\nMvav 104,224,196\nEX 193\nMvav 108,228,196\nTa 204,209,41472\nCV 202,192\nMvav 112,232,196\nEX 194\n"
"Mvav 116,236,196\nTa 206,209,45568\nMvav 120,240,196\nEX 195\nMvav 124,244,196\nTa 208,208,41728\nCV 203,194\nCvaa 188,248,16,164\nTa 210,208,45824\n"
"Mvaa 188,252,20\nEX 180\nMvva 188,216,24\nTa 212,209,41728\nv_add_u32 v208, 0x10000, v208\nMvva 188,220,28\nEX 181\n"
"v_smfmac_f32_16x16x64_bf16 a[176:179], a[180:183], v[196:203], v251\nTa 214,209,45824\nCV 196,180\nCvva 192,224,16,164\nEX 182\nMvva 192,228,20\n"
"v_add_u32 v209, 0x10000, v209\nMvva 192,232,24\nEX 183\nMvva 192,236,28\nCV 197,182\nMvav 128,216,200\ns_waitcnt vmcnt(8) lgkmcnt(0)\nEX 184\n"
"Mvav 132,220,200\ns_barrier\nMvav 136,224,200\ns_add_u32 s76, s85, 0x80\nEX 185\nMvav 140,228,200\ns_and_b32 s76, s76, 0x3ff\nRa 216,204,0\n"
"CV 198,184\nMvav 144,232,200\ns_add_u32 s76, s76, 0x40000\nEX 186\nMvav 148,236,200\ns_add_u32 m0, s94, 0x8000\nRa 220,205,0\nMvav 152,240,200\n"
"EX 187\nMvav 156,244,200\nRa 224,206,0\nCV 199,186\nCvaa 180,248,32,168\nEX 188\nMvaa 180,252,36\nDL 240,52,76,0\nMvva 180,216,40\nEX 189\n"
"Mvva 180,220,44\nRa 228,207,0\nCV 200,188\nCvva 184,224,32,168\nEX 190\nMvva 184,228,36\nRa 232,204,4096\nMvva 184,232,40\nEX 191\nMvva 184,236,44\n"
"Ra 236,205,4096\nCV 201,190\nMvav 0,184,196\nEX 192\nMvav 4,188,196\nDL 241,52,76,1024\nMvav 8,192,196\nEX 193\nMvav 12,196,196\nRa 240,206,4096\n"
"CV 202,192\nMvav 16,200,196\nEX 194\nMvav 20,204,196\nRa 244,207,4096\nMvav 24,208,196\nEX 195\nMvav 28,212,196\nCV 203,194\nCvaa 188,248,48,172\n"
"Mvaa 188,252,52\nEX 180\nMvva 188,216,56\nDL 242,52,76,2048\nMvva 188,220,60\nEX 181\n"
"v_smfmac_f32_16x16x64_bf16 a[160:163], a[180:183], v[196:203], v251\nCV 196,180\nCvva 192,224,48,172\nEX 182\nMvva 192,228,52\nMvva 192,232,56\n"
"EX 183\nMvva 192,236,60\nCV 197,182\nMvav 32,184,200\nEX 184\nMvav 36,188,200\nDL 243,52,76,3072\nMvav 40,192,200\nEX 185\nMvav 44,196,200\n"
"CV 198,184\nMvav 48,200,200\nEX 186\nMvav 52,204,200\ns_add_u32 s76, s85, 0x80\nMvav 56,208,200\ns_and_b32 s76, s76, 0x3ff\nEX 187\nMvav 60,212,200\n"
"s_add_u32 s76, s76, 0x50000\nCV 199,186\nCvaa 180,248,64,176\ns_add_u32 m0, s94, 0xc000\nEX 188\nMvaa 180,252,68\nDL 240,52,76,0\nMvva 180,216,72\n"
"EX 189\nMvva 180,220,76\nTa 248,208,8192\nCV 200,188\nCvva 184,224,64,176\nEX 190\nMvva 184,228,68\nTa 250,208,12288\nMvva 184,232,72\nEX 191\n"
"Mvva 184,236,76\nTa 252,209,8192\nCV 201,190\nMvav 64,184,196\nEX 192\nMvav 68,188,196\nDL 241,52,76,1024\nMvav 72,192,196\nEX 193\nMvav 76,196,196\n"
"Ta 254,209,12288\nCV 202,192\nMvav 80,200,196\nEX 194\nMvav 84,204,196\nTv 216,208,8448\nMvav 88,208,196\nEX 195\nMvav 92,212,196\n"
"s_waitcnt lgkmcnt(2)\nTv 218,208,12544\nCV 203,194\nCvaa 188,216,0,160\nTv 220,209,8448\nMvaa 188,220,4\nEX 180\nMvaa 188,224,8\nDL 242,52,76,2048\n"
"Mvaa 188,228,12\nEX 181\nv_smfmac_f32_16x16x64_bf16 a[164:167], a[180:183], v[196:203], v251\nTv 222,209,12544\nCV 196,180\nCvaa 192,232,0,160\n"
"EX 182\nMvaa 192,236,4\nTv 224,208,8704\nMvaa 192,240,8\nEX 183\nMvaa 192,244,12\nTv 226,208,12800\nCV 197,182\nMvav 96,184,200\nEX 184\n"
"Mvav 100,188,200\nDL 243,52,76,3072\nMvav 104,192,200\nEX 185\nMvav 108,196,200\nTv 228,209,8704\nCV 198,184\nMvav 112,200,200\nEX 186\n"
"Mvav 116,204,200\nTv 230,209,12800\nMvav 120,208,200\nEX 187\nMvav 124,212,200\nTv 232,208,8960\nCV 199,186\nCvaa 180,216,16,164\nEX 188\n"
"Mvaa 180,220,20\nTv 234,208,13056\nMvaa 180,224,24\nEX 189\nMvaa 180,228,28\nTv 236,209,8960\nCV 200,188\nCvaa 184,232,16,164\nEX 190\n"
"Mvaa 184,236,20\nTv 238,209,13056\nMvaa 184,240,24\nEX 191\nMvaa 184,244,28\nCV 201,190\nMvav 128,184,196\ns_waitcnt lgkmcnt(0)\nEX 192\n"
"Mvav 132,188,196\ns_barrier\nMvav 136,192,196\nEX 193\nMvav 140,196,196\nCV 202,192\nMvav 144,200,196\nEX 194\nMvav 148,204,196\n"
"ds_write_b128 v248, a[112:115] offset:0\nMvav 152,208,196\nEX 195\nMvav 156,212,196\nCV 203,194\nCvaa 188,216,32,168\nMvaa 188,220,36\nEX 180\n"
"Mvaa 188,224,40\nds_write_b128 v248, a[116:119] offset:1024\nMvaa 188,228,44\nEX 181\n"
"v_smfmac_f32_16x16x64_bf16 a[168:171], a[180:183], v[196:203], v251\nCV 196,180\nCvaa 192,232,32,168\nEX 182\nMvaa 192,236,36\n"
"buffer_load_dword v253, v249, s[52:55], 0 offen offset:0\nMvaa 192,240,40\nEX 183\nMvaa 192,244,44\nCV 197,182\nMvav 0,248,200\nEX 184\n"
"Mvav 4,252,200\nds_write_b128 v248, a[120:123] offset:2048\nMvvv 8,216,200\nEX 185\nMvvv 12,220,200\nCV 198,184\nMvvv 16,224,200\nEX 186\n"
"Mvvv 20,228,200\nds_write_b128 v248, a[124:127] offset:3072\nMvvv 24,232,200\nEX 187\nMvvv 28,236,200\nCV 199,186\nCvaa 180,216,48,172\nEX 188\n"
"Mvaa 180,220,52\nds_write_b128 v248, a[96:99] offset:4096\nMvaa 180,224,56\nEX 189\nMvaa 180,228,60\nCV 200,188\nCvaa 184,232,48,172\nEX 190\n"
"Mvaa 184,236,52\nds_write_b128 v248, a[100:103] offset:5120\nMvaa 184,240,56\nEX 191\nMvaa 184,244,60\nCV 201,190\nMvav 32,248,196\nEX 192\n"
"Mvav 36,252,196\nbuffer_load_dword v253, v250, s[52:55], 0 offen offset:0\nMvvv 40,216,196\nEX 193\nMvvv 44,220,196\nCV 202,192\nMvvv 48,224,196\n"
"EX 194\nMvvv 52,228,196\nds_write_b128 v248, a[104:107] offset:6144\nMvvv 56,232,196\nEX 195\nMvvv 60,236,196\nCV 203,194\nCvaa 188,216,64,176\n"
"Mvaa 188,220,68\nEX 180\nMvaa 188,224,72\nds_write_b128 v248, a[108:111] offset:7168\nMvaa 188,228,76\nEX 181\n"
"v_smfmac_f32_16x16x64_bf16 a[172:175], a[180:183], v[196:203], v251\nCV 196,180\nCvaa 192,232,64,176\nEX 182\nMvaa 192,236,68\n"
"ds_write_b128 v248, a[144:147] offset:8192\nMvaa 192,240,72\nEX 183\nMvaa 192,244,76\nCV 197,182\nMvav 64,248,200\nEX 184\nMvav 68,252,200\n"
"ds_write_b128 v248, a[148:151] offset:9216\nMvvv 72,216,200\nEX 185\nMvvv 76,220,200\nCV 198,184\nMvvv 80,224,200\nEX 186\nMvvv 84,228,200\n"
"ds_write_b128 v248, a[152:155] offset:10240\nMvvv 88,232,200\nEX 187\nMvvv 92,236,200\nCV 199,186\ns_nop 1\nMvav 96,248,196\nEX 188\n"
"Mvav 100,252,196\nEX 189\nMvvv 104,216,196\nEX 190\nCV 200,188\nMvvv 108,220,196\nEX 191\nMvvv 112,224,196\nEX 192\nCV 201,190\nMvvv 116,228,196\n"
"EX 193\nMvvv 120,232,196\nEX 194\nCV 202,192\nMvvv 124,236,196\nEX 195\ns_nop 0\nCV 203,194\ns_nop 1\nMvav 128,248,200\n"
"ds_write_b128 v248, a[156:159] offset:11264\nMvav 132,252,200\nMvvv 136,216,200\nMvvv 140,220,200\n"
"v_smfmac_f32_16x16x64_bf16 a[176:179], a[180:183], v[196:203], v251\nMvvv 144,224,200\nMvvv 148,228,200\nMvvv 152,232,200\n"
"s_waitcnt vmcnt(10) lgkmcnt(0)\nMvvv 156,236,200\ns_barrier\ns_nop 7\ns_cmp_eq_u32 s83, 0\ns_movk_i32 s88, 0x6000\ns_movk_i32 s89, 0xa000\n"
"s_cselect_b32 s88, s88, s89\nv_add_u32 v204, s88, v248\nRa 0,204,0\nRa 4,204,1024\nRa 8,204,2048\nRa 12,204,3072\nRa 16,204,4096\nRa 20,204,5120\n"
"Ra 24,204,6144\nRa 28,204,7168\nRa 32,204,8192\nRa 36,204,9216\nRa 40,204,10240\nRa 44,204,11264\nv_xor_b32 v249, 0x2000, v244\n"
"v_xor_b32 v250, 0x2000, v245\nv_xor_b32 v251, 0x2000, v246\nv_xor_b32 v252, 0x2000, v247\nTa 184,244,0\nTa 186,244,2048\nTa 188,244,4096\n"
"Ta 190,244,6144\nTa 192,249,0\nTa 194,249,2048\nTa 196,249,4096\nTa 198,249,6144\nTa 200,244,16384\nTa 202,244,18432\nTa 204,244,20480\n"
"Ta 206,244,22528\nTa 208,249,16384\nTa 210,249,18432\nTa 212,249,20480\nTa 214,249,22528\nv_accvgpr_read_b32 v160, a160\n"
"v_accvgpr_read_b32 v161, a161\nv_accvgpr_read_b32 v162, a162\nv_accvgpr_read_b32 v163, a163\nv_accvgpr_read_b32 v164, a164\n"
"v_accvgpr_read_b32 v165, a165\nv_accvgpr_read_b32 v166, a166\nv_accvgpr_read_b32 v167, a167\nv_accvgpr_read_b32 v168, a168\n"
"v_accvgpr_read_b32 v169, a169\nv_accvgpr_read_b32 v170, a170\nv_accvgpr_read_b32 v171, a171\nv_accvgpr_read_b32 v172, a172\n"
"v_accvgpr_read_b32 v173, a173\nv_accvgpr_read_b32 v174, a174\nv_accvgpr_read_b32 v175, a175\nv_accvgpr_read_b32 v176, a176\n"
"v_accvgpr_read_b32 v177, a177\nv_accvgpr_read_b32 v178, a178\nv_accvgpr_read_b32 v179, a179\nAD 160,161\nAD 162,163\nAD 164,165\nAD 166,167\n"
"AD 168,169\nAD 170,171\nAD 172,173\nAD 174,175\nAD 176,177\nAD 178,179\nv_add_f32 v180, v160, v170\nv_add_f32 v182, v172, v162\n"
"v_add_f32 v184, v164, v174\nv_add_f32 v186, v176, v166\nv_add_f32 v188, v168, v178\nv_add_f32 v161, v180, v182\nv_add_f32 v162, v184, v186\n"
"AD 161,188\nAD 161,162\nv_rcp_f32 v180, v180\nv_rcp_f32 v182, v182\nv_rcp_f32 v184, v184\nv_rcp_f32 v186, v186\nv_rcp_f32 v188, v188\n"
"v_add_f32 v162, v180, v182\nv_add_f32 v163, v184, v186\nAD 161,162\nAD 163,188\nAD 161,163\nv_cmp_ngt_f32 vcc, s82, v161\ns_nop 1\n"
"s_cmp_lg_u64 vcc, 0\ns_cselect_b32 s79, 1, s79\nPV 64,184\nPV 66,184\nPV 68,184\nPV 70,184\nCV 64,64\nCV 65,66\nCV 66,68\nCV 67,70\n"
"ds_write_b128 v248, v[64:67] offset:49152\nPV 0,180\nPV 2,180\nPV 4,180\nPV 72,184\nPV 74,184\nPV 76,184\nPV 78,184\nCV 72,72\nCV 73,74\nCV 74,76\n"
"CV 75,78\nds_write_b128 v248, v[72:75] offset:50176\nPV 6,180\nPV 8,180\nPV 10,180\nPV 80,184\nPV 82,184\nPV 84,184\nPV 86,184\nCV 80,80\nCV 81,82\n"
"CV 82,84\nCV 83,86\nds_write_b128 v248, v[80:83] offset:51200\nPV 12,180\nPV 14,180\nPV 16,180\nPV 88,184\nPV 90,184\nPV 92,184\nPV 94,184\nCV 88,88\n"
"CV 89,90\nCV 90,92\nCV 91,94\nds_write_b128 v248, v[88:91] offset:52224\nPV 18,180\nPV 20,180\nPV 22,180\nPV 32,182\nPV 34,182\nPV 36,182\nPV 38,182\n"
"CV 32,32\nCV 33,34\nCV 34,36\nCV 35,38\nds_write_b128 v248, v[32:35] offset:53248\nPV 24,180\nPV 26,180\nPV 28,180\nPV 40,182\nPV 42,182\nPV 44,182\n"
"PV 46,182\nCV 40,40\nCV 41,42\nCV 42,44\nCV 43,46\nds_write_b128 v248, v[40:43] offset:54272\nPV 30,180\nPV 96,186\nPV 98,186\nPV 48,182\nPV 50,182\n"
"PV 52,182\nPV 54,182\nCV 48,48\nCV 49,50\nCV 50,52\nCV 51,54\nds_write_b128 v248, v[48:51] offset:55296\nPV 100,186\nPV 102,186\nPV 104,186\n"
"PV 56,182\n.Lfa_pd20229:\ns_cbranch_execz .Lfa_wr10\nPV 58,182\nPV 60,182\nPV 62,182\nCV 56,56\nCV 57,58\nCV 58,60\nCV 59,62\n"
"ds_write_b128 v248, v[56:59] offset:56320\nPV 106,186\nPV 108,186\nPV 110,186\nPV 128,188\nPV 130,188\nPV 132,188\nPV 134,188\nCV 128,128\n"
"CV 129,130\nCV 130,132\nCV 131,134\nds_write_b128 v248, v[128:131] offset:57344\nPV 112,186\nPV 114,186\nPV 116,186\nPV 136,188\nPV 138,188\n"
"PV 140,188\nPV 142,188\nCV 136,136\nCV 137,138\nCV 138,140\nCV 139,142\nds_write_b128 v248, v[136:139] offset:58368\nPV 118,186\nPV 120,186\n"
"PV 122,186\nPV 144,188\nPV 146,188\nPV 148,188\nPV 150,188\nCV 144,144\nCV 145,146\nCV 146,148\nCV 147,150\n"
"ds_write_b128 v248, v[144:147] offset:59392\nPV 124,186\nPV 126,186\nPV 152,188\nPV 154,188\nPV 156,188\nPV 158,188\nCV 152,152\nCV 153,154\n"
"CV 154,156\nCV 155,158\nds_write_b128 v248, v[152:155] offset:60416\ns_waitcnt vmcnt(10) lgkmcnt(0)\ns_barrier\n.Lfa_h:\nZvaa 160,184,80\n"
"s_add_u32 s76, s85, 0x100\nTa 216,246,0\nMvaa 160,188,84\ns_and_b32 s76, s76, 0x3ff\nTa 218,246,2048\nMvaa 160,192,0\ns_add_u32 s76, s76, 0x0\n"
"Ta 220,246,4096\nMvaa 160,196,4\ns_add_u32 m0, s94, 0x10000\nTa 222,246,6144\nMvaa 160,200,88\ns_lshl_b32 s88, s60, 2\nDL 240,52,76,0\n"
"Mvaa 160,204,92\ns_add_u32 s88, s88, 0x18000\nTa 224,251,0\nv_mov_b32 v76, s88\nMvaa 160,208,8\nTa 226,251,2048\nv_mov_b32 v77, s79\nMvaa 160,212,12\n"
"DL 241,52,76,1024\nZvaa 176,184,128\nds_write_b32 v76, v77\nv_mov_b32 v78, 0x18000\nMvaa 176,188,132\nRv 36,204,53248\nMvaa 176,192,32\n"
"DL 242,52,76,2048\nMvaa 176,196,36\nRv 44,204,54272\nMvaa 176,200,136\nRv 52,204,55296\nMvaa 176,204,140\nDL 243,52,76,3072\nMvaa 176,208,40\n"
"s_add_u32 s76, s85, 0x100\nRv 60,204,56320\nCV 0,0\nMvaa 176,212,44\ns_and_b32 s76, s76, 0x3ff\nTa 228,251,4096\nTa 230,251,6144\nZvaa 192,184,96\n"
"s_add_u32 s76, s76, 0x10000\nTa 232,246,16384\nCV 1,2\nMvaa 192,188,100\ns_add_u32 m0, s94, 0x14000\nTa 234,246,18432\nTa 236,246,20480\n"
"Mvaa 192,192,16\nDL 240,52,76,0\nMvaa 192,196,20\nTa 238,246,22528\nCV 2,4\nMvaa 192,200,104\nTa 240,251,16384\nTa 242,251,18432\nMvaa 192,204,108\n"
"Ta 244,251,20480\nTa 246,251,22528\nMvaa 192,208,24\nDL 241,52,76,1024\nMvaa 192,212,28\ns_waitcnt lgkmcnt(7)\nTa 48,245,0\nCV 3,6\nZvaa 168,216,80\n"
"Ta 50,245,2048\nCV 8,8\nMvaa 168,220,84\nDL 242,52,76,2048\nMvaa 168,224,0\nTa 52,245,4096\nTa 54,245,6144\nMvaa 168,228,4\ns_waitcnt lgkmcnt(6)\n"
"Ta 56,250,0\nCV 9,10\nMvaa 168,232,88\nDL 243,52,76,3072\nMvaa 168,236,92\nTa 58,250,2048\nTa 60,250,4096\nMvaa 168,240,8\ns_waitcnt lgkmcnt(6)\n"
"Ta 62,250,6144\nCV 10,12\nMvaa 168,244,12\nTa 64,245,16384\nCV 11,14\nZvaa 184,216,128\nTa 66,245,18432\nCV 16,16\nMvaa 184,220,132\nTa 68,245,20480\n"
"CV 17,18\nMvaa 184,224,32\nTa 70,245,22528\nCV 18,20\nMvaa 184,228,36\nTa 72,250,16384\nCV 19,22\nMvaa 184,232,136\nTa 74,250,18432\nCV 24,24\n"
"Mvaa 184,236,140\nTa 76,250,20480\nTa 78,250,22528\nMvaa 184,240,40\ns_waitcnt vmcnt(10) lgkmcnt(0)\nTa 184,247,0\nCV 25,26\nMvaa 184,244,44\n"
"s_barrier\nZvaa 164,48,80\nTa 186,247,2048\nCV 26,28\nMvaa 164,52,84\nTa 188,247,4096\nCV 27,30\nMvaa 164,56,0\nTa 190,247,6144\nCV 96,96\n"
"Mvaa 164,60,4\nTa 192,252,0\nCV 97,98\nMvaa 164,64,88\nTa 194,252,2048\nCV 98,100\nMvaa 164,68,92\nTa 196,252,4096\nCV 99,102\nMvaa 164,72,8\n"
"Ta 198,252,6144\nCV 104,104\nMvaa 164,76,12\nTa 200,247,16384\nCV 105,106\nZvaa 180,48,128\nTa 202,247,18432\nCV 106,108\nMvaa 180,52,132\n"
"Ta 204,247,20480\nCV 107,110\nMvaa 180,56,32\nTa 206,247,22528\nCV 112,112\nMvaa 180,60,36\nTa 208,252,16384\nCV 113,114\nMvaa 180,64,136\n"
"Ta 210,252,18432\nCV 114,116\nMvaa 180,68,140\nTa 212,252,20480\nCV 115,118\nMvaa 180,72,40\nTa 214,252,22528\nCV 120,120\nMvaa 180,76,44\n"
"Ta 216,244,32768\nCV 121,122\nZvaa 196,48,96\nTa 218,244,34816\nCV 122,124\nMvaa 196,52,100\nTa 220,244,36864\nCV 123,126\nMvaa 196,56,16\n"
"Rv 84,78,0\nMvaa 196,60,20\nRv 4,204,49152\nMvaa 196,64,104\nRv 12,204,50176\nMvaa 196,68,108\nRv 20,204,51200\nMvaa 196,72,24\nRv 28,204,52224\n"
"Mvaa 196,76,28\ns_waitcnt lgkmcnt(6)\nRv 100,204,57344\nZvaa 172,184,80\nRv 108,204,58368\nMvaa 172,188,84\nRv 116,204,59392\nMvaa 172,192,0\n"
"s_waitcnt lgkmcnt(6)\nRv 124,204,60416\nv_or3_b32 v84, v84, v85, v86\nMvaa 172,196,4\nTa 222,244,38912\nTa 224,249,32768\nMvaa 172,200,88\n"
"Ta 226,249,34816\nv_or_b32 v84, v84, v87\nMvaa 172,204,92\nTa 228,249,36864\ns_nop 0\nMvaa 172,208,8\nTa 230,249,38912\nv_readfirstlane_b32 s88, v84\n"
"Mvaa 172,212,12\nTa 232,244,49152\ns_nop 1\nZvaa 188,184,128\ns_cmp_lg_u32 s88, 0\ns_cbranch_scc1 .Lfa_exit\nMvaa 188,188,132\nTa 234,244,51200\n"
"Mvaa 188,192,32\nTa 236,244,53248\nMvaa 188,196,36\nTa 238,244,55296\nMvaa 188,200,136\nTa 240,249,49152\nTa 242,249,51200\nMvaa 188,204,140\n"
"Ta 244,249,53248\nTa 246,249,55296\nMvaa 188,208,40\ns_waitcnt vmcnt(10) lgkmcnt(0)\nTa 48,246,32768\nv_add_u32 v244, 0x10000, v244\nMvaa 188,212,44\n"
"s_barrier\n.Lfa_h2:\nMvav 160,216,0\ns_add_u32 s76, s85, 0x100\nTa 50,246,34816\nv_add_u32 v249, 0x10000, v249\nMvav 160,220,8\n"
"s_and_b32 s76, s76, 0x3ff\nTa 52,246,36864\nv_mbcnt_lo_u32_b32 v128, -1, 0\nMvav 160,224,4\ns_add_u32 s76, s76, 0x40000\nTa 54,246,38912\n"
"v_mbcnt_hi_u32_b32 v128, -1, v128\nMvav 160,228,12\ns_add_u32 m0, s94, 0x18000\nTa 56,251,32768\nv_and_b32 v129, 15, v128\nMvav 160,232,16\n"
"DL 240,52,76,0\nMvav 160,236,24\nTa 58,251,34816\nv_lshrrev_b32 v130, 4, v128\nMvav 160,240,20\nTa 60,251,36864\nv_bfe_u32 v131, v128, 2, 2\n"
"Mvav 160,244,28\nDL 241,52,76,1024\nMvav 176,216,96\nTa 62,251,38912\nv_and_b32 v132, 3, v128\nMvav 176,220,104\ns_mul_i32 s88, s60, 5120\n"
"Ta 64,246,49152\nv_lshlrev_b32 v133, 4, v128\nMvav 176,224,100\nDL 242,52,76,2048\nMvav 176,228,108\nTa 66,246,51200\nv_add_u32 v68, s88, v133\n"
"Mvav 176,232,112\nTa 68,246,53248\nv_add_u32 v133, s60, v130\nMvav 176,236,120\nDL 243,52,76,3072\nMvav 176,240,116\ns_add_u32 s76, s85, 0x100\n"
"Ta 70,246,55296\nv_lshlrev_b32 v133, 24, v133\nMvav 176,244,124\ns_and_b32 s76, s76, 0x3ff\nTa 72,251,49152\nv_lshl_add_u32 v69, v129, 3, v133\n"
"Mvav 192,216,32\ns_add_u32 s76, s76, 0x50000\nTa 74,251,51200\nv_add_u32 v246, 0x10000, v246\nMvav 192,220,40\ns_add_u32 m0, s94, 0x1c000\n"
"Ta 76,251,53248\nv_and_b32 v133, 1, v128\nMvav 192,224,36\nDL 240,52,76,0\nMvav 192,228,44\nTa 78,251,55296\nv_lshlrev_b32 v70, 8, v133\n"
"Mvav 192,232,48\ns_lshl_b32 s72, s61, 11\nTa 184,245,32768\nMvav 192,236,56\ns_add_u32 s72, s72, 0x118000\nDL 241,52,76,1024\nMvav 192,240,52\n"
"Ta 186,245,34816\nv_add_u32 v251, 0x10000, v251\nMvav 192,244,60\ns_waitcnt lgkmcnt(4)\nTa 188,245,36864\nv_accvgpr_write_b32 a248, s86\n"
"Mvav 168,48,0\nDL 242,52,76,2048\nMvav 168,52,8\nTa 190,245,38912\nv_accvgpr_write_b32 a249, s87\nMvav 168,56,4\nTa 192,250,32768\n"
"v_and_b32 v133, 1, v132\nMvav 168,60,12\nDL 243,52,76,3072\nMvav 168,64,16\nTa 194,250,34816\nv_lshlrev_b32 v133, 4, v133\nMvav 168,68,24\n"
"Ta 196,250,36864\nv_mov_b32 v134, 0x3f80\nMvav 168,72,20\ns_waitcnt lgkmcnt(4)\nTa 198,250,38912\nv_lshlrev_b32 v133, v133, v134\nMvav 168,76,28\n"
"Ta 200,245,49152\nv_cmp_eq_u32 vcc, v130, v131\nMvav 184,48,96\nTa 202,245,51200\ns_nop 1\nMvav 184,52,104\nTa 204,245,53248\n"
"v_cndmask_b32 v133, 0, v133, vcc\nMvav 184,56,100\nTa 206,245,55296\nv_cmp_gt_u32 vcc, 2, v132\nMvav 184,60,108\nTa 208,250,49152\ns_nop 1\n"
"Mvav 184,64,112\nTa 210,250,51200\nv_add_u32 v245, 0x10000, v245\nMvav 184,68,120\nTa 212,250,53248\nTa 214,250,55296\nMvav 184,72,116\n"
"s_waitcnt vmcnt(8)\nTa 216,247,32768\nv_cndmask_b32 v134, 0, v133, vcc\nMvav 184,76,124\ns_barrier\nMvav 164,184,0\ns_add_u32 s76, s85, 0x180\n"
"Ta 218,247,34816\nv_add_u32 v250, 0x10000, v250\ns_waitcnt lgkmcnt(7)\nMvav 164,188,8\ns_and_b32 s76, s76, 0x3ff\nTa 220,247,36864\n"
"v_sub_u32 v135, v133, v134\nMvav 164,192,4\ns_add_u32 s76, s76, 0x0\nTa 222,247,38912\nv_and_b32 v64, 1, v132\nMvav 164,196,12\n"
"s_add_u32 m0, s94, 0x20000\nTa 224,252,32768\nv_lshlrev_b32 v64, 4, v64\nMvav 164,200,16\ns_waitcnt lgkmcnt(5)\nDL 240,52,76,0\nMvav 164,204,24\n"
"Ta 226,252,34816\nv_mov_b32 v65, 0x3f80\nMvav 164,208,20\nTa 228,252,36864\nv_lshlrev_b32 v64, v64, v65\nMvav 164,212,28\nDL 241,52,76,1024\n"
"Mvav 180,184,96\nTa 230,252,38912\nv_lshrrev_b32 v65, 1, v128\nMvav 180,188,104\nTa 232,247,49152\nv_xor_b32 v65, v65, v128\nMvav 180,192,100\n"
"DL 242,52,76,2048\nMvav 180,196,108\nTa 234,247,51200\nv_bfe_u32 v65, v65, 3, 1\nMvav 180,200,112\nTa 236,247,53248\nv_subrev_u32 v65, 1, v65\n"
"Mvav 180,204,120\nDL 243,52,76,3072\nMvav 180,208,116\ns_add_u32 s76, s85, 0x180\nTa 238,247,55296\nv_and_b32 v66, v64, v65\nMvav 180,212,124\n"
"s_and_b32 s76, s76, 0x3ff\nTa 240,252,49152\nv_bfe_u32 v65, v128, 2, 1\nMvav 196,184,32\ns_add_u32 s76, s76, 0x10000\nTa 242,252,51200\n"
"v_add_u32 v247, 0x10000, v247\nMvav 196,188,40\ns_add_u32 m0, s94, 0x24000\nTa 244,252,53248\nv_subrev_u32 v65, 1, v65\nMvav 196,192,36\n"
"DL 240,52,76,0\nMvav 196,196,44\nTa 246,252,55296\nv_and_b32 v64, v66, v65\nMvav 196,200,48\nTa 48,244,0\nv_sub_u32 v66, v66, v64\nMvav 196,204,56\n"
"DL 241,52,76,1024\nMvav 196,208,52\nTa 50,244,2048\nv_add_u32 v252, 0x10000, v252\nMvav 196,212,60\ns_waitcnt lgkmcnt(4)\nTa 52,244,4096\n"
"v_mov_b32 v65, 0\nMvav 172,216,0\nDL 242,52,76,2048\nMvav 172,220,8\nTa 54,244,6144\nv_mov_b32 v67, 0\nMvav 172,224,4\nTa 56,249,0\nv_mov_b32 v72, 0\n"
"Mvav 172,228,12\nDL 243,52,76,3072\nMvav 172,232,16\nTa 58,249,2048\nv_mov_b32 v74, 0\nMvav 172,236,24\nTa 60,249,4096\nv_mov_b32 v73, v64\n"
"Mvav 172,240,20\ns_waitcnt lgkmcnt(4)\nTa 62,249,6144\nv_mov_b32 v75, v66\nMvav 172,244,28\nTa 64,244,16384\nv_bfe_u32 v79, v128, 1, 1\n"
"Mvav 188,216,96\nTa 66,244,18432\nv_mul_u32_u24 v79, 0xaaaa, v79\nMvav 188,220,104\nTa 68,244,20480\nv_add_u32 v79, 0x4444, v79\nMvav 188,224,100\n"
"Ta 70,244,22528\nAW 180,134\nMvav 188,228,108\nTa 72,249,16384\nAW 181,135\nMvav 188,232,112\nTa 74,249,18432\nv_accvgpr_write_b32 a182, 0\n"
"Mvav 188,236,120\nTa 76,249,20480\nTa 78,249,22528\nMvav 188,240,116\nTa 184,246,0\nv_accvgpr_write_b32 a183, 0\nMvav 188,244,124\nTa 186,246,2048\n"
"v_accvgpr_write_b32 a252, 0\nv_accvgpr_write_b32 a253, 0\nAW 254,134\nAW 255,135\ns_mov_b32 s95, 0x400000\ns_waitcnt vmcnt(16) lgkmcnt(0)\ns_barrier\n"
"Zvaa 200,48,80\ns_add_u32 s76, s85, 0x180\nTa 188,246,4096\nMvaa 200,52,84\ns_and_b32 s76, s76, 0x3ff\nTa 190,246,6144\n"
".Lfa_pd20778:\ns_cbranch_execz .Lfa_pd22896\nMvaa 200,56,0\ns_add_u32 s76, s76, 0x40000\nTa 192,251,0\nMvaa 200,60,4\ns_add_u32 m0, s94, 0x0\n"
"Ta 194,251,2048\nCV 160,160\nMvaa 200,64,88\ns_add_u32 s78, s61, 1\nDL 240,52,76,0\nMvaa 200,68,92\ns_and_b32 s78, s78, 7\nTa 196,251,4096\n"
"CV 161,162\nMvaa 200,72,8\ns_mul_i32 s78, s78, 143360\nTa 198,251,6144\nCV 162,164\nMvaa 200,76,12\ns_add_u32 s78, s78, 0\nDL 241,52,76,1024\n"
"Zvaa 216,48,128\ns_add_u32 s77, s78, 0x1000\nTa 200,246,16384\nCV 163,166\nMvaa 216,52,132\nTa 202,246,18432\nCV 168,168\nMvaa 216,56,32\n"
"buffer_store_dwordx4 v[160:163], v68, s[56:59], s78 offen offset:0 sc1\nMvaa 216,60,36\nTa 204,246,20480\nCV 169,170\nMvaa 216,64,136\n"
"Ta 206,246,22528\nCV 170,172\nMvaa 216,68,140\nDL 242,52,76,2048\nMvaa 216,72,40\nTa 208,251,16384\nCV 171,174\nMvaa 216,76,44\nTa 210,251,18432\n"
"CV 176,176\nZvaa 232,48,96\nDL 243,52,76,3072\nMvaa 232,52,100\nTa 212,251,20480\nCV 177,178\nMvaa 232,56,16\nTa 214,251,22528\nCV 178,180\n"
"Mvaa 232,60,20\nTa 216,245,0\nCV 179,182\nMvaa 232,64,104\nTa 218,245,2048\nCV 184,184\nMvaa 232,68,108\nTa 220,245,4096\nCV 185,186\nMvaa 232,72,24\n"
"Ta 222,245,6144\nCV 186,188\nMvaa 232,76,28\nTa 224,250,0\nCV 187,190\nZvaa 208,184,80\ns_waitcnt lgkmcnt(6)\n"
"buffer_store_dwordx4 v[168:171], v68, s[56:59], s78 offen offset:1024 sc1\nMvaa 208,188,84\nTa 226,250,2048\nCV 192,192\nMvaa 208,192,0\n"
"Ta 228,250,4096\nCV 193,194\nMvaa 208,196,4\nTa 230,250,6144\nCV 194,196\nMvaa 208,200,88\nTa 232,245,16384\nCV 195,198\nMvaa 208,204,92\n"
"Ta 234,245,18432\nMvaa 208,208,8\ns_waitcnt lgkmcnt(5)\nTa 236,245,20480\nMvaa 208,212,12\nTa 238,245,22528\nZvaa 224,184,128\nTa 240,250,16384\n"
"Mvaa 224,188,132\nTa 242,250,18432\nMvaa 224,192,32\nTa 244,250,20480\nMvaa 224,196,36\nTa 246,250,22528\nMvaa 224,200,136\nTa 48,247,0\n"
"Mvaa 224,204,140\nTa 50,247,2048\nMvaa 224,208,40\ns_waitcnt vmcnt(14)\nTa 52,247,4096\nMvaa 224,212,44\ns_barrier\nZvaa 204,216,80\n"
"s_add_u32 s76, s85, 0x180\nTa 54,247,6144\nMvaa 204,220,84\ns_waitcnt lgkmcnt(5)\ns_and_b32 s76, s76, 0x3ff\n"
"buffer_store_dwordx4 v[176:179], v68, s[56:59], s78 offen offset:2048 sc1\nMvaa 204,224,0\ns_add_u32 s76, s76, 0x50000\nTa 56,252,0\nMvaa 204,228,4\n"
"s_add_u32 m0, s94, 0x4000\nTa 58,252,2048\nMvaa 204,232,88\nDL 240,52,76,0\nMvaa 204,236,92\nTa 60,252,4096\nMvaa 204,240,8\ns_waitcnt lgkmcnt(4)\n"
"Ta 62,252,6144\nMvaa 204,244,12\nDL 241,52,76,1024\nZvaa 220,216,128\nTa 64,247,16384\nMvaa 220,220,132\nTa 66,247,18432\nMvaa 220,224,32\n"
"DL 242,52,76,2048\nMvaa 220,228,36\nTa 68,247,20480\nMvaa 220,232,136\nTa 70,247,22528\nMvaa 220,236,140\nDL 243,52,76,3072\nMvaa 220,240,40\n"
"s_add_u32 s76, s85, 0x200\nTa 72,252,16384\nMvaa 220,244,44\ns_and_b32 s76, s76, 0x3ff\nTa 74,252,18432\nZvaa 236,216,96\ns_add_u32 s76, s76, 0x0\n"
"buffer_store_dwordx4 v[184:187], v68, s[56:59], s78 offen offset:3072 sc1\nMvaa 236,220,100\nTa 76,252,20480\nMvaa 236,224,16\n"
"s_add_u32 m0, s94, 0x8000\nTa 78,252,22528\nMvaa 236,228,20\nDL 240,52,76,0\nMvaa 236,232,104\nTa 184,244,32768\nMvaa 236,236,108\nTa 186,244,34816\n"
"Mvaa 236,240,24\nDL 241,52,76,1024\nMvaa 236,244,28\nTa 188,244,36864\nZvaa 212,48,80\ns_waitcnt lgkmcnt(4)\nTa 190,244,38912\nMvaa 212,52,84\n"
"DL 242,52,76,2048\nMvaa 212,56,0\nTa 192,249,32768\nMvaa 212,60,4\nTa 194,249,34816\nMvaa 212,64,88\nDL 243,52,76,3072\nMvaa 212,68,92\n"
"Ta 196,249,36864\nMvaa 212,72,8\ns_waitcnt lgkmcnt(4)\nTa 198,249,38912\nMvaa 212,76,12\n"
"buffer_store_dwordx4 v[192:195], v68, s[56:59], s77 offen offset:0 sc1\nZvaa 228,48,128\nTa 200,244,49152\nMvaa 228,52,132\nTa 202,244,51200\n"
"Mvaa 228,56,32\nTa 204,244,53248\nMvaa 228,60,36\nTa 206,244,55296\nMvaa 228,64,136\nTa 208,249,49152\nTa 210,249,51200\nMvaa 228,68,140\n"
"Ta 212,249,53248\nTa 214,249,55296\nMvaa 228,72,40\ns_waitcnt vmcnt(17)\nTa 216,246,32768\nv_add_u32 v244, 0x10000, v244\nMvaa 228,76,44\ns_barrier\n"
"Mvav 200,184,0\ns_add_u32 s76, s85, 0x200\nTa 218,246,34816\nv_add_u32 v249, 0x10000, v249\ns_waitcnt lgkmcnt(8)\nMvav 200,188,8\n"
"s_and_b32 s76, s76, 0x3ff\nTa 220,246,36864\nMvav 200,192,4\ns_add_u32 s76, s76, 0x10000\nTa 222,246,38912\nMvav 200,196,12\n"
"s_add_u32 m0, s94, 0xc000\nTa 224,251,32768\nMvav 200,200,16\ns_waitcnt lgkmcnt(5)\nDL 240,52,76,0\nMvav 200,204,24\nTa 226,251,34816\n"
"Mvav 200,208,20\nTa 228,251,36864\nMvav 200,212,28\nDL 241,52,76,1024\nMvav 216,184,96\nTa 230,251,38912\nMvav 216,188,104\nTa 232,246,49152\n"
"Mvav 216,192,100\nDL 242,52,76,2048\nMvav 216,196,108\nTa 234,246,51200\nMvav 216,200,112\nTa 236,246,53248\nMvav 216,204,120\nDL 243,52,76,3072\n"
"Mvav 216,208,116\ns_add_u32 s76, s85, 0x200\nTa 238,246,55296\nMvav 216,212,124\ns_and_b32 s76, s76, 0x3ff\nTa 240,251,49152\nMvav 232,184,32\n"
"s_add_u32 s76, s76, 0x40000\nTa 242,251,51200\nv_add_u32 v246, 0x10000, v246\nMvav 232,188,40\ns_add_u32 m0, s94, 0x10000\nTa 244,251,53248\n"
"Mvav 232,192,36\nDL 240,52,76,0\nMvav 232,196,44\nTa 246,251,55296\nMvav 232,200,48\nTa 48,245,32768\nMvav 232,204,56\nDL 241,52,76,1024\n"
"Mvav 232,208,52\nTa 50,245,34816\nv_add_u32 v251, 0x10000, v251\nMvav 232,212,60\ns_waitcnt lgkmcnt(4)\nTa 52,245,36864\nMvav 208,216,0\n"
"DL 242,52,76,2048\nMvav 208,220,8\nTa 54,245,38912\nMvav 208,224,4\nTa 56,250,32768\nMvav 208,228,12\nDL 243,52,76,3072\nMvav 208,232,16\n"
"Ta 58,250,34816\nMvav 208,236,24\nTa 60,250,36864\nMvav 208,240,20\ns_waitcnt lgkmcnt(4)\nTa 62,250,38912\nMvav 208,244,28\nTa 64,245,49152\n"
"Mvav 224,216,96\nTa 66,245,51200\nMvav 224,220,104\nTa 68,245,53248\nMvav 224,224,100\nTa 70,245,55296\nMvav 224,228,108\nTa 72,250,49152\n"
"Mvav 224,232,112\nTa 74,250,51200\nv_add_u32 v245, 0x10000, v245\nMvav 224,236,120\nTa 76,250,53248\nTa 78,250,55296\nMvav 224,240,116\n"
"Ta 184,247,32768\nMvav 224,244,124\nTa 186,247,34816\nv_add_u32 v250, 0x10000, v250\nMvav 204,48,0\ns_waitcnt lgkmcnt(7)\ns_add_u32 s76, s85, 0x200\n"
"Ta 188,247,36864\nMvav 204,52,8\ns_and_b32 s76, s76, 0x3ff\nTa 190,247,38912\nMvav 204,56,4\ns_add_u32 s76, s76, 0x50000\nTa 192,252,32768\n"
"Mvav 204,60,12\ns_add_u32 m0, s94, 0x14000\nTa 194,252,34816\nMvav 204,64,16\ns_waitcnt lgkmcnt(6)\ns_add_u32 s78, s61, 2\nDL 240,52,76,0\n"
"Mvav 204,68,24\ns_and_b32 s78, s78, 7\nTa 196,252,36864\nMvav 204,72,20\ns_mul_i32 s78, s78, 143360\nTa 198,252,38912\nMvav 204,76,28\n"
"s_add_u32 s78, s78, 20480\nDL 241,52,76,1024\nMvav 220,48,96\ns_add_u32 s77, s78, 0x1000\nTa 200,247,49152\nMvav 220,52,104\nTa 202,247,51200\n"
"Mvav 220,56,100\nDL 242,52,76,2048\nMvav 220,60,108\nTa 204,247,53248\nMvav 220,64,112\nTa 206,247,55296\nMvav 220,68,120\nDL 243,52,76,3072\n"
"Mvav 220,72,116\nTa 208,252,49152\nv_add_u32 v247, 0x10000, v247\nMvav 220,76,124\nTa 210,252,51200\nMvav 236,48,32\nTa 212,252,53248\n"
"Mvav 236,52,40\nTa 214,252,55296\nMvav 236,56,36\nTa 216,244,0\nMvav 236,60,44\nTa 218,244,2048\nv_add_u32 v252, 0x10000, v252\nMvav 236,64,48\n"
"Ta 220,244,4096\nMvav 236,68,56\nTa 222,244,6144\nMvav 236,72,52\nTa 224,249,0\nMvav 236,76,60\ns_waitcnt lgkmcnt(6)\nTa 226,249,2048\n"
"Mvav 212,184,0\nTa 228,249,4096\nMvav 212,188,8\nTa 230,249,6144\nMvav 212,192,4\nTa 232,244,16384\nMvav 212,196,12\nTa 234,244,18432\nCV 200,200\n"
"Mvav 212,200,16\nTa 236,244,20480\nCV 201,202\nMvav 212,204,24\nTa 238,244,22528\nCV 202,204\nMvav 212,208,20\ns_waitcnt lgkmcnt(6)\n"
"Ta 240,249,16384\nCV 203,206\nMvav 212,212,28\nbuffer_store_dwordx4 v[200:203], v68, s[56:59], s78 offen offset:0 sc1\nMvav 228,184,96\n"
"Ta 242,249,18432\nv_add_u32 v244, 0xfffe0000, v244\nMvav 228,188,104\nTa 244,249,20480\nCV 216,216\nMvav 228,192,100\nTa 246,249,22528\nCV 217,218\n"
"Mvav 228,196,108\nTa 48,246,0\nCV 218,220\nMvav 228,200,112\nTa 50,246,2048\nv_add_u32 v249, 0xfffe0000, v249\nMvav 228,204,120\nTa 52,246,4096\n"
"CV 219,222\nMvav 228,208,116\ns_waitcnt vmcnt(13)\nTa 54,246,6144\nCV 232,232\nMvav 228,212,124\ns_barrier\nZvaa 160,216,80\n"
"s_add_u32 s76, s85, 0x280\nTa 56,251,0\nMvaa 160,220,84\ns_and_b32 s76, s76, 0x3ff\nTa 58,251,2048\nMvaa 160,224,0\ns_waitcnt lgkmcnt(5)\n"
"s_add_u32 s76, s76, 0x0\nTa 60,251,4096\nMvaa 160,228,4\ns_add_u32 m0, s94, 0x18000\nTa 62,251,6144\nMvaa 160,232,88\ns_add_u32 s88, s61, 1\n"
"DL 240,52,76,0\nMvaa 160,236,92\ns_and_b32 s88, s88, 7\nTa 64,246,16384\nMvaa 160,240,8\ns_lshl_b32 s88, s88, 11\nTa 66,246,18432\nMvaa 160,244,12\n"
"s_add_u32 s88, s88, 0x118000\nbuffer_store_dwordx2 a[248:249], v69, s[56:59], s88 offen sc1\nZvaa 176,216,128\nTa 68,246,20480\nCV 233,234\n"
"Mvaa 176,220,132\nTa 70,246,22528\nCV 234,236\nMvaa 176,224,32\nbuffer_store_dwordx4 v[216:219], v68, s[56:59], s78 offen offset:2048 sc1\n"
"Mvaa 176,228,36\nTa 72,251,16384\nv_add_u32 v246, 0xfffe0000, v246\nMvaa 176,232,136\nTa 74,251,18432\nCV 235,238\nMvaa 176,236,140\n"
"DL 241,52,76,1024\nMvaa 176,240,40\nTa 76,251,20480\nCV 208,208\nMvaa 176,244,44\nTa 78,251,22528\nCV 209,210\nZvaa 192,216,96\nDL 242,52,76,2048\n"
"Mvaa 192,220,100\nTa 184,245,0\nv_add_u32 v251, 0xfffe0000, v251\nMvaa 192,224,16\nTa 186,245,2048\nCV 210,212\nMvaa 192,228,20\nDL 243,52,76,3072\n"
"Mvaa 192,232,104\nTa 188,245,4096\nCV 211,214\nMvaa 192,236,108\nTa 190,245,6144\nCV 224,224\nMvaa 192,240,24\nTa 192,250,0\nCV 225,226\n"
"Mvaa 192,244,28\ns_waitcnt lgkmcnt(5)\nTa 194,250,2048\nCV 226,228\nZvaa 168,48,80\n"
"buffer_store_dwordx4 v[232:235], v68, s[56:59], s77 offen offset:0 sc1\nMvaa 168,52,84\nTa 196,250,4096\nCV 227,230\nMvaa 168,56,0\nTa 198,250,6144\n"
"Mvaa 168,60,4\nTa 200,245,16384\nMvaa 168,64,88\nTa 202,245,18432\nMvaa 168,68,92\nTa 204,245,20480\nMvaa 168,72,8\nTa 206,245,22528\nMvaa 168,76,12\n"
"Ta 208,250,16384\nZvaa 184,48,128\nTa 210,250,18432\nv_add_u32 v245, 0xfffe0000, v245\nMvaa 184,52,132\nTa 212,250,20480\nMvaa 184,56,32\n"
"Ta 214,250,22528\nMvaa 184,60,36\nTa 216,247,0\nMvaa 184,64,136\nTa 218,247,2048\nv_add_u32 v250, 0xfffe0000, v250\nMvaa 184,68,140\nTa 220,247,4096\n"
"Mvaa 184,72,40\nbuffer_store_dwordx4 v[208:211], v68, s[56:59], s78 offen offset:1024 sc1\nMvaa 184,76,44\ns_waitcnt lgkmcnt(5)\nTa 222,247,6144\n"
"Zvaa 164,184,80\ns_add_u32 s76, s85, 0x280\nTa 224,252,0\nMvaa 164,188,84\ns_and_b32 s76, s76, 0x3ff\nTa 226,252,2048\nMvaa 164,192,0\n"
"s_add_u32 s76, s76, 0x10000\nTa 228,252,4096\nMvaa 164,196,4\ns_add_u32 m0, s94, 0x1c000\nTa 230,252,6144\nMvaa 164,200,88\nDL 240,52,76,0\n"
"Mvaa 164,204,92\n.Lfa_pd21307:\ns_cbranch_execz .Lfa_pd23441\nTa 232,247,16384\nMvaa 164,208,8\ns_waitcnt lgkmcnt(5)\nTa 234,247,18432\n"
"Mvaa 164,212,12\nDL 241,52,76,1024\nZvaa 180,184,128\nTa 236,247,20480\nMvaa 180,188,132\nTa 238,247,22528\nMvaa 180,192,32\nDL 242,52,76,2048\n"
"Mvaa 180,196,36\nTa 240,252,16384\nv_add_u32 v247, 0xfffe0000, v247\nMvaa 180,200,136\nTa 242,252,18432\nMvaa 180,204,140\n"
"buffer_store_dwordx4 v[224:227], v68, s[56:59], s78 offen offset:3072 sc1\nMvaa 180,208,40\nTa 244,252,20480\nMvaa 180,212,44\nTa 246,252,22528\n"
"Zvaa 196,184,96\nDL 243,52,76,3072\nMvaa 196,188,100\nTa 48,244,0\nv_add_u32 v252, 0xfffe0000, v252\nMvaa 196,192,16\nTa 50,244,2048\n"
"Mvaa 196,196,20\nTa 52,244,4096\nMvaa 196,200,104\nTa 54,244,6144\nMvaa 196,204,108\nTa 56,249,0\nMvaa 196,208,24\nTa 58,249,2048\nMvaa 196,212,28\n"
"Ta 60,249,4096\nZvaa 172,216,80\nTa 62,249,6144\nMvaa 172,220,84\ns_waitcnt lgkmcnt(6)\nTa 64,244,16384\nMvaa 172,224,0\nTa 66,244,18432\n"
"Mvaa 172,228,4\nTa 68,244,20480\nMvaa 172,232,88\nTa 70,244,22528\nMvaa 172,236,92\nTa 72,249,16384\nMvaa 172,240,8\nTa 74,249,18432\n"
"Mvaa 172,244,12\nTa 76,249,20480\nZvaa 188,216,128\nTa 78,249,22528\nMvaa 188,220,132\nTa 184,246,0\nMvaa 188,224,32\nTa 186,246,2048\n"
"Mvaa 188,228,36\nTa 188,246,4096\nMvaa 188,232,136\nTa 190,246,6144\nMvaa 188,236,140\nTa 192,251,0\nMvaa 188,240,40\ns_waitcnt vmcnt(22)\n"
"Ta 194,251,2048\nMvaa 188,244,44\ns_barrier\nMvav 160,48,0\ns_waitcnt lgkmcnt(5)\ns_add_u32 s76, s85, 0x280\nTa 196,251,4096\nMvav 160,52,8\n"
"s_and_b32 s76, s76, 0x3ff\nTa 198,251,6144\nMvav 160,56,4\ns_add_u32 s76, s76, 0x40000\nTa 200,246,16384\nMvav 160,60,12\ns_add_u32 m0, s94, 0x20000\n"
"Ta 202,246,18432\nMvav 160,64,16\nDL 240,52,76,0\nMvav 160,68,24\nTa 204,246,20480\nMvav 160,72,20\nTa 206,246,22528\nMvav 160,76,28\n"
"DL 241,52,76,1024\nMvav 176,48,96\nTa 208,251,16384\nMvav 176,52,104\nTa 210,251,18432\nMvav 176,56,100\nDL 242,52,76,2048\nMvav 176,60,108\n"
"Ta 212,251,20480\nMvav 176,64,112\nTa 214,251,22528\nMvav 176,68,120\nDL 243,52,76,3072\nMvav 176,72,116\nTa 216,245,0\nMvav 176,76,124\n"
"Ta 218,245,2048\nMvav 192,48,32\nTa 220,245,4096\nMvav 192,52,40\nTa 222,245,6144\nMvav 192,56,36\nTa 224,250,0\nMvav 192,60,44\nTa 226,250,2048\n"
"Mvav 192,64,48\nTa 228,250,4096\nMvav 192,68,56\nTa 230,250,6144\nMvav 192,72,52\nTa 232,245,16384\nMvav 192,76,60\ns_waitcnt lgkmcnt(6)\n"
"Ta 234,245,18432\nMvav 168,184,0\nTa 236,245,20480\nMvav 168,188,8\nTa 238,245,22528\nMvav 168,192,4\nTa 240,250,16384\nMvav 168,196,12\n"
"Ta 242,250,18432\nMvav 168,200,16\nTa 244,250,20480\nMvav 168,204,24\nTa 246,250,22528\nMvav 168,208,20\nTa 48,247,0\nMvav 168,212,28\n"
"Ta 50,247,2048\nMvav 184,184,96\nTa 52,247,4096\nMvav 184,188,104\nTa 54,247,6144\nMvav 184,192,100\nTa 56,252,0\nMvav 184,196,108\nTa 58,252,2048\n"
"Mvav 184,200,112\nTa 60,252,4096\nMvav 184,204,120\nTa 62,252,6144\nMvav 184,208,116\nTa 64,247,16384\nMvav 184,212,124\nTa 66,247,18432\n"
"Mvav 164,216,0\ns_waitcnt lgkmcnt(6)\ns_add_u32 s76, s85, 0x280\nTa 184,247,20480\nMvav 164,220,8\ns_and_b32 s76, s76, 0x3ff\n"
"buffer_load_dwordx2 v[76:77], off, s[56:59], s72 offset:0 sc1\nMvav 164,224,4\ns_add_u32 s76, s76, 0x50000\nTa 186,247,22528\nMvav 164,228,12\n"
"s_add_u32 m0, s94, 0x24000\nTa 188,252,16384\nMvav 164,232,16\ns_add_u32 s78, s61, 3\nDL 240,52,76,0\nMvav 164,236,24\ns_and_b32 s78, s78, 7\n"
"Ta 190,252,18432\nMvav 164,240,20\ns_mul_i32 s78, s78, 143360\nTa 192,252,20480\nMvav 164,244,28\ns_add_u32 s78, s78, 40960\nDL 241,52,76,1024\n"
"Mvav 180,216,96\ns_add_u32 s77, s78, 0x1000\nTa 194,252,22528\nMvav 180,220,104\nTa 196,244,32768\nMvav 180,224,100\nDL 242,52,76,2048\n"
"Mvav 180,228,108\nTa 198,244,34816\nMvav 180,232,112\nTa 200,244,36864\nMvav 180,236,120\nDL 243,52,76,3072\nMvav 180,240,116\nTa 202,244,38912\n"
"Mvav 180,244,124\nTa 204,249,32768\nMvav 196,216,32\nTa 206,249,34816\nMvav 196,220,40\nTa 208,249,36864\nMvav 196,224,36\nTa 210,249,38912\n"
"Mvav 196,228,44\nTa 212,244,49152\nMvav 196,232,48\nTa 214,244,51200\nMvav 196,236,56\nTa 216,244,53248\nMvav 196,240,52\nTa 218,244,55296\n"
"Mvav 196,244,60\nTa 220,249,49152\nMvav 172,48,0\nTa 222,249,51200\nv_add_u32 v244, 0x10000, v244\nMvav 172,52,8\ns_waitcnt lgkmcnt(6)\n"
"Ta 224,249,53248\nMvav 172,56,4\nTa 226,249,55296\nMvav 172,60,12\nTa 228,246,32768\nCV 160,160\nMvav 172,64,16\nTa 230,246,34816\n"
"v_add_u32 v249, 0x10000, v249\nMvav 172,184,24\nTa 232,246,36864\nCV 161,162\nMvav 172,188,20\nTa 234,246,38912\nCV 162,164\nMvav 172,192,28\n"
"Ta 236,251,32768\nCV 163,166\nMvav 188,48,96\nbuffer_store_dwordx4 v[160:163], v68, s[56:59], s78 offen offset:0 sc1\nMvav 188,52,104\n"
"Ta 238,251,34816\nCV 176,176\nMvav 188,56,100\nTa 240,251,36864\nCV 177,178\nMvav 188,60,108\nTa 242,251,38912\nCV 178,180\nMvav 188,64,112\n"
"Ta 244,246,49152\nCV 179,182\nMvav 188,184,120\nTa 246,246,51200\nCV 192,192\nMvav 188,188,116\ns_waitcnt vmcnt(11)\nTa 184,246,53248\nCV 193,194\n"
"Mvav 188,192,124\ns_barrier\nZvaa 200,196,80\ns_add_u32 s76, s85, 0x300\nTa 186,246,55296\nMvaa 200,200,84\ns_and_b32 s76, s76, 0x3ff\n"
"Ta 188,251,49152\nMvaa 200,204,0\ns_add_u32 s76, s76, 0x0\nTa 190,251,51200\nv_add_u32 v246, 0x10000, v246\nMvaa 200,208,4\ns_waitcnt lgkmcnt(5)\n"
"s_add_u32 m0, s94, 0x0\nTa 192,251,53248\nMvaa 200,212,88\ns_add_u32 s88, s61, 2\nDL 240,52,76,0\nMvaa 200,216,92\ns_and_b32 s88, s88, 7\n"
"Ta 194,251,55296\nMvaa 200,220,8\ns_lshl_b32 s88, s88, 11\nMvaa 200,224,12\ns_add_u32 s88, s88, 0x118100\n"
"buffer_store_dwordx2 a[248:249], v69, s[56:59], s88 offen sc1\nZvaa 216,196,128\nv_add_u32 v251, 0x10000, v251\nCV 194,196\nMvaa 216,200,132\n"
"CV 195,198\nMvaa 216,204,32\nbuffer_store_dwordx4 v[176:179], v68, s[56:59], s78 offen offset:2048 sc1\nMvaa 216,208,36\nCV 168,168\nCV 169,170\n"
"Mvaa 216,212,136\nCV 170,172\nCV 171,174\nMvaa 216,216,140\nDL 241,52,76,1024\nMvaa 216,220,40\nCV 184,184\nCV 185,186\nMvaa 216,224,44\nCV 186,188\n"
"CV 187,190\nZvaa 232,196,96\nDL 242,52,76,2048\nMvaa 232,200,100\nTa 196,245,32768\nMvaa 232,204,16\nTa 198,245,34816\nMvaa 232,208,20\n"
"DL 243,52,76,3072\nMvaa 232,212,104\nTa 200,245,36864\nMvaa 232,216,108\nTa 202,245,38912\nMvaa 232,220,24\nTa 204,250,32768\nMvaa 232,224,28\n"
"Ta 206,250,34816\nZvaa 208,228,80\nbuffer_store_dwordx4 v[192:195], v68, s[56:59], s77 offen offset:0 sc1\nMvaa 208,232,84\nTa 208,250,36864\n"
"Mvaa 208,236,0\nTa 210,250,38912\nMvaa 208,240,4\ns_waitcnt lgkmcnt(5)\nTa 212,245,49152\nMvaa 208,244,88\nTa 214,245,51200\nMvaa 208,184,92\n"
"Ta 216,245,53248\nMvaa 208,188,8\nTa 218,245,55296\nMvaa 208,192,12\nTa 220,250,49152\nZvaa 224,228,128\nTa 222,250,51200\n"
"v_add_u32 v245, 0x10000, v245\nMvaa 224,232,132\nTa 224,250,53248\nMvaa 224,236,32\nTa 226,250,55296\nMvaa 224,240,36\nTa 228,247,32768\n"
"Mvaa 224,244,136\nTa 230,247,34816\nv_add_u32 v250, 0x10000, v250\nMvaa 224,184,140\nTa 232,247,36864\nMvaa 224,188,40\n"
"buffer_store_dwordx4 v[168:171], v68, s[56:59], s78 offen offset:1024 sc1\nMvaa 224,192,44\nTa 234,247,38912\nZvaa 204,196,80\ns_waitcnt lgkmcnt(5)\n"
"s_add_u32 s76, s85, 0x300\nTa 236,252,32768\nMvaa 204,200,84\ns_and_b32 s76, s76, 0x3ff\nTa 238,252,34816\nMvaa 204,204,0\n"
"s_add_u32 s76, s76, 0x10000\nTa 240,252,36864\nMvaa 204,208,4\ns_add_u32 m0, s94, 0x4000\nTa 242,252,38912\nMvaa 204,212,88\nDL 240,52,76,0\n"
"Mvaa 204,216,92\nTa 244,247,49152\nMvaa 204,220,8\ns_waitcnt lgkmcnt(5)\nTa 246,247,51200\nMvaa 204,224,12\nDL 241,52,76,1024\nZvaa 220,196,128\n"
"Ta 184,247,53248\nMvaa 220,200,132\nTa 186,247,55296\nMvaa 220,204,32\nDL 242,52,76,2048\nMvaa 220,208,36\nTa 188,252,49152\n"
"v_add_u32 v247, 0x10000, v247\nMvaa 220,212,136\nTa 190,252,51200\nMvaa 220,216,140\n"
"buffer_store_dwordx4 v[184:187], v68, s[56:59], s78 offen offset:3072 sc1\nMvaa 220,220,40\nTa 192,252,53248\nMvaa 220,224,44\nTa 194,252,55296\n"
"Zvaa 236,196,96\nDL 243,52,76,3072\nMvaa 236,200,100\nTa 196,244,0\nv_add_u32 v252, 0x10000, v252\nMvaa 236,204,16\nTa 198,244,2048\nMvaa 236,208,20\n"
"Ta 200,244,4096\nMvaa 236,212,104\nTa 202,244,6144\nMvaa 236,216,108\nTa 204,249,0\nMvaa 236,220,24\nTa 206,249,2048\nMvaa 236,224,28\n"
"Ta 208,249,4096\nZvaa 212,228,80\nTa 210,249,6144\nMvaa 212,232,84\ns_waitcnt lgkmcnt(6)\nTa 212,244,16384\nMvaa 212,236,0\nTa 214,244,18432\n"
"Mvaa 212,240,4\nTa 216,244,20480\nMvaa 212,244,88\nTa 218,244,22528\nMvaa 212,184,92\nTa 220,249,16384\nMvaa 212,188,8\nTa 222,249,18432\n"
"Mvaa 212,192,12\nTa 224,249,20480\nZvaa 228,228,128\nTa 226,249,22528\nMvaa 228,232,132\nTa 228,246,0\nMvaa 228,236,32\nTa 230,246,2048\n"
"Mvaa 228,240,36\nTa 232,246,4096\nMvaa 228,244,136\nTa 234,246,6144\nMvaa 228,184,140\nTa 236,251,0\nMvaa 228,188,40\ns_waitcnt vmcnt(23)\n"
"Ta 238,251,2048\nMvaa 228,192,44\ns_barrier\nMvav 200,196,0\ns_waitcnt lgkmcnt(5)\ns_add_u32 s76, s85, 0x300\ns_waitcnt vmcnt(18)\nMvav 200,200,8\n"
"s_and_b32 s76, s76, 0x3ff\nTa 240,251,4096\nv_cmp_ne_u64 vcc, s[86:87], v[76:77]\nMvav 200,204,4\ns_add_u32 s76, s76, 0x40000\ns_cmp_lg_u64 vcc, 0\n"
"s_cbranch_scc1 .Lfa_sp86\n.Lfa_sr86:\nMvav 200,208,12\ns_add_u32 m0, s94, 0x8000\nTa 242,251,6144\nMvav 200,212,16\ns_mul_i32 s90, s61, 143360\n"
"DL 240,52,76,0\nMvav 200,216,24\ns_add_u32 s90, s90, 0\nTa 244,246,16384\nMvav 200,220,20\ns_add_u32 s91, s90, 0x1000\nTa 246,246,18432\n"
"Mvav 200,224,28\nbuffer_load_dwordx4 a[48:51], v68, s[56:59], s90 offen offset:0 sc1\nMvav 216,196,96\nTa 184,246,20480\nMvav 216,200,104\n"
"Ta 186,246,22528\nMvav 216,204,100\nDL 241,52,76,1024\nMvav 216,208,108\nTa 188,251,16384\nMvav 216,212,112\nTa 190,251,18432\nMvav 216,216,120\n"
"DL 242,52,76,2048\nMvav 216,220,116\nTa 192,251,20480\nMvav 216,224,124\nTa 194,251,22528\nMvav 232,196,32\nDL 243,52,76,3072\nMvav 232,200,40\n"
"Ta 196,245,0\nMvav 232,204,36\nTa 198,245,2048\nMvav 232,208,44\nTa 200,245,4096\nMvav 232,212,48\nTa 202,245,6144\nMvav 232,216,56\n"
"buffer_load_dwordx4 a[56:59], v68, s[56:59], s90 offen offset:1024 sc1\nMvav 232,220,52\nTa 204,250,0\nMvav 232,224,60\ns_waitcnt lgkmcnt(5)\n"
"Ta 206,250,2048\nMvav 208,228,0\nTa 208,250,4096\nMvav 208,232,8\nTa 210,250,6144\nMvav 208,236,4\nTa 212,245,16384\nMvav 208,240,12\n"
"Ta 214,245,18432\nMvav 208,244,16\nTa 216,245,20480\nMvav 208,184,24\nTa 218,245,22528\nMvav 208,188,20\nTa 220,250,16384\nMvav 208,192,28\n"
"Ta 222,250,18432\nMvav 224,228,96\n.Lfa_pd21836:\ns_cbranch_execz .Lfa_wr11\nTa 224,250,20480\nMvav 224,232,104\nTa 226,250,22528\nMvav 224,236,100\n"
"Ta 228,247,0\nMvav 224,240,108\nbuffer_load_dwordx4 a[64:67], v68, s[56:59], s90 offen offset:2048 sc1\nMvav 224,244,112\nTa 230,247,2048\n"
"Mvav 224,184,120\nTa 232,247,4096\nMvav 224,188,116\nTa 234,247,6144\nMvav 224,192,124\ns_waitcnt lgkmcnt(5)\nTa 236,252,0\nMvav 204,196,0\n"
"s_add_u32 s76, s85, 0x300\nTa 238,252,2048\nMvav 204,200,8\ns_and_b32 s76, s76, 0x3ff\n"
"buffer_load_dwordx2 v[76:77], off, s[56:59], s72 offset:256 sc1\nMvav 204,204,4\ns_add_u32 s76, s76, 0x50000\nTa 240,252,4096\nMvav 204,208,12\n"
"s_add_u32 m0, s94, 0xc000\nTa 242,252,6144\nMvav 204,212,16\nDL 240,52,76,0\nMvav 204,216,24\nTa 244,247,16384\nMvav 204,220,20\n"
"s_waitcnt lgkmcnt(4)\nTa 246,247,18432\nMvav 204,224,28\nDL 241,52,76,1024\nMvav 220,196,96\nTa 184,247,20480\nMvav 220,200,104\nTa 186,247,22528\n"
"Mvav 220,204,100\nbuffer_load_dwordx4 a[72:75], v68, s[56:59], s90 offen offset:3072 sc1\nMvav 220,208,108\nTa 188,252,16384\nMvav 220,212,112\n"
"Ta 190,252,18432\nMvav 220,216,120\nDL 242,52,76,2048\nMvav 220,220,116\nTa 192,252,20480\nMvav 220,224,124\nTa 194,252,22528\nMvav 236,196,32\n"
"DL 243,52,76,3072\nMvav 236,200,40\nTa 196,244,32768\nMvav 236,204,36\nTa 198,244,34816\nMvav 236,208,44\nTa 200,244,36864\nMvav 236,212,48\n"
"Ta 202,244,38912\nMvav 236,216,56\nTa 204,249,32768\nMvav 236,220,52\nTa 206,249,34816\nMvav 236,224,60\nTa 208,249,36864\nMvav 212,228,0\n"
"buffer_load_dwordx4 a[112:115], v68, s[56:59], s91 offen offset:0 sc1\nMvav 212,232,8\ns_waitcnt lgkmcnt(5)\ns_add_u32 s78, s61, 4\nTa 210,249,38912\n"
"Mvav 212,236,4\ns_and_b32 s78, s78, 7\nTa 212,244,49152\nMvav 212,240,12\ns_mul_i32 s78, s78, 143360\nTa 214,244,51200\nCV 200,200\nMvav 212,244,16\n"
"s_add_u32 s78, s78, 61440\nTa 216,244,53248\nCV 201,202\nMvav 212,184,24\ns_add_u32 s77, s78, 0x1000\nTa 218,244,55296\nCV 202,204\nMvav 212,188,20\n"
"Ta 220,249,49152\nCV 203,206\nMvav 212,192,28\nTa 222,249,51200\nv_add_u32 v244, 0x10000, v244\nMvav 228,228,96\nTa 224,249,53248\nCV 216,216\n"
"Mvav 228,232,104\nTa 226,249,55296\nCV 217,218\nMvav 228,236,100\nTa 228,246,32768\nCV 218,220\nMvav 228,240,108\nTa 230,246,34816\n"
"v_add_u32 v249, 0x10000, v249\nMvav 228,244,112\nTa 232,246,36864\nCV 219,222\nMvav 228,184,120\nTa 234,246,38912\nCV 232,232\nMvav 228,188,116\n"
"s_waitcnt vmcnt(15)\nTa 236,251,32768\nCV 233,234\nMvav 228,192,124\ns_barrier\nZvaa 160,196,80\ns_waitcnt lgkmcnt(5)\ns_add_u32 s76, s85, 0x380\n"
"Ta 238,251,34816\nMvaa 160,200,84\ns_and_b32 s76, s76, 0x3ff\nbuffer_store_dwordx4 v[200:203], v68, s[56:59], s78 offen offset:0 sc1\nMvaa 160,204,0\n"
"s_add_u32 s76, s76, 0x0\nTa 240,251,36864\nMvaa 160,208,4\ns_add_u32 m0, s94, 0x10000\nTa 242,251,38912\nMvaa 160,212,88\ns_add_u32 s88, s61, 3\n"
"DL 240,52,76,0\nMvaa 160,216,92\ns_and_b32 s88, s88, 7\nTa 244,246,49152\nMvaa 160,220,8\ns_lshl_b32 s88, s88, 11\nTa 246,246,51200\nMvaa 160,224,12\n"
"s_add_u32 s88, s88, 0x118200\nbuffer_store_dwordx2 a[248:249], v69, s[56:59], s88 offen sc1\nZvaa 176,196,128\nTa 184,246,53248\nCV 234,236\n"
"Mvaa 176,200,132\nTa 186,246,55296\nCV 235,238\nMvaa 176,204,32\nDL 241,52,76,1024\nMvaa 176,208,36\nTa 188,251,49152\nv_add_u32 v246, 0x10000, v246\n"
"Mvaa 176,212,136\nTa 190,251,51200\nCV 208,208\nMvaa 176,216,140\nDL 242,52,76,2048\nMvaa 176,220,40\nTa 192,251,53248\nCV 209,210\nMvaa 176,224,44\n"
"Ta 194,251,55296\nCV 210,212\nZvaa 192,196,96\nbuffer_store_dwordx4 v[216:219], v68, s[56:59], s78 offen offset:2048 sc1\nMvaa 192,200,100\n"
"Ta 196,245,32768\nv_add_u32 v251, 0x10000, v251\nMvaa 192,204,16\nTa 198,245,34816\nCV 211,214\nMvaa 192,208,20\nDL 243,52,76,3072\nMvaa 192,212,104\n"
"Ta 200,245,36864\nCV 224,224\nMvaa 192,216,108\nTa 202,245,38912\nCV 225,226\nMvaa 192,220,24\nTa 204,250,32768\nCV 226,228\nMvaa 192,224,28\n"
"s_waitcnt lgkmcnt(5)\nTa 206,250,34816\nCV 227,230\nZvaa 168,228,80\nTa 208,250,36864\nMvaa 168,232,84\nTa 210,250,38912\nMvaa 168,236,0\n"
"Ta 212,245,49152\nMvaa 168,240,4\nTa 214,245,51200\nMvaa 168,244,88\nTa 216,245,53248\nMvaa 168,184,92\nTa 218,245,55296\nMvaa 168,188,8\n"
"buffer_store_dwordx4 v[232:235], v68, s[56:59], s77 offen offset:0 sc1\nMvaa 168,192,12\nTa 220,250,49152\nv_add_u32 v245, 0x10000, v245\n"
"Zvaa 184,228,128\nTa 222,250,51200\nMvaa 184,232,132\nTa 224,250,53248\nMvaa 184,236,32\nTa 226,250,55296\nMvaa 184,240,36\nTa 228,247,32768\n"
"Mvaa 184,244,136\nTa 230,247,34816\nv_add_u32 v250, 0x10000, v250\nMvaa 184,184,140\nTa 232,247,36864\nMvaa 184,188,40\nTa 234,247,38912\n"
"Mvaa 184,192,44\ns_waitcnt lgkmcnt(6)\nTa 236,252,32768\nZvaa 164,196,80\ns_add_u32 s76, s85, 0x380\nTa 238,252,34816\nMvaa 164,200,84\n"
"s_and_b32 s76, s76, 0x3ff\nTa 240,252,36864\nMvaa 164,204,0\ns_add_u32 s76, s76, 0x10000\nTa 242,252,38912\nMvaa 164,208,4\n"
"s_add_u32 m0, s94, 0x14000\nTa 244,247,49152\nMvaa 164,212,88\nbuffer_store_dwordx4 v[208:211], v68, s[56:59], s78 offen offset:1024 sc1\n"
"Mvaa 164,216,92\nTa 246,247,51200\nMvaa 164,220,8\ns_waitcnt lgkmcnt(5)\nTa 184,247,53248\nMvaa 164,224,12\nDL 240,52,76,0\nZvaa 180,196,128\n"
"Ta 186,247,55296\nMvaa 180,200,132\nTa 188,252,49152\nMvaa 180,204,32\nDL 241,52,76,1024\nMvaa 180,208,36\nTa 190,252,51200\n"
"v_add_u32 v247, 0x10000, v247\nMvaa 180,212,136\nTa 192,252,53248\nMvaa 180,216,140\nDL 242,52,76,2048\nMvaa 180,220,40\nTa 194,252,55296\n"
"Mvaa 180,224,44\nZvaa 196,196,96\nDL 243,52,76,3072\nMvaa 196,200,100\nTa 196,244,0\nv_add_u32 v252, 0x10000, v252\nMvaa 196,204,16\nTa 198,244,2048\n"
"Mvaa 196,208,20\nbuffer_store_dwordx4 v[224:227], v68, s[56:59], s78 offen offset:3072 sc1\nMvaa 196,212,104\nTa 200,244,4096\nMvaa 196,216,108\n"
"Ta 202,244,6144\nMvaa 196,220,24\nTa 204,249,0\nMvaa 196,224,28\nTa 206,249,2048\nZvaa 172,228,80\nTa 208,249,4096\nMvaa 172,232,84\n"
"s_waitcnt lgkmcnt(5)\nTa 210,249,6144\nMvaa 172,236,0\nTa 212,244,16384\nMvaa 172,240,4\nTa 214,244,18432\nMvaa 172,244,88\nTa 216,244,20480\n"
"Mvaa 172,184,92\nTa 218,244,22528\nMvaa 172,188,8\nTa 220,249,16384\nMvaa 172,192,12\nTa 222,249,18432\nv_add_u32 v244, 0xfffe0000, v244\n"
"Zvaa 188,228,128\nTa 224,249,20480\nMvaa 188,232,132\nTa 226,249,22528\nMvaa 188,236,32\nTa 228,246,0\nMvaa 188,240,36\nTa 230,246,2048\n"
"v_add_u32 v249, 0xfffe0000, v249\nMvaa 188,244,136\nTa 232,246,4096\nMvaa 188,184,140\nTa 234,246,6144\nMvaa 188,188,40\ns_waitcnt vmcnt(28)\n"
"Ta 236,251,0\nMvaa 188,192,44\ns_barrier\nMvav 160,196,0\ns_waitcnt lgkmcnt(5)\ns_add_u32 s76, s85, 0x380\ns_waitcnt vmcnt(20)\nMvav 160,200,8\n"
"s_and_b32 s76, s76, 0x3ff\nTa 238,251,2048\nv_cmp_ne_u64 vcc, s[86:87], v[76:77]\nMvav 160,204,4\ns_add_u32 s76, s76, 0x40000\ns_cmp_lg_u64 vcc, 0\n"
"s_cbranch_scc1 .Lfa_sp87\n.Lfa_sr87:\nMvav 160,208,12\ns_add_u32 m0, s94, 0x18000\nTa 240,251,4096\nMvav 160,212,16\ns_mul_i32 s90, s61, 143360\n"
"DL 240,52,76,0\nMvav 160,216,24\ns_add_u32 s90, s90, 20480\nTa 242,251,6144\nMvav 160,220,20\ns_add_u32 s91, s90, 0x1000\nTa 244,246,16384\n"
"Mvav 160,224,28\nbuffer_load_dwordx4 a[52:55], v68, s[56:59], s90 offen offset:0 sc1\nMvav 176,196,96\nTa 246,246,18432\nMvav 176,200,104\n"
"Ta 184,246,20480\nMvav 176,204,100\nDL 241,52,76,1024\nMvav 176,208,108\nTa 186,246,22528\nMvav 176,212,112\nTa 188,251,16384\nMvav 176,216,120\n"
"DL 242,52,76,2048\nMvav 176,220,116\nTa 190,251,18432\nv_add_u32 v246, 0xfffe0000, v246\nMvav 176,224,124\nTa 192,251,20480\nMvav 192,196,32\n"
"DL 243,52,76,3072\nMvav 192,200,40\nTa 194,251,22528\nMvav 192,204,36\nTa 196,245,0\nMvav 192,208,44\nTa 198,245,2048\n"
"v_add_u32 v251, 0xfffe0000, v251\nMvav 192,212,48\nTa 200,245,4096\nMvav 192,216,56\n"
"buffer_load_dwordx4 a[60:63], v68, s[56:59], s90 offen offset:1024 sc1\nMvav 192,220,52\nTa 202,245,6144\nMvav 192,224,60\ns_waitcnt lgkmcnt(5)\n"
"Ta 204,250,0\nMvav 168,228,0\nTa 206,250,2048\nMvav 168,232,8\nTa 208,250,4096\nMvav 168,236,4\nTa 210,250,6144\nMvav 168,240,12\nTa 212,245,16384\n"
"Mvav 168,244,16\nTa 214,245,18432\nMvav 168,184,24\nTa 216,245,20480\nMvav 168,188,20\ns_waitcnt lgkmcnt(6)\nTa 218,245,22528\nMvav 168,192,28\n"
"Ta 220,250,16384\nMvav 184,228,96\nTa 222,250,18432\nv_add_u32 v245, 0xfffe0000, v245\nMvav 184,232,104\nTa 224,250,20480\nMvav 184,236,100\n"
"Ta 226,250,22528\nMvav 184,240,108\nbuffer_load_dwordx4 a[68:71], v68, s[56:59], s90 offen offset:2048 sc1\nMvav 184,244,112\nTa 228,247,0\n"
"v_add_u32 v250, 0xfffe0000, v250\nMvav 184,184,120\nTa 230,247,2048\nMvav 184,188,116\nTa 232,247,4096\nMvav 184,192,124\nTa 234,247,6144\n"
"Mvav 164,196,0\ns_add_u32 s76, s85, 0x380\nTa 236,252,0\nMvav 164,200,8\ns_waitcnt lgkmcnt(5)\ns_and_b32 s76, s76, 0x3ff\n"
"buffer_load_dwordx2 v[76:77], off, s[56:59], s72 offset:512 sc1\nMvav 164,204,4\ns_add_u32 s76, s76, 0x50000\nTa 238,252,2048\nMvav 164,208,12\n"
"s_add_u32 m0, s94, 0x1c000\nTa 240,252,4096\nMvav 164,212,16\nDL 240,52,76,0\nMvav 164,216,24\nTa 242,252,6144\nMvav 164,220,20\nTa 244,247,16384\n"
"Mvav 164,224,28\nDL 241,52,76,1024\nMvav 180,196,96\nTa 246,247,18432\nMvav 180,200,104\nTa 184,247,20480\nMvav 180,204,100\n"
"buffer_load_dwordx4 a[76:79], v68, s[56:59], s90 offen offset:3072 sc1\nMvav 180,208,108\nTa 186,247,22528\nMvav 180,212,112\nTa 188,252,16384\n"
"Mvav 180,216,120\nDL 242,52,76,2048\nMvav 180,220,116\nTa 190,252,18432\nv_add_u32 v247, 0xfffe0000, v247\nMvav 180,224,124\nTa 192,252,20480\n"
"Mvav 196,196,32\nDL 243,52,76,3072\nMvav 196,200,40\nTa 194,252,22528\nMvav 196,204,36\nTa 196,244,0\nMvav 196,208,44\nTa 198,244,2048\n"
"v_add_u32 v252, 0xfffe0000, v252\nMvav 196,212,48\nTa 200,244,4096\nMvav 196,216,56\nTa 202,244,6144\nMvav 196,220,52\nTa 204,249,0\nMvav 196,224,60\n"
"s_waitcnt lgkmcnt(6)\nTa 206,249,2048\nMvav 172,228,0\nbuffer_load_dwordx4 a[116:119], v68, s[56:59], s91 offen offset:0 sc1\nMvav 172,232,8\n"
"s_add_u32 s78, s61, 5\nTa 208,249,4096\nMvav 172,236,4\ns_and_b32 s78, s78, 7\nTa 210,249,6144\nMvav 172,240,12\ns_mul_i32 s78, s78, 143360\n"
"Ta 212,244,16384\nCV 160,160\nMvav 172,244,16\ns_add_u32 s78, s78, 81920\nTa 214,244,18432\nCV 161,162\nMvav 172,184,24\ns_add_u32 s77, s78, 0x1000\n"
"Ta 216,244,20480\nCV 162,164\nMvav 172,188,20\ns_waitcnt lgkmcnt(5)\nTa 218,244,22528\nCV 163,166\nMvav 172,192,28\nTa 220,249,16384\nCV 176,176\n"
"Mvav 188,228,96\nTa 222,249,18432\nCV 177,178\nMvav 188,232,104\nTa 224,249,20480\nCV 178,180\nMvav 188,236,100\nTa 226,249,22528\nCV 179,182\n"
"Mvav 188,240,108\nTa 228,246,0\nCV 192,192\nMvav 188,244,112\nTa 230,246,2048\nCV 193,194\nMvav 188,184,120\nTa 232,246,4096\nCV 194,196\n"
"Mvav 188,188,116\ns_waitcnt vmcnt(14)\nTa 234,246,6144\nCV 195,198\nMvav 188,192,124\ns_barrier\nZvaa 200,196,80\ns_add_u32 s76, s85, 0x400\n"
"Ta 236,251,0\nMvaa 200,200,84\ns_and_b32 s76, s76, 0x3ff\nbuffer_store_dwordx4 v[160:163], v68, s[56:59], s78 offen offset:0 sc1\nMvaa 200,204,0\n"
"s_waitcnt lgkmcnt(4)\ns_add_u32 s76, s76, 0x0\nTa 238,251,2048\nMvaa 200,208,4\n.Lfa_pd22367:\ns_cbranch_execz .Lfa_wr11\ns_add_u32 m0, s94, 0x20000\n"
"Ta 240,251,4096\nMvaa 200,212,88\ns_add_u32 s88, s61, 4\nDL 240,52,76,0\nMvaa 200,216,92\ns_and_b32 s88, s88, 7\nTa 242,251,6144\nMvaa 200,220,8\n"
"s_lshl_b32 s88, s88, 11\nTa 244,246,16384\nMvaa 200,224,12\ns_add_u32 s88, s88, 0x118300\n"
"buffer_store_dwordx2 a[248:249], v69, s[56:59], s88 offen sc1\nZvaa 216,196,128\nTa 246,246,18432\nCV 168,168\nMvaa 216,200,132\nTa 184,246,20480\n"
"CV 169,170\nMvaa 216,204,32\nDL 241,52,76,1024\nMvaa 216,208,36\nTa 186,246,22528\nCV 170,172\nMvaa 216,212,136\nTa 188,251,16384\nCV 171,174\n"
"Mvaa 216,216,140\nDL 242,52,76,2048\nMvaa 216,220,40\nTa 190,251,18432\nCV 184,184\nMvaa 216,224,44\nTa 192,251,20480\nCV 185,186\nZvaa 232,196,96\n"
"buffer_store_dwordx4 v[176:179], v68, s[56:59], s78 offen offset:2048 sc1\nMvaa 232,200,100\nTa 194,251,22528\nCV 186,188\nMvaa 232,204,16\n"
"Ta 196,245,0\nCV 187,190\nMvaa 232,208,20\nDL 243,52,76,3072\nMvaa 232,212,104\nTa 198,245,2048\nMvaa 232,216,108\nTa 200,245,4096\nMvaa 232,220,24\n"
"Ta 202,245,6144\nMvaa 232,224,28\ns_waitcnt lgkmcnt(5)\nTa 204,250,0\nZvaa 208,228,80\nTa 206,250,2048\nMvaa 208,232,84\nTa 208,250,4096\n"
"Mvaa 208,236,0\nTa 210,250,6144\nMvaa 208,240,4\nTa 212,245,16384\nMvaa 208,244,88\nTa 214,245,18432\nMvaa 208,184,92\nTa 216,245,20480\n"
"Mvaa 208,188,8\ns_waitcnt lgkmcnt(6)\nbuffer_store_dwordx4 v[192:195], v68, s[56:59], s77 offen offset:0 sc1\nMvaa 208,192,12\nTa 218,245,22528\n"
"Zvaa 224,228,128\nTa 220,250,16384\nMvaa 224,232,132\nTa 222,250,18432\nMvaa 224,236,32\nTa 224,250,20480\nMvaa 224,240,36\nTa 226,250,22528\n"
"Mvaa 224,244,136\nTa 228,247,0\nMvaa 224,184,140\nTa 230,247,2048\nMvaa 224,188,40\nTa 232,247,4096\nMvaa 224,192,44\nTa 234,247,6144\n"
"Zvaa 204,196,80\ns_add_u32 s76, s85, 0x400\nTa 236,252,0\nMvaa 204,200,84\ns_waitcnt lgkmcnt(6)\ns_and_b32 s76, s76, 0x3ff\nTa 238,252,2048\n"
"Mvaa 204,204,0\ns_add_u32 s76, s76, 0x10000\nTa 240,252,4096\nMvaa 204,208,4\ns_add_u32 m0, s94, 0x24000\nTa 242,252,6144\nMvaa 204,212,88\n"
"buffer_store_dwordx4 v[168:171], v68, s[56:59], s78 offen offset:1024 sc1\nMvaa 204,216,92\nTa 244,247,16384\nMvaa 204,220,8\ns_waitcnt lgkmcnt(5)\n"
"Ta 246,247,18432\nMvaa 204,224,12\nDL 240,52,76,0\nZvaa 220,196,128\nTa 184,247,20480\nMvaa 220,200,132\nTa 186,247,22528\nMvaa 220,204,32\n"
"DL 241,52,76,1024\nMvaa 220,208,36\nTa 188,252,16384\nMvaa 220,212,136\nTa 190,252,18432\nMvaa 220,216,140\nDL 242,52,76,2048\nMvaa 220,220,40\n"
"Ta 192,252,20480\nMvaa 220,224,44\nTa 194,252,22528\nZvaa 236,196,96\nDL 243,52,76,3072\nMvaa 236,200,100\nTa 196,244,32768\nMvaa 236,204,16\n"
"Ta 198,244,34816\nMvaa 236,208,20\nbuffer_store_dwordx4 v[184:187], v68, s[56:59], s78 offen offset:3072 sc1\nMvaa 236,212,104\nTa 200,244,36864\n"
"Mvaa 236,216,108\nTa 202,244,38912\nMvaa 236,220,24\nTa 204,249,32768\nMvaa 236,224,28\nTa 206,249,34816\nZvaa 212,228,80\nTa 208,249,36864\n"
"Mvaa 212,232,84\ns_waitcnt lgkmcnt(5)\nTa 210,249,38912\nMvaa 212,236,0\nTa 212,244,49152\nMvaa 212,240,4\nTa 214,244,51200\nMvaa 212,244,88\n"
"Ta 216,244,53248\nMvaa 212,184,92\nTa 218,244,55296\nMvaa 212,188,8\nTa 220,249,49152\nMvaa 212,192,12\nTa 222,249,51200\n"
"v_add_u32 v244, 0x10000, v244\nZvaa 228,228,128\nTa 224,249,53248\nMvaa 228,232,132\nTa 226,249,55296\nMvaa 228,236,32\nTa 228,246,32768\n"
"Mvaa 228,240,36\nTa 230,246,34816\nv_add_u32 v249, 0x10000, v249\nMvaa 228,244,136\nTa 232,246,36864\nMvaa 228,184,140\nTa 234,246,38912\n"
"Mvaa 228,188,40\ns_waitcnt vmcnt(29)\nTa 236,251,32768\nMvaa 228,192,44\ns_barrier\nMvav 200,196,0\ns_waitcnt lgkmcnt(5)\ns_add_u32 s76, s85, 0x400\n"
"s_waitcnt vmcnt(20)\nMvav 200,200,8\ns_and_b32 s76, s76, 0x3ff\nTa 238,251,34816\nv_cmp_ne_u64 vcc, s[86:87], v[76:77]\nMvav 200,204,4\n"
"s_add_u32 s76, s76, 0x40000\ns_cmp_lg_u64 vcc, 0\ns_cbranch_scc1 .Lfa_sp88\n.Lfa_sr88:\nMvav 200,208,12\ns_add_u32 m0, s94, 0x0\nTa 240,251,36864\n"
"Mvav 200,212,16\ns_mul_i32 s90, s61, 143360\nDL 240,52,76,0\nMvav 200,216,24\ns_add_u32 s90, s90, 40960\nTa 242,251,38912\nMvav 200,220,20\n"
"s_add_u32 s91, s90, 0x1000\nTa 244,246,49152\nMvav 200,224,28\nbuffer_load_dwordx4 a[120:123], v68, s[56:59], s90 offen offset:0 sc1\n"
"Mvav 216,196,96\nTa 246,246,51200\nMvav 216,200,104\nTa 184,246,53248\nMvav 216,204,100\nDL 241,52,76,1024\nMvav 216,208,108\nTa 186,246,55296\n"
"Mvav 216,212,112\nTa 188,251,49152\nMvav 216,216,120\nDL 242,52,76,2048\nMvav 216,220,116\nTa 190,251,51200\nv_add_u32 v246, 0x10000, v246\n"
"Mvav 216,224,124\nTa 192,251,53248\nMvav 232,196,32\nDL 243,52,76,3072\nMvav 232,200,40\nTa 194,251,55296\nMvav 232,204,36\nTa 196,245,32768\n"
"Mvav 232,208,44\nTa 198,245,34816\nv_add_u32 v251, 0x10000, v251\nMvav 232,212,48\nTa 200,245,36864\nMvav 232,216,56\n"
"buffer_load_dwordx4 a[144:147], v68, s[56:59], s90 offen offset:1024 sc1\nMvav 232,220,52\nTa 202,245,38912\nMvav 232,224,60\ns_waitcnt lgkmcnt(5)\n"
"Ta 204,250,32768\nMvav 208,228,0\nTa 206,250,34816\nMvav 208,232,8\nTa 208,250,36864\nMvav 208,236,4\nTa 210,250,38912\nMvav 208,240,12\n"
"Ta 212,245,49152\nMvav 208,244,16\nTa 214,245,51200\nMvav 208,184,24\nTa 216,245,53248\nMvav 208,188,20\ns_waitcnt lgkmcnt(6)\nTa 218,245,55296\n"
"Mvav 208,192,28\nTa 220,250,49152\nMvav 224,228,96\nTa 222,250,51200\nv_add_u32 v245, 0x10000, v245\nMvav 224,232,104\nTa 224,250,53248\n"
"Mvav 224,236,100\nTa 226,250,55296\nMvav 224,240,108\nbuffer_load_dwordx4 a[152:155], v68, s[56:59], s90 offen offset:2048 sc1\nMvav 224,244,112\n"
"Ta 228,247,32768\nv_add_u32 v250, 0x10000, v250\nMvav 224,184,120\nTa 230,247,34816\nMvav 224,188,116\nTa 232,247,36864\nMvav 224,192,124\n"
"Ta 234,247,38912\nMvav 204,196,0\ns_add_u32 s76, s85, 0x400\nTa 236,252,32768\nMvav 204,200,8\ns_waitcnt lgkmcnt(5)\ns_and_b32 s76, s76, 0x3ff\n"
"buffer_load_dwordx2 v[76:77], off, s[56:59], s72 offset:768 sc1\nMvav 204,204,4\ns_add_u32 s76, s76, 0x50000\nTa 238,252,34816\nMvav 204,208,12\n"
"s_add_u32 m0, s94, 0x4000\nTa 240,252,36864\nMvav 204,212,16\nDL 240,52,76,0\nMvav 204,216,24\nTa 242,252,38912\nMvav 204,220,20\nTa 244,247,49152\n"
"Mvav 204,224,28\nDL 241,52,76,1024\nMvav 220,196,96\nTa 246,247,51200\nMvav 220,200,104\nTa 184,247,53248\nMvav 220,204,100\n"
"buffer_load_dwordx4 a[160:163], v68, s[56:59], s90 offen offset:3072 sc1\nMvav 220,208,108\nTa 186,247,55296\nMvav 220,212,112\nTa 188,252,49152\n"
"Mvav 220,216,120\nDL 242,52,76,2048\nMvav 220,220,116\nTa 190,252,51200\nv_add_u32 v247, 0x10000, v247\nMvav 220,224,124\nTa 192,252,53248\n"
"Mvav 236,196,32\nDL 243,52,76,3072\nMvav 236,200,40\nTa 194,252,55296\nMvav 236,204,36\nTa 196,244,0\nMvav 236,208,44\nTa 198,244,2048\n"
"v_add_u32 v252, 0x10000, v252\nMvav 236,212,48\nTa 200,244,4096\nMvav 236,216,56\nTa 202,244,6144\nMvav 236,220,52\nTa 204,249,0\nMvav 236,224,60\n"
"s_waitcnt lgkmcnt(6)\nTa 206,249,2048\nMvav 212,228,0\nbuffer_load_dwordx4 a[168:171], v68, s[56:59], s91 offen offset:0 sc1\nMvav 212,232,8\n"
"s_add_u32 s78, s61, 6\nTa 208,249,4096\nMvav 212,236,4\ns_and_b32 s78, s78, 7\nTa 210,249,6144\nMvav 212,240,12\ns_mul_i32 s78, s78, 143360\n"
"Ta 212,244,16384\nCV 200,200\nMvav 212,244,16\ns_add_u32 s78, s78, 102400\nTa 214,244,18432\nCV 201,202\nMvav 212,184,24\ns_add_u32 s77, s78, 0x1000\n"
"Ta 216,244,20480\nCV 202,204\nMvav 212,188,20\ns_waitcnt lgkmcnt(5)\nTa 218,244,22528\nCV 203,206\nMvav 212,192,28\nTa 220,249,16384\nCV 216,216\n"
"Mvav 228,228,96\nTa 222,249,18432\nCV 217,218\nMvav 228,232,104\nTa 224,249,20480\nCV 218,220\nMvav 228,236,100\nTa 226,249,22528\nCV 219,222\n"
"Mvav 228,240,108\nTa 228,246,0\nCV 232,232\nMvav 228,244,112\nTa 230,246,2048\nCV 233,234\nMvav 228,184,120\nTa 232,246,4096\nCV 234,236\n"
"Mvav 228,188,116\ns_waitcnt vmcnt(14)\nTa 234,246,6144\nCV 235,238\nMvav 228,192,124\ns_barrier\nZvaa 160,196,80\ns_add_u32 s88, s61, 5\n"
"Ta 236,251,0\nMvaa 160,200,84\ns_and_b32 s88, s88, 7\nbuffer_store_dwordx4 v[200:203], v68, s[56:59], s78 offen offset:0 sc1\nMvaa 160,204,0\n"
"s_waitcnt lgkmcnt(4)\ns_lshl_b32 s88, s88, 11\nTa 238,251,2048\nMvaa 160,208,4\ns_add_u32 s88, s88, 0x118400\nTa 240,251,4096\nMvaa 160,212,88\n"
"buffer_store_dwordx2 a[248:249], v69, s[56:59], s88 offen sc1\nMvaa 160,216,92\nTa 242,251,6144\nCV 208,208\nMvaa 160,220,8\nTa 244,246,16384\n"
"CV 209,210\nMvaa 160,224,12\nTa 246,246,18432\nCV 210,212\nZvaa 176,196,128\nTa 184,246,20480\nCV 211,214\nMvaa 176,200,132\nTa 186,246,22528\n"
"CV 224,224\nMvaa 176,204,32\nTa 188,251,16384\nCV 225,226\nMvaa 176,208,36\nTa 190,251,18432\nCV 226,228\nMvaa 176,212,136\nTa 192,251,20480\n"
"CV 227,230\nMvaa 176,216,140\nTa 194,251,22528\nMvaa 176,220,40\nMvaa 176,224,44\n"
"buffer_store_dwordx4 v[216:219], v68, s[56:59], s78 offen offset:2048 sc1\nZvaa 192,196,96\nTa 196,245,0\nMvaa 192,200,100\nTa 198,245,2048\n"
"Mvaa 192,204,16\nTa 200,245,4096\nMvaa 192,208,20\nTa 202,245,6144\nMvaa 192,212,104\nTa 204,250,0\nMvaa 192,216,108\nTa 206,250,2048\n"
"Mvaa 192,220,24\nTa 208,250,4096\nMvaa 192,224,28\ns_waitcnt lgkmcnt(6)\nTa 210,250,6144\nZvaa 168,228,80\nTa 212,245,16384\nMvaa 168,232,84\n"
"Ta 214,245,18432\nMvaa 168,236,0\nTa 216,245,20480\nMvaa 168,240,4\nTa 218,245,22528\nMvaa 168,244,88\nTa 220,250,16384\nMvaa 168,184,92\n"
"buffer_store_dwordx4 v[232:235], v68, s[56:59], s77 offen offset:0 sc1\nMvaa 168,188,8\nTa 222,250,18432\nMvaa 168,192,12\nTa 224,250,20480\n"
"Zvaa 184,228,128\nTa 226,250,22528\nMvaa 184,232,132\nTa 228,247,0\nMvaa 184,236,32\nTa 230,247,2048\nMvaa 184,240,36\nTa 232,247,4096\n"
"Mvaa 184,244,136\nTa 234,247,6144\nMvaa 184,184,140\nTa 236,252,0\nMvaa 184,188,40\nTa 238,252,2048\nMvaa 184,192,44\ns_waitcnt lgkmcnt(6)\n"
"Ta 240,252,4096\nZvaa 164,196,80\nTa 242,252,6144\nMvaa 164,200,84\nTa 244,247,16384\nMvaa 164,204,0\nTa 246,247,18432\nMvaa 164,208,4\n"
"buffer_store_dwordx4 v[208:211], v68, s[56:59], s78 offen offset:1024 sc1\nMvaa 164,212,88\nTa 184,247,20480\nMvaa 164,216,92\nTa 186,247,22528\n"
"Mvaa 164,220,8\nTa 188,252,16384\nMvaa 164,224,12\nTa 190,252,18432\nZvaa 180,196,128\nTa 192,252,20480\nMvaa 180,200,132\nTa 194,252,22528\n"
"Mvaa 180,204,32\nMvaa 180,208,36\nMvaa 180,212,136\nMvaa 180,216,140\nMvaa 180,220,40\nMvaa 180,224,44\nZvaa 196,196,96\nTa 196,244,32768\n"
"Mvaa 196,200,100\nbuffer_store_dwordx4 v[224:227], v68, s[56:59], s78 offen offset:3072 sc1\nMvaa 196,204,16\nTa 198,244,34816\nMvaa 196,208,20\n"
"Ta 200,244,36864\nMvaa 196,212,104\nTa 202,244,38912\nMvaa 196,216,108\nTa 204,249,32768\nMvaa 196,220,24\nTa 206,249,34816\nMvaa 196,224,28\n"
"s_waitcnt lgkmcnt(5)\nTa 208,249,36864\nZvaa 172,228,80\nTa 210,249,38912\nMvaa 172,232,84\nTa 212,244,49152\nMvaa 172,236,0\nTa 214,244,51200\n"
"Mvaa 172,240,4\nTa 216,244,53248\nMvaa 172,244,88\n.Lfa_pd22896:\ns_cbranch_execz .Lfa_wr11\nTa 218,244,55296\nMvaa 172,184,92\nTa 220,249,49152\n"
"Mvaa 172,188,8\nTa 222,249,51200\nv_add_u32 v244, 0x10000, v244\nMvaa 172,192,12\nTa 224,249,53248\nZvaa 188,228,128\nTa 226,249,55296\n"
"Mvaa 188,232,132\nTa 228,246,32768\nMvaa 188,236,32\nTa 230,246,34816\nv_add_u32 v249, 0x10000, v249\nMvaa 188,240,36\nTa 232,246,36864\n"
"Mvaa 188,244,136\nTa 234,246,38912\nMvaa 188,184,140\nTa 236,251,32768\nMvaa 188,188,40\ns_waitcnt vmcnt(21) lgkmcnt(5)\nTa 238,251,34816\n"
"Mvaa 188,192,44\ns_barrier\nMvav 160,196,0\ns_waitcnt vmcnt(12)\nMvav 160,200,8\nTa 240,251,36864\nv_cmp_ne_u64 vcc, s[86:87], v[76:77]\n"
"Mvav 160,204,4\ns_cmp_lg_u64 vcc, 0\ns_cbranch_scc1 .Lfa_sp89\n.Lfa_sr89:\nMvav 160,208,12\n"
"buffer_load_dwordx2 v[76:77], off, s[56:59], s72 offset:1024 sc1\nMvav 160,212,16\ns_mul_i32 s90, s61, 143360\nTa 242,251,38912\n"
"v_mbcnt_lo_u32_b32 v85, -1, 0\nMvav 160,216,24\ns_add_u32 s90, s90, 61440\nTa 244,246,49152\nv_mbcnt_hi_u32_b32 v85, -1, v85\nMvav 160,220,20\n"
"s_add_u32 s91, s90, 0x1000\nbuffer_load_dwordx4 a[124:127], v68, s[56:59], s90 offen offset:0 sc1\nMvav 160,224,28\nTa 246,246,51200\n"
"v_bfrev_b32 v86, v85\nMvav 176,196,96\nTa 184,246,53248\nv_bfe_u32 v86, v86, 26, 2\nMvav 176,200,104\ns_lshl_b32 s75, s83, 2\nTa 186,246,55296\n"
"v_or_b32 v86, s75, v86\nMvav 176,204,100\nTa 188,251,49152\nv_and_b32 v87, 7, v85\nMvav 176,208,108\nTa 190,251,51200\nv_add_u32 v246, 0x10000, v246\n"
"Mvav 176,212,112\nTa 192,251,53248\nv_xor_b32 v86, v86, v87\nMvav 176,216,120\nTa 194,251,55296\nv_lshlrev_b32 v86, 4, v86\nMvav 176,220,116\n"
"v_and_b32 v248, 15, v85\nv_lshl_add_u32 v86, v248, 7, v86\nMvav 176,224,124\ns_mul_i32 s75, s84, 10240\nv_add_u32 v251, 0x10000, v251\n"
"Mvav 192,196,32\ns_add_u32 s75, s75, 0x10000\nTa 196,245,32768\nv_add_u32 v86, s75, v86\nMvav 192,200,40\nTa 198,245,34816\n"
"v_add_u32 v248, 0x800, v86\nMvav 192,204,36\ns_lshl_b32 s76, s83, 12\nTa 200,245,36864\nv_add_u32 v240, s76, v86\nMvav 192,208,44\nTa 202,245,38912\n"
"v_xor_b32 v241, 64, v240\nMvav 192,212,48\ns_lshl_b32 s76, s83, 11\nbuffer_load_dwordx4 a[148:151], v68, s[56:59], s90 offen offset:1024 sc1\n"
"Mvav 192,216,56\ns_add_u32 s76, s76, 0x1800\nTa 204,250,32768\nv_add_u32 v242, s76, v86\nMvav 192,220,52\nTa 206,250,34816\nv_xor_b32 v243, 64, v242\n"
"Mvav 192,224,60\ns_waitcnt lgkmcnt(5)\nTa 208,250,36864\nv_lshrrev_b32 v86, 3, v85\nMvav 168,228,0\nTa 210,250,38912\nv_lshlrev_b32 v84, 4, v87\n"
"Mvav 168,232,8\nTa 212,245,49152\nv_lshl_add_u32 v84, v86, 11, v84\nMvav 168,236,4\nTa 214,245,51200\nv_xor_b32 v87, v87, v86\nMvav 168,240,12\n"
"Ta 216,245,53248\nv_lshlrev_b32 v87, 4, v87\nMvav 168,244,16\nTa 218,245,55296\nv_lshl_add_u32 v87, v86, 7, v87\nMvav 168,184,24\n"
"s_mul_i32 s76, s83, 5120\nTa 220,250,49152\nMvav 168,188,20\ns_add_u32 s75, s75, s76\nTa 222,250,51200\nv_add_u32 v245, 0x10000, v245\n"
"Mvav 168,192,28\nTa 224,250,53248\nv_add_u32 v253, s75, v87\nMvav 184,228,96\nTa 226,250,55296\nMvav 184,232,104\nTa 228,247,32768\nMvav 184,236,100\n"
"buffer_load_dwordx4 a[156:159], v68, s[56:59], s90 offen offset:2048 sc1\nMvav 184,240,108\nTa 230,247,34816\nv_add_u32 v250, 0x10000, v250\n"
"Mvav 184,244,112\nTa 232,247,36864\nMvav 184,184,120\nTa 234,247,38912\nMvav 184,188,116\nTa 236,252,32768\nMvav 184,192,124\ns_waitcnt lgkmcnt(5)\n"
"Ta 238,252,34816\nMvav 164,196,0\nTa 240,252,36864\nMvav 164,200,8\nTa 242,252,38912\nMvav 164,204,4\nTa 244,247,49152\nMvav 164,208,12\n"
"Ta 246,247,51200\nMvav 164,212,16\nTa 184,247,53248\nMvav 164,216,24\nTa 186,247,55296\nMvav 164,220,20\nTa 188,252,49152\nMvav 164,224,28\n"
"Ta 190,252,51200\nv_add_u32 v247, 0x10000, v247\nMvav 180,196,96\nbuffer_load_dwordx4 a[164:167], v68, s[56:59], s90 offen offset:3072 sc1\n"
"Mvav 180,200,104\nTa 192,252,53248\nMvav 180,204,100\nTa 194,252,55296\nMvav 180,208,108\nMvav 180,212,112\nv_add_u32 v252, 0x10000, v252\n"
"Mvav 180,216,120\nbuffer_load_dwordx4 a[172:175], v68, s[56:59], s91 offen offset:0 sc1\nMvav 180,220,116\ns_add_u32 s78, s61, 7\nMvav 180,224,124\n"
"s_and_b32 s78, s78, 7\nMvav 196,196,32\ns_mul_i32 s78, s78, 143360\nTa 196,244,0\nMvav 196,200,40\ns_add_u32 s78, s78, 122880\nTa 198,244,2048\n"
"Mvav 196,204,36\ns_add_u32 s77, s78, 0x1000\nTa 200,244,4096\nMvav 196,208,44\nTa 202,244,6144\nMvav 196,212,48\nTa 204,249,0\nMvav 196,216,56\n"
"Ta 206,249,2048\nMvav 196,220,52\nTa 208,249,4096\nMvav 196,224,60\ns_waitcnt lgkmcnt(6)\nTa 210,249,6144\nMvav 172,228,0\nTa 212,244,16384\n"
"Mvav 172,232,8\nTa 214,244,18432\nMvav 172,236,4\nTa 216,244,20480\nMvav 172,240,12\nTa 218,244,22528\nCV 160,160\nMvav 172,244,16\nTa 220,249,16384\n"
"CV 161,162\nMvav 172,184,24\nTa 222,249,18432\nv_add_u32 v244, 0xfffe0000, v244\nMvav 172,188,20\nTa 224,249,20480\nCV 162,164\nMvav 172,192,28\n"
"Ta 226,249,22528\nCV 163,166\nMvav 188,228,96\nbuffer_store_dwordx4 v[160:163], v68, s[56:59], s78 offen offset:0 sc1\nMvav 188,232,104\n"
"Ta 228,246,0\nv_add_u32 v249, 0xfffe0000, v249\nMvav 188,236,100\nTa 230,246,2048\nCV 176,176\nMvav 188,240,108\nTa 232,246,4096\nCV 177,178\n"
"Mvav 188,244,112\nTa 234,246,6144\nCV 178,180\nMvav 188,184,120\nTa 236,251,0\nCV 179,182\nMvav 188,188,116\ns_waitcnt vmcnt(7) lgkmcnt(5)\n"
"Ta 238,251,2048\nCV 192,192\nMvav 188,192,124\ns_barrier\nZvaa 200,196,80\ns_add_u32 s88, s61, 6\nTa 240,251,4096\nMvaa 200,200,84\n"
"s_and_b32 s88, s88, 7\nbuffer_store_dwordx4 v[176:179], v68, s[56:59], s78 offen offset:2048 sc1\nMvaa 200,204,0\ns_lshl_b32 s88, s88, 11\n"
"Ta 242,251,6144\nMvaa 200,208,4\ns_add_u32 s88, s88, 0x118500\nTa 244,246,16384\nMvaa 200,212,88\n"
"buffer_store_dwordx2 a[248:249], v69, s[56:59], s88 offen sc1\nMvaa 200,216,92\nTa 246,246,18432\nCV 193,194\nMvaa 200,220,8\nTa 184,246,20480\n"
"CV 194,196\nMvaa 200,224,12\nTa 186,246,22528\nCV 195,198\nZvaa 216,196,128\nbuffer_store_dwordx4 v[192:195], v68, s[56:59], s77 offen offset:0 sc1\n"
"Mvaa 216,200,132\nTa 188,251,16384\nv_add_u32 v246, 0xfffe0000, v246\nMvaa 216,204,32\nTa 190,251,18432\nCV 168,168\nMvaa 216,208,36\n"
"Ta 192,251,20480\nCV 169,170\nMvaa 216,212,136\nTa 194,251,22528\nCV 170,172\nMvaa 216,216,140\nCV 171,174\nMvaa 216,220,40\n"
"buffer_store_dwordx4 v[168:171], v68, s[56:59], s78 offen offset:1024 sc1\nMvaa 216,224,44\nv_add_u32 v251, 0xfffe0000, v251\nCV 184,184\n"
"Zvaa 232,196,96\nTa 196,245,0\nCV 185,186\nMvaa 232,200,100\nTa 198,245,2048\nCV 186,188\nMvaa 232,204,16\nTa 200,245,4096\nCV 187,190\n"
"Mvaa 232,208,20\ns_waitcnt vmcnt(10)\nMvaa 232,212,104\nbuffer_store_dwordx4 v[184:187], v68, s[56:59], s78 offen offset:3072 sc1\nMvaa 232,216,108\n"
"Ta 202,245,6144\nv_cmp_ne_u64 vcc, s[86:87], v[76:77]\nMvaa 232,220,24\ns_cmp_lg_u64 vcc, 0\ns_cbranch_scc1 .Lfa_sp90\n.Lfa_sr90:\nMvaa 232,224,28\n"
"s_waitcnt lgkmcnt(3)\ns_mul_i32 s90, s61, 143360\nTa 204,250,0\nZvaa 208,228,80\ns_add_u32 s90, s90, 81920\nTa 206,250,2048\nMvaa 208,232,84\n"
"s_add_u32 s91, s90, 0x1000\nbuffer_load_dwordx4 v[128:131], v68, s[56:59], s90 offen offset:0 sc1\nMvaa 208,236,0\nTa 208,250,4096\nMvaa 208,240,4\n"
"Ta 210,250,6144\nMvaa 208,244,88\nTa 212,245,16384\nMvaa 208,184,92\nTa 214,245,18432\nMvaa 208,188,8\n"
"buffer_load_dwordx4 v[136:139], v68, s[56:59], s90 offen offset:1024 sc1\nMvaa 208,192,12\nTa 216,245,20480\nZvaa 224,228,128\nTa 218,245,22528\n"
"Mvaa 224,232,132\nTa 220,250,16384\nMvaa 224,236,32\nTa 222,250,18432\nv_add_u32 v245, 0xfffe0000, v245\nMvaa 224,240,36\n"
"buffer_load_dwordx4 v[144:147], v68, s[56:59], s90 offen offset:2048 sc1\nMvaa 224,244,136\nTa 224,250,20480\nMvaa 224,184,140\nTa 226,250,22528\n"
"Mvaa 224,188,40\nTa 228,247,0\nMvaa 224,192,44\ns_waitcnt lgkmcnt(5)\nTa 230,247,2048\nv_add_u32 v250, 0xfffe0000, v250\nZvaa 204,196,80\n"
"Ta 232,247,4096\nMvaa 204,200,84\nbuffer_load_dwordx2 v[76:77], off, s[56:59], s72 offset:1280 sc1\nMvaa 204,204,0\nTa 234,247,6144\nMvaa 204,208,4\n"
"Ta 236,252,0\nMvaa 204,212,88\nbuffer_load_dwordx4 v[152:155], v68, s[56:59], s90 offen offset:3072 sc1\nMvaa 204,216,92\ns_waitcnt lgkmcnt(4)\n"
"Ta 238,252,2048\nMvaa 204,220,8\nTa 240,252,4096\nMvaa 204,224,12\nTa 242,252,6144\nZvaa 220,196,128\nTa 244,247,16384\nMvaa 220,200,132\n"
"buffer_load_dwordx4 v[88:91], v68, s[56:59], s91 offen offset:0 sc1\nMvaa 220,204,32\nTa 246,247,18432\nMvaa 220,208,36\nTa 184,247,20480\n"
"Mvaa 220,212,136\nTa 186,247,22528\nMvaa 220,216,140\nTa 188,252,16384\nMvaa 220,220,40\nTa 190,252,18432\nv_add_u32 v247, 0xfffe0000, v247\n"
"Mvaa 220,224,44\nTa 192,252,20480\nZvaa 236,196,96\nTa 194,252,22528\nMvaa 236,200,100\nTa 196,244,0\nMvaa 236,204,16\nTa 198,244,2048\n"
"v_add_u32 v252, 0xfffe0000, v252\nMvaa 236,208,20\nTa 200,244,4096\nMvaa 236,212,104\nTa 202,244,6144\nMvaa 236,216,108\nTa 204,249,0\n"
"Mvaa 236,220,24\nTa 206,249,2048\nMvaa 236,224,28\ns_waitcnt lgkmcnt(6)\nTa 208,249,4096\nZvaa 212,228,80\nTa 210,249,6144\nMvaa 212,232,84\n"
"Ta 212,244,16384\nMvaa 212,236,0\nTa 214,244,18432\nMvaa 212,240,4\nTa 216,244,20480\nMvaa 212,244,88\nTa 218,244,22528\nMvaa 212,184,92\n"
"Ta 220,249,16384\nMvaa 212,188,8\nTa 222,249,18432\nMvaa 212,192,12\nTa 224,249,20480\nZvaa 228,228,128\nTa 226,249,22528\nMvaa 228,232,132\n"
"Ta 228,246,0\nMvaa 228,236,32\nTa 230,246,2048\nMvaa 228,240,36\nTa 232,246,4096\nMvaa 228,244,136\nTa 234,246,6144\nMvaa 228,184,140\nTa 236,251,0\n"
"Mvaa 228,188,40\ns_waitcnt vmcnt(6) lgkmcnt(5)\nTa 238,251,2048\nMvaa 228,192,44\ns_barrier\nMvav 200,196,0\ns_add_u32 s88, s61, 7\nTa 240,251,4096\n"
"Mvav 200,200,8\ns_and_b32 s88, s88, 7\nTa 242,251,6144\nMvav 200,204,4\ns_lshl_b32 s88, s88, 11\nTa 244,246,16384\nMvav 200,208,12\n"
"s_add_u32 s88, s88, 0x118600\nbuffer_store_dwordx2 a[248:249], v69, s[56:59], s88 offen sc1\nMvav 200,212,16\ns_waitcnt vmcnt(3)\nMvav 200,216,24\n"
"Ta 246,246,18432\nv_cmp_ne_u64 vcc, s[86:87], v[76:77]\nMvav 200,220,20\ns_cmp_lg_u64 vcc, 0\ns_cbranch_scc1 .Lfa_sp91\n.Lfa_sr91:\nMvav 200,224,28\n"
"s_mul_i32 s90, s61, 143360\nTa 184,246,20480\nMvav 216,196,96\ns_add_u32 s90, s90, 102400\nTa 186,246,22528\nMvav 216,200,104\n"
"s_add_u32 s91, s90, 0x1000\nbuffer_load_dwordx4 v[132:135], v68, s[56:59], s90 offen offset:0 sc1\nMvav 216,204,100\nTa 188,251,16384\n"
"Mvav 216,208,108\nTa 190,251,18432\nMvav 216,212,112\nTa 192,251,20480\nMvav 216,216,120\nTa 194,251,22528\nMvav 216,220,116\n"
"buffer_load_dwordx4 v[140:143], v68, s[56:59], s90 offen offset:1024 sc1\nMvav 216,224,124\nMvav 232,196,32\nTa 196,245,0\nMvav 232,200,40\n"
"Ta 198,245,2048\nMvav 232,204,36\nTa 200,245,4096\nMvav 232,208,44\nbuffer_load_dwordx4 v[148:151], v68, s[56:59], s90 offen offset:2048 sc1\n"
"Mvav 232,212,48\nTa 202,245,6144\nMvav 232,216,56\nTa 204,250,0\nMvav 232,220,52\nTa 206,250,2048\nMvav 232,224,60\ns_waitcnt lgkmcnt(5)\n"
"Ta 208,250,4096\nMvav 208,228,0\nbuffer_load_dwordx4 v[156:159], v68, s[56:59], s90 offen offset:3072 sc1\nMvav 208,232,8\nTa 210,250,6144\n"
"Mvav 208,236,4\nTa 212,245,16384\nMvav 208,240,12\nTa 214,245,18432\nMvav 208,244,16\nTa 216,245,20480\nMvav 208,184,24\n"
"buffer_load_dwordx4 v[92:95], v68, s[56:59], s91 offen offset:0 sc1\nMvav 208,188,20\nTa 218,245,22528\nMvav 208,192,28\nTa 220,250,16384\n"
"Mvav 224,228,96\nTa 222,250,18432\nMvav 224,232,104\nTa 224,250,20480\nMvav 224,236,100\nTa 226,250,22528\nMvav 224,240,108\nTa 228,247,0\n"
"Mvav 224,244,112\nTa 230,247,2048\nMvav 224,184,120\nTa 232,247,4096\nMvav 224,188,116\nTa 234,247,6144\nMvav 224,192,124\ns_waitcnt lgkmcnt(6)\n"
"Ta 236,252,0\nMvav 204,196,0\nTa 238,252,2048\nMvav 204,200,8\nTa 240,252,4096\nMvav 204,204,4\nTa 242,252,6144\nMvav 204,208,12\nTa 244,247,16384\n"
"Mvav 204,212,16\nTa 246,247,18432\nMvav 204,216,24\nTa 184,247,20480\nMvav 204,220,20\ns_waitcnt lgkmcnt(6)\nTa 186,247,22528\nMvav 204,224,28\n"
"Ta 188,252,16384\nMvav 220,196,96\nTa 190,252,18432\nMvav 220,200,104\n.Lfa_pd23441:\ns_cbranch_execz .Lfa_wr11\nTa 192,252,20480\nMvav 220,204,100\n"
"Ta 194,252,22528\nMvav 220,208,108\nMvav 220,212,112\nMvav 220,216,120\nMvav 220,220,116\nMvav 220,224,124\nMvav 236,196,32\n"
"buffer_load_dwordx2 v[76:77], off, s[56:59], s72 offset:1536 sc1\nMvav 236,200,40\ns_mul_i32 s75, s83, 81920\nMvav 236,204,36\n"
"s_add_u32 s76, s75, 16384\nMvav 236,208,44\ns_add_u32 s73, s75, 32768\nMvav 236,212,48\ns_add_u32 s74, s75, 49152\nMvav 236,216,56\n"
"s_add_u32 s85, s75, 65536\nMvav 236,220,52\ns_lshl_b32 s89, s61, 24\nMvav 236,224,60\ns_add_u32 s89, s89, 0x11c000\nMvav 212,228,0\nMvav 212,232,8\n"
"s_waitcnt lgkmcnt(0)\nMvav 212,236,4\nMvav 212,240,12\nMvav 212,244,16\nMvav 212,184,24\nMvav 212,188,20\nMvav 212,192,28\nMvav 228,228,96\n"
"Mvav 228,232,104\nMvav 228,236,100\nMvav 228,240,108\nMvav 228,244,112\nMvav 228,184,120\nMvav 228,188,116\nMvav 228,192,124\ns_waitcnt vmcnt(0)\n"
"v_cmp_ne_u64 vcc, s[86:87], v[76:77]\ns_nop 1\ns_cmp_lg_u64 vcc, 0\ns_cbranch_scc1 .Lfa_sp92\n.Lfa_sr92:\ns_mul_i32 s90, s61, 143360\n"
"s_add_u32 s90, s90, 122880\ns_add_u32 s91, s90, 0x1000\ns_waitcnt vmcnt(1)\nv_smfmac_f32_16x16x64_bf16 v[200:203], v[64:67], a[48:55], v79\n"
"buffer_load_dwordx4 v[160:163], v68, s[56:59], s90 offen offset:0 sc1\nv_smfmac_f32_16x16x64_bf16 v[200:203], v[64:67], a[120:127], v79\n"
"v_smfmac_f32_16x16x64_bf16 v[200:203], v[64:67], v[128:135], v79\nbuffer_load_dwordx4 v[164:167], v68, s[56:59], s90 offen offset:1024 sc1\n"
"v_smfmac_f32_16x16x64_bf16 v[204:207], v[72:75], a[48:55], v79\nv_smfmac_f32_16x16x64_bf16 v[204:207], v[72:75], a[120:127], v79\n"
"buffer_load_dwordx4 v[168:171], v68, s[56:59], s90 offen offset:2048 sc1\nv_smfmac_f32_16x16x64_bf16 v[204:207], v[72:75], v[128:135], v79\n"
"v_smfmac_f32_16x16x64_bf16 v[208:211], v[64:67], a[56:63], v79\nbuffer_load_dwordx4 v[172:175], v68, s[56:59], s90 offen offset:3072 sc1\n"
"v_smfmac_f32_16x16x64_bf16 v[208:211], v[64:67], a[144:151], v79\nv_smfmac_f32_16x16x64_bf16 v[208:211], v[64:67], v[136:143], v79\n"
"buffer_load_dwordx4 v[176:179], v68, s[56:59], s91 offen offset:0 sc1\nv_smfmac_f32_16x16x64_bf16 v[212:215], v[72:75], a[56:63], v79\n"
"v_smfmac_f32_16x16x64_bf16 v[212:215], v[72:75], a[144:151], v79\nbuffer_store_dwordx2 a[248:249], v69, s[56:59], s89 offen sc1\n"
"v_smfmac_f32_16x16x64_bf16 v[212:215], v[72:75], v[136:143], v79\nv_smfmac_f32_16x16x64_bf16 v[216:219], v[64:67], a[64:71], v79\n"
"v_smfmac_f32_16x16x64_bf16 v[216:219], v[64:67], a[152:159], v79\nv_smfmac_f32_16x16x64_bf16 v[216:219], v[64:67], v[144:151], v79\n"
"v_smfmac_f32_16x16x64_bf16 v[220:223], v[72:75], a[64:71], v79\nv_smfmac_f32_16x16x64_bf16 v[220:223], v[72:75], a[152:159], v79\n"
"v_smfmac_f32_16x16x64_bf16 v[220:223], v[72:75], v[144:151], v79\nv_smfmac_f32_16x16x64_bf16 v[224:227], v[64:67], a[72:79], v79\n"
"v_smfmac_f32_16x16x64_bf16 v[224:227], v[64:67], a[160:167], v79\nv_smfmac_f32_16x16x64_bf16 v[224:227], v[64:67], v[152:159], v79\n"
"v_smfmac_f32_16x16x64_bf16 v[228:231], v[72:75], a[72:79], v79\nv_smfmac_f32_16x16x64_bf16 v[228:231], v[72:75], a[160:167], v79\n"
"v_smfmac_f32_16x16x64_bf16 v[228:231], v[72:75], v[152:159], v79\nv_smfmac_f32_16x16x64_bf16 v[232:235], v[64:67], a[112:119], v79\n"
"v_smfmac_f32_16x16x64_bf16 v[232:235], v[64:67], a[168:175], v79\nv_smfmac_f32_16x16x64_bf16 v[232:235], v[64:67], v[88:95], v79\n"
"v_smfmac_f32_16x16x64_bf16 v[236:239], v[72:75], a[112:119], v79\nv_smfmac_f32_16x16x64_bf16 v[236:239], v[72:75], a[168:175], v79\n"
"v_smfmac_f32_16x16x64_bf16 v[236:239], v[72:75], v[88:95], v79\ns_waitcnt vmcnt(1)\nMvav 200,180,160\nMvav 204,252,160\nMvav 208,180,164\n"
"Mvav 212,252,164\nMvav 216,180,168\nMvav 220,252,168\nMvav 224,180,172\nMvav 228,252,172\nMvav 232,180,176\nMvav 236,252,176\nCV 200,200\nCV 201,202\n"
"CV 202,204\nCV 203,206\nCV 208,208\nCV 209,210\nCV 210,212\nCV 211,214\nv_permlane16_swap_b32 v200, v202\nv_permlane16_swap_b32 v201, v203\n"
"ds_write_b128 v240, v[200:203]\nCV 216,216\nCV 217,218\nCV 218,220\nCV 219,222\nv_permlane16_swap_b32 v208, v210\nv_permlane16_swap_b32 v209, v211\n"
"ds_write_b128 v241, v[208:211]\nCV 224,224\nCV 225,226\nCV 226,228\nCV 227,230\nv_permlane16_swap_b32 v216, v218\nv_permlane16_swap_b32 v217, v219\n"
"ds_write_b128 v242, v[216:219]\nCV 232,232\nCV 233,234\nCV 234,236\nCV 235,238\nv_permlane16_swap_b32 v224, v226\nv_permlane16_swap_b32 v225, v227\n"
"ds_write_b128 v243, v[224:227]\nv_permlane16_swap_b32 v232, v234\nv_permlane16_swap_b32 v233, v235\nds_write_b128 v248, v[232:235]\n"
"s_waitcnt lgkmcnt(0)\ns_barrier\nRv 160,253,0\nRv 164,253,1024\nRv 168,253,2048\nRv 172,253,3072\nRv 176,253,4096\ns_waitcnt lgkmcnt(4)\n"
"buffer_store_dwordx4 v[160:163], v84, s[64:67], s75 offen sc1\ns_waitcnt lgkmcnt(3)\nbuffer_store_dwordx4 v[164:167], v84, s[64:67], s76 offen sc1\n"
"s_waitcnt lgkmcnt(2)\nbuffer_store_dwordx4 v[168:171], v84, s[64:67], s73 offen sc1\ns_waitcnt lgkmcnt(1)\n"
"buffer_store_dwordx4 v[172:175], v84, s[64:67], s74 offen sc1\ns_waitcnt lgkmcnt(0)\nbuffer_store_dwordx4 v[176:179], v84, s[64:67], s85 offen sc1\n"
"s_endpgm\n.Lfa_sp86:\ns_cmp_eq_u32 s95, 0\ns_cbranch_scc1 .Lfa_sr86\ns_sub_u32 s95, s95, 1\ns_sleep 1\n"
"buffer_load_dwordx2 v[76:77], off, s[56:59], s72 offset:0 sc1\ns_waitcnt vmcnt(0)\nv_cmp_ne_u64 vcc, s[86:87], v[76:77]\ns_nop 1\n"
"s_cmp_lg_u64 vcc, 0\ns_cbranch_scc1 .Lfa_sp86\ns_branch .Lfa_sr86\n.Lfa_sp87:\ns_cmp_eq_u32 s95, 0\ns_cbranch_scc1 .Lfa_sr87\ns_sub_u32 s95, s95, 1\n"
"s_sleep 1\nbuffer_load_dwordx2 v[76:77], off, s[56:59], s72 offset:256 sc1\ns_waitcnt vmcnt(0)\nv_cmp_ne_u64 vcc, s[86:87], v[76:77]\ns_nop 1\n"
"s_cmp_lg_u64 vcc, 0\ns_cbranch_scc1 .Lfa_sp87\ns_branch .Lfa_sr87\n.Lfa_sp88:\ns_cmp_eq_u32 s95, 0\ns_cbranch_scc1 .Lfa_sr88\ns_sub_u32 s95, s95, 1\n"
"s_sleep 1\nbuffer_load_dwordx2 v[76:77], off, s[56:59], s72 offset:512 sc1\ns_waitcnt vmcnt(0)\nv_cmp_ne_u64 vcc, s[86:87], v[76:77]\ns_nop 1\n"
"s_cmp_lg_u64 vcc, 0\ns_cbranch_scc1 .Lfa_sp88\ns_branch .Lfa_sr88\n.Lfa_sp89:\ns_cmp_eq_u32 s95, 0\ns_cbranch_scc1 .Lfa_sr89\ns_sub_u32 s95, s95, 1\n"
"s_sleep 1\nbuffer_load_dwordx2 v[76:77], off, s[56:59], s72 offset:768 sc1\ns_waitcnt vmcnt(0)\nv_cmp_ne_u64 vcc, s[86:87], v[76:77]\ns_nop 1\n"
"s_cmp_lg_u64 vcc, 0\ns_cbranch_scc1 .Lfa_sp89\ns_branch .Lfa_sr89\n.Lfa_sp90:\ns_cmp_eq_u32 s95, 0\ns_cbranch_scc1 .Lfa_sr90\ns_sub_u32 s95, s95, 1\n"
"s_sleep 1\nbuffer_load_dwordx2 v[76:77], off, s[56:59], s72 offset:1024 sc1\ns_waitcnt vmcnt(0)\nv_cmp_ne_u64 vcc, s[86:87], v[76:77]\ns_nop 1\n"
"s_cmp_lg_u64 vcc, 0\ns_cbranch_scc1 .Lfa_sp90\ns_branch .Lfa_sr90\n.Lfa_sp91:\ns_cmp_eq_u32 s95, 0\ns_cbranch_scc1 .Lfa_sr91\ns_sub_u32 s95, s95, 1\n"
"s_sleep 1\nbuffer_load_dwordx2 v[76:77], off, s[56:59], s72 offset:1280 sc1\ns_waitcnt vmcnt(0)\nv_cmp_ne_u64 vcc, s[86:87], v[76:77]\ns_nop 1\n"
"s_cmp_lg_u64 vcc, 0\ns_cbranch_scc1 .Lfa_sp91\ns_branch .Lfa_sr91\n.Lfa_sp92:\ns_cmp_eq_u32 s95, 0\ns_cbranch_scc1 .Lfa_sr92\ns_sub_u32 s95, s95, 1\n"
"s_sleep 1\nbuffer_load_dwordx2 v[76:77], off, s[56:59], s72 offset:1536 sc1\ns_waitcnt vmcnt(0)\nv_cmp_ne_u64 vcc, s[86:87], v[76:77]\ns_nop 1\n"
"s_cmp_lg_u64 vcc, 0\ns_cbranch_scc1 .Lfa_sp92\ns_branch .Lfa_sr92\n.Lfa_c1:\n.Lfa_exit:\ns_waitcnt vmcnt(0) lgkmcnt(0)\ns_nop 7\ns_mov_b32 %0, 3\n"
".set .Lfa_w6_0, .Lfa_pd10812\n.set .Lfa_w6_1, .Lfa_pd11329\n.set .Lfa_w6_2, .Lfa_pd11875\n.set .Lfa_w6_3, .Lfa_pd12441\n.set .Lfa_w7_0, .Lfa_pd13009\n"
".set .Lfa_w7_1, .Lfa_pd13574\n.set .Lfa_w7_2, .Lfa_pd14140\n.set .Lfa_w7_3, .Lfa_pd14710\n.set .Lfa_w8_0, .Lfa_pd15279\n.set .Lfa_w8_1, .Lfa_pd15781\n"
".set .Lfa_w8_2, .Lfa_pd16279\n.set .Lfa_w8_3, .Lfa_pd16811\n.set .Lfa_w9_0, .Lfa_pd17368\n.set .Lfa_w9_1, .Lfa_pd17936\n.set .Lfa_w9_2, .Lfa_pd18512\n"
".set .Lfa_w9_3, .Lfa_pd19073\n.set .Lfa_w10_0, .Lfa_pd19647\n.set .Lfa_w10_1, .Lfa_pd20229\n.set .Lfa_w10_2, .Lfa_wr10\n.set .Lfa_w10_3, .Lfa_wr10\n"
".set .Lfa_w11_0, .Lfa_pd20778\n.set .Lfa_w11_1, .Lfa_pd21307\n.set .Lfa_w11_2, .Lfa_pd21836\n.set .Lfa_w11_3, .Lfa_pd22367\n"
".set .Lfa_w0_0, .Lfa_pd785\n.set .Lfa_w0_1, .Lfa_pd1300\n.set .Lfa_w0_2, .Lfa_pd1839\n.set .Lfa_w0_3, .Lfa_pd2343\n.set .Lfa_w1_0, .Lfa_pd2845\n"
".set .Lfa_w1_1, .Lfa_pd3376\n.set .Lfa_w1_2, .Lfa_pd3931\n.set .Lfa_w1_3, .Lfa_pd4504\n.set .Lfa_w2_0, .Lfa_pd5072\n.set .Lfa_w2_1, .Lfa_pd5639\n"
".set .Lfa_w2_2, .Lfa_pd6213\n.set .Lfa_w2_3, .Lfa_pd6785\n.set .Lfa_w3_0, .Lfa_pd7350\n.set .Lfa_w3_1, .Lfa_pd7918\n.set .Lfa_w3_2, .Lfa_pd8489\n"
".set .Lfa_w3_3, .Lfa_pd9054\n.set .Lfa_w4_0, .Lfa_pd9628\n.set .Lfa_w4_1, .Lfa_pd10212\n.set .Lfa_w4_2, .Lfa_wr4\n.set .Lfa_w4_3, .Lfa_wr4\n"
".set .Lfa_w5_0, .Lfa_pd20778\n.set .Lfa_w5_1, .Lfa_pd21307\n.set .Lfa_w5_2, .Lfa_pd21836\n.set .Lfa_w5_3, .Lfa_pd22367\n"
    FA_IO);
  }
  __syncthreads();
  {
    FA_DECODE
    unsigned lane; asm volatile("v_mbcnt_lo_u32_b32 %0, -1, 0\n\tv_mbcnt_hi_u32_b32 %0, -1, %0":"=v"(lane));
    for(int rnd=0;rnd<2;rnd++) if(status>>rnd&1u){
      const size_t kvo=(size_t)(b*NH+h0+2*rnd)*131072u;
      slow_head((const u16*)(((const char*)rec[0])+(size_t)b*(SQ*DM*2)),(const u16*)((const char*)rec[3]),(const u16*)(((const char*)rec[1])+kvo),(const u16*)(((const char*)rec[2])+kvo),
                (u16*)(((char*)rec[5])+O_OFF+(size_t)(br*NB+b)*(128*10*1024)),h0+2*rnd,80*r,(float*)lds+w*1024,lane);
    }
  }
  asm volatile("s_waitcnt vmcnt(0) lgkmcnt(0)":::"memory");
  __syncthreads();
  {
    FA_DECODE FA_OPERANDS
    asm volatile(
"s_mov_b64 s[40:41], %2\ns_mov_b32 s42, %11\ns_mov_b32 s43, 0x20000\ns_mov_b64 s[36:37], %1\ns_mov_b32 s38, 163840\ns_mov_b32 s39, 0x20000\n"
"s_lshr_b32 s83, %9, 1\ns_and_b32 s84, %9, 1\ns_lshl_b32 s88, s83, 14\ns_lshl_b32 s89, s84, 12\ns_add_u32 s72, s88, s89\ns_mul_i32 s89, s84, 3072\n"
"s_add_u32 s73, s88, s89\ns_add_u32 s73, s73, 10240\ns_add_u32 s74, s72, 10240\ns_lshl3_add_u32 s63, s84, s72\ns_and_b32 s90, %13, 0x380000\n"
"s_lshr_b32 s91, s90, 12\ns_mov_b32 s60, %9\ns_mov_b32 s61, %10\ns_mov_b32 s62, %15\ns_mov_b64 s[68:69], %8\ns_mov_b32 s70, 158720\n"
"s_mov_b32 s71, 0x20000\ns_lshl_b32 s94, s60, 12\ns_mov_b64 s[44:45], %3\ns_mov_b32 s46, 389120\ns_mov_b32 s47, 0x20000\ns_mov_b64 s[48:49], %4\n"
"s_mov_b32 s50, 389120\ns_mov_b32 s51, 0x20000\ns_mov_b64 s[52:53], %5\ns_mov_b32 s54, 360448\ns_mov_b32 s55, 0x20000\ns_mov_b64 s[56:57], %6\n"
"s_mov_b32 s58, 1163520\ns_mov_b32 s59, 0x20000\ns_mov_b64 s[64:65], %7\ns_mov_b32 s66, %12\ns_mov_b32 s67, 0x20000\ns_mov_b32 s79, 0\n"
"s_mov_b32 s80, 0x3e0293ee\ns_mov_b32 s81, 0x80008000\ns_mov_b32 s82, 0x6c800000\ns_lshl_b32 s85, s61, 7\ns_and_b32 s97, %13, 0x1e000\n"
"v_mbcnt_lo_u32_b32 v253, -1, 0\nv_mbcnt_hi_u32_b32 v253, -1, v253\nv_lshrrev_b32 v249, 5, v253\nv_bfe_u32 v250, v253, 4, 1\n"
"v_bfe_u32 v248, v253, 2, 2\nv_and_b32 v243, 3, v253\nv_lshlrev_b32 v240, 1, v249\nv_xor_b32 v240, v240, v243\nv_lshlrev_b32 v240, 4, v240\n"
"v_lshl_add_u32 v240, v250, 6, v240\nv_lshl_add_u32 v240, v248, 10, v240\nv_lshl_add_u32 v240, v249, 12, v240\ns_mul_i32 s88, s83, 8448\n"
"v_mul_u32_u24 v244, 544, v250\nv_lshl_add_u32 v244, v249, 10, v244\nv_lshl_add_u32 v244, v248, 6, v244\nv_lshl_add_u32 v244, v243, 3, v244\n"
"v_add_u32 v244, s88, v244\nv_xor_b32 v245, 32, v244\nv_xor_b32 v246, 0x100, v244\nv_xor_b32 v247, 0x120, v244\nv_add_u32 v241, 0x1c00, v240\n"
"v_add_u32 v242, 0x3800, v240\nv_add_u32 v243, 0x5400, v240\ns_and_b32 s88, s62, 7\ns_lshl_b32 s88, s88, 12\nv_mul_u32_u24 v249, 61440, v249\n"
"v_lshl_add_u32 v249, v253, 7, v249\nv_add_u32 v249, s88, v249\nv_add_u32 v250, 0x40000, v249\ns_mul_i32 s88, s60, 12288\ns_add_u32 s88, s88, 0x10000\n"
"v_lshlrev_b32 v248, 4, v253\nv_add_u32 v248, s88, v248\ns_mov_b32 s88, 0x11c000\nbuffer_load_dwordx2 v[180:181], off, s[56:59], s88 sc1\n"
"s_waitcnt vmcnt(0)\nv_readfirstlane_b32 s86, v180\nv_readfirstlane_b32 s87, v181\ns_nop 3\ns_mul_i32 s87, s87, 0x19660d\n"
"s_add_u32 s87, s87, 0x3c6ef35f\ns_mul_i32 s86, s86, 0x41c64e6d\ns_add_u32 s86, s86, 0x9e3779b9\ns_add_u32 s76, s85, 0x80\ns_and_b32 s76, s76, 0x3ff\n"
"s_add_u32 s76, s76, 0x0\ns_add_u32 m0, s94, 0x0\ns_nop 0\nDL 240,52,76,0\nDL 241,52,76,1024\nDL 242,52,76,2048\nDL 243,52,76,3072\n"
"s_add_u32 s76, s85, 0x80\ns_and_b32 s76, s76, 0x3ff\ns_add_u32 s76, s76, 0x10000\ns_add_u32 m0, s94, 0x4000\ns_nop 0\nDL 240,52,76,0\n"
"DL 241,52,76,1024\nDL 242,52,76,2048\nDL 243,52,76,3072\ns_add_u32 s76, s85, 0x80\ns_and_b32 s76, s76, 0x3ff\ns_add_u32 s76, s76, 0x40000\n"
"s_add_u32 m0, s94, 0x8000\ns_nop 0\nDL 240,52,76,0\nDL 241,52,76,1024\nDL 242,52,76,2048\nDL 243,52,76,3072\ns_add_u32 s76, s85, 0x80\n"
"s_and_b32 s76, s76, 0x3ff\ns_add_u32 s76, s76, 0x50000\ns_add_u32 m0, s94, 0xc000\ns_nop 0\nDL 240,52,76,0\nDL 241,52,76,1024\nDL 242,52,76,2048\n"
"DL 243,52,76,3072\nv_mbcnt_lo_u32_b32 v200, -1, 0\nv_mbcnt_hi_u32_b32 v200, -1, v200\nv_lshlrev_b32 v200, 4, v200\ns_mul_i32 s89, s83, 40960\n"
"s_mul_i32 s88, s83, 2048\ns_add_u32 s89, s89, s88\ns_add_u32 s78, s89, 0x0\nbuffer_load_dwordx4 a[80:83], v200, s[68:71], s78 offen sc1\n"
"s_add_u32 s78, s89, 0x2800\nbuffer_load_dwordx4 a[84:87], v200, s[68:71], s78 offen sc1\ns_add_u32 s78, s89, 0x5000\n"
"buffer_load_dwordx4 a[88:91], v200, s[68:71], s78 offen sc1\ns_add_u32 s78, s89, 0x7800\nbuffer_load_dwordx4 a[92:95], v200, s[68:71], s78 offen sc1\n"
"s_add_u32 s78, s89, 0x14000\nbuffer_load_dwordx4 v[0:3], v200, s[68:71], s78 offen sc1\ns_add_u32 s78, s89, 0x16800\n"
"buffer_load_dwordx4 v[8:11], v200, s[68:71], s78 offen sc1\ns_add_u32 s78, s89, 0x19000\nbuffer_load_dwordx4 v[16:19], v200, s[68:71], s78 offen sc1\n"
"s_add_u32 s78, s89, 0x1b800\nbuffer_load_dwordx4 v[24:27], v200, s[68:71], s78 offen sc1\ns_mul_i32 s89, s83, 40960\ns_mul_i32 s88, s83, 0\n"
"s_add_u32 s89, s89, s88\ns_add_u32 s78, s89, 0x400\nbuffer_load_dwordx4 a[96:99], v200, s[68:71], s78 offen sc1\ns_add_u32 s78, s89, 0x2c00\n"
"buffer_load_dwordx4 a[100:103], v200, s[68:71], s78 offen sc1\ns_add_u32 s78, s89, 0x5400\n"
"buffer_load_dwordx4 a[104:107], v200, s[68:71], s78 offen sc1\ns_add_u32 s78, s89, 0x7c00\n"
"buffer_load_dwordx4 a[108:111], v200, s[68:71], s78 offen sc1\ns_add_u32 s78, s89, 0x14400\n"
"buffer_load_dwordx4 v[32:35], v200, s[68:71], s78 offen sc1\ns_add_u32 s78, s89, 0x16c00\n"
"buffer_load_dwordx4 v[40:43], v200, s[68:71], s78 offen sc1\ns_add_u32 s78, s89, 0x19400\n"
"buffer_load_dwordx4 v[48:51], v200, s[68:71], s78 offen sc1\ns_add_u32 s78, s89, 0x1bc00\n"
"buffer_load_dwordx4 v[56:59], v200, s[68:71], s78 offen sc1\ns_mul_i32 s89, s83, 40960\ns_mul_i32 s88, s83, 1024\ns_add_u32 s89, s89, s88\n"
"s_add_u32 s78, s89, 0xc00\nbuffer_load_dwordx4 a[128:131], v200, s[68:71], s78 offen sc1\ns_add_u32 s78, s89, 0x3400\n"
"buffer_load_dwordx4 a[132:135], v200, s[68:71], s78 offen sc1\ns_add_u32 s78, s89, 0x5c00\n"
"buffer_load_dwordx4 a[136:139], v200, s[68:71], s78 offen sc1\ns_add_u32 s78, s89, 0x8400\n"
"buffer_load_dwordx4 a[140:143], v200, s[68:71], s78 offen sc1\ns_add_u32 s78, s89, 0x14c00\n"
"buffer_load_dwordx4 v[96:99], v200, s[68:71], s78 offen sc1\ns_add_u32 s78, s89, 0x17400\n"
"buffer_load_dwordx4 v[104:107], v200, s[68:71], s78 offen sc1\ns_add_u32 s78, s89, 0x19c00\n"
"buffer_load_dwordx4 v[112:115], v200, s[68:71], s78 offen sc1\ns_add_u32 s78, s89, 0x1c400\n"
"buffer_load_dwordx4 v[120:123], v200, s[68:71], s78 offen sc1\ns_xor_b32 s89, s83, 1\ns_mul_i32 s89, s89, 40960\ns_mul_i32 s88, s83, 2048\n"
"s_add_u32 s89, s89, s88\ns_add_u32 s78, s89, 0x0\nbuffer_load_dwordx4 a[0:3], v200, s[68:71], s78 offen sc1\ns_add_u32 s78, s89, 0x2800\n"
"buffer_load_dwordx4 a[4:7], v200, s[68:71], s78 offen sc1\ns_add_u32 s78, s89, 0x5000\nbuffer_load_dwordx4 a[8:11], v200, s[68:71], s78 offen sc1\n"
"s_add_u32 s78, s89, 0x7800\nbuffer_load_dwordx4 a[12:15], v200, s[68:71], s78 offen sc1\ns_add_u32 s78, s89, 0x14000\n"
"buffer_load_dwordx4 v[4:7], v200, s[68:71], s78 offen sc1\ns_add_u32 s78, s89, 0x16800\nbuffer_load_dwordx4 v[12:15], v200, s[68:71], s78 offen sc1\n"
"s_add_u32 s78, s89, 0x19000\nbuffer_load_dwordx4 v[20:23], v200, s[68:71], s78 offen sc1\ns_add_u32 s78, s89, 0x1b800\n"
"buffer_load_dwordx4 v[28:31], v200, s[68:71], s78 offen sc1\ns_xor_b32 s89, s83, 1\ns_mul_i32 s89, s89, 40960\ns_mul_i32 s88, s83, 0\n"
"s_add_u32 s89, s89, s88\ns_add_u32 s78, s89, 0x400\nbuffer_load_dwordx4 a[16:19], v200, s[68:71], s78 offen sc1\ns_add_u32 s78, s89, 0x2c00\n"
"buffer_load_dwordx4 a[20:23], v200, s[68:71], s78 offen sc1\ns_add_u32 s78, s89, 0x5400\nbuffer_load_dwordx4 a[24:27], v200, s[68:71], s78 offen sc1\n"
"s_add_u32 s78, s89, 0x7c00\nbuffer_load_dwordx4 a[28:31], v200, s[68:71], s78 offen sc1\ns_add_u32 s78, s89, 0x14400\n"
"buffer_load_dwordx4 v[36:39], v200, s[68:71], s78 offen sc1\ns_add_u32 s78, s89, 0x16c00\n"
"buffer_load_dwordx4 v[44:47], v200, s[68:71], s78 offen sc1\ns_add_u32 s78, s89, 0x19400\n"
"buffer_load_dwordx4 v[52:55], v200, s[68:71], s78 offen sc1\ns_add_u32 s78, s89, 0x1bc00\n"
"buffer_load_dwordx4 v[60:63], v200, s[68:71], s78 offen sc1\ns_xor_b32 s89, s83, 1\ns_mul_i32 s89, s89, 40960\ns_mul_i32 s88, s83, 1024\n"
"s_add_u32 s89, s89, s88\ns_add_u32 s78, s89, 0xc00\nbuffer_load_dwordx4 a[32:35], v200, s[68:71], s78 offen sc1\ns_add_u32 s78, s89, 0x3400\n"
"buffer_load_dwordx4 a[36:39], v200, s[68:71], s78 offen sc1\ns_add_u32 s78, s89, 0x5c00\nbuffer_load_dwordx4 a[40:43], v200, s[68:71], s78 offen sc1\n"
"s_add_u32 s78, s89, 0x8400\nbuffer_load_dwordx4 a[44:47], v200, s[68:71], s78 offen sc1\ns_add_u32 s78, s89, 0x14c00\n"
"buffer_load_dwordx4 v[100:103], v200, s[68:71], s78 offen sc1\ns_add_u32 s78, s89, 0x17400\n"
"buffer_load_dwordx4 v[108:111], v200, s[68:71], s78 offen sc1\ns_add_u32 s78, s89, 0x19c00\n"
"buffer_load_dwordx4 v[116:119], v200, s[68:71], s78 offen sc1\ns_add_u32 s78, s89, 0x1c400\n"
"buffer_load_dwordx4 v[124:127], v200, s[68:71], s78 offen sc1\ns_waitcnt vmcnt(0)\ns_barrier\nv_xor_b32 v249, 0x2000, v244\n"
"v_xor_b32 v250, 0x2000, v245\nv_xor_b32 v251, 0x2000, v246\nv_xor_b32 v252, 0x2000, v247\nTa 184,244,0\nTa 186,244,2048\nTa 188,244,4096\n"
"Ta 190,244,6144\nTa 192,249,0\nTa 194,249,2048\nTa 196,249,4096\nTa 198,249,6144\nTa 200,244,16384\nTa 202,244,18432\nTa 204,244,20480\n"
"Ta 206,244,22528\nTa 208,249,16384\nTa 210,249,18432\nTa 212,249,20480\nTa 214,249,22528\ns_waitcnt lgkmcnt(14)\nZvaa 160,184,80\n"
"s_waitcnt lgkmcnt(12)\ns_add_u32 s76, s85, 0x100\nTa 216,246,0\nMvaa 160,188,84\ns_waitcnt lgkmcnt(11)\ns_and_b32 s76, s76, 0x3ff\nTa 218,246,2048\n"
"Mvaa 160,192,0\ns_waitcnt lgkmcnt(10)\ns_add_u32 s76, s76, 0x0\nTa 220,246,4096\nMvaa 160,196,4\ns_waitcnt lgkmcnt(9)\ns_add_u32 m0, s94, 0x10000\n"
"Ta 222,246,6144\nMvaa 160,200,88\ns_waitcnt lgkmcnt(8)\nDL 240,52,76,0\nMvaa 160,204,92\ns_waitcnt lgkmcnt(6)\nTa 224,251,0\nMvaa 160,208,8\n"
"s_waitcnt lgkmcnt(5)\nTa 226,251,2048\nMvaa 160,212,12\nDL 241,52,76,1024\nZvaa 176,184,128\nMvaa 176,188,132\nMvaa 176,192,32\nDL 242,52,76,2048\n"
"Mvaa 176,196,36\nMvaa 176,200,136\nMvaa 176,204,140\nDL 243,52,76,3072\nMvaa 176,208,40\ns_add_u32 s76, s85, 0x100\nMvaa 176,212,44\n"
"s_and_b32 s76, s76, 0x3ff\nTa 228,251,4096\nTa 230,251,6144\nZvaa 192,184,96\ns_add_u32 s76, s76, 0x10000\nTa 232,246,16384\nMvaa 192,188,100\n"
"s_add_u32 m0, s94, 0x14000\nTa 234,246,18432\nTa 236,246,20480\nMvaa 192,192,16\nDL 240,52,76,0\nMvaa 192,196,20\nTa 238,246,22528\nMvaa 192,200,104\n"
"Ta 240,251,16384\nTa 242,251,18432\nMvaa 192,204,108\nTa 244,251,20480\nTa 246,251,22528\nMvaa 192,208,24\nDL 241,52,76,1024\nMvaa 192,212,28\n"
"s_waitcnt lgkmcnt(7)\nTa 48,245,0\nZvaa 168,216,80\nTa 50,245,2048\nMvaa 168,220,84\nDL 242,52,76,2048\nMvaa 168,224,0\nTa 52,245,4096\n"
"Ta 54,245,6144\nMvaa 168,228,4\ns_waitcnt lgkmcnt(6)\nTa 56,250,0\nMvaa 168,232,88\nDL 243,52,76,3072\nMvaa 168,236,92\nTa 58,250,2048\n"
"Ta 60,250,4096\nMvaa 168,240,8\ns_waitcnt lgkmcnt(6)\nTa 62,250,6144\nMvaa 168,244,12\nTa 64,245,16384\nZvaa 184,216,128\nTa 66,245,18432\n"
"Mvaa 184,220,132\nTa 68,245,20480\nMvaa 184,224,32\nTa 70,245,22528\nMvaa 184,228,36\nTa 72,250,16384\nMvaa 184,232,136\nTa 74,250,18432\n"
"Mvaa 184,236,140\nTa 76,250,20480\nTa 78,250,22528\nMvaa 184,240,40\ns_waitcnt vmcnt(10) lgkmcnt(0)\nTa 184,247,0\nMvaa 184,244,44\ns_barrier\n"
"Zvaa 164,48,80\nTa 186,247,2048\nMvaa 164,52,84\nTa 188,247,4096\nMvaa 164,56,0\nTa 190,247,6144\nMvaa 164,60,4\nTa 192,252,0\nMvaa 164,64,88\n"
"Ta 194,252,2048\nMvaa 164,68,92\nTa 196,252,4096\nMvaa 164,72,8\nTa 198,252,6144\nMvaa 164,76,12\nTa 200,247,16384\nZvaa 180,48,128\n"
"Ta 202,247,18432\nMvaa 180,52,132\nTa 204,247,20480\nMvaa 180,56,32\nTa 206,247,22528\nMvaa 180,60,36\nTa 208,252,16384\nMvaa 180,64,136\n"
"Ta 210,252,18432\nMvaa 180,68,140\nTa 212,252,20480\nMvaa 180,72,40\nTa 214,252,22528\nMvaa 180,76,44\nTa 216,244,32768\nZvaa 196,48,96\n"
"Ta 218,244,34816\nMvaa 196,52,100\nTa 220,244,36864\nMvaa 196,56,16\nMvaa 196,60,20\nMvaa 196,64,104\nMvaa 196,68,108\nMvaa 196,72,24\n"
"Mvaa 196,76,28\ns_waitcnt lgkmcnt(1)\nZvaa 172,184,80\nMvaa 172,188,84\nMvaa 172,192,0\nMvaa 172,196,4\nTa 222,244,38912\nTa 224,249,32768\n"
"Mvaa 172,200,88\nTa 226,249,34816\nMvaa 172,204,92\nTa 228,249,36864\nMvaa 172,208,8\nTa 230,249,38912\nMvaa 172,212,12\nTa 232,244,49152\n"
"Zvaa 188,184,128\nMvaa 188,188,132\nTa 234,244,51200\nMvaa 188,192,32\nTa 236,244,53248\nMvaa 188,196,36\nTa 238,244,55296\nMvaa 188,200,136\n"
"Ta 240,249,49152\nTa 242,249,51200\nMvaa 188,204,140\nTa 244,249,53248\nTa 246,249,55296\nMvaa 188,208,40\ns_waitcnt vmcnt(10) lgkmcnt(0)\n"
"Ta 48,246,32768\nv_add_u32 v244, 0x10000, v244\nMvaa 188,212,44\ns_barrier\ns_branch .Lfa_h2\n"
    FA_IO);
  }
}

extern "C" void kernel_launch(void* const* d_in, const int* in_sizes, int n_in, void* d_out, int out_size, void* d_ws, size_t ws_size, hipStream_t stream) {
  P1 a;
  for(int i=0;i<2;i++){ Rec& r=a.r[i]; r.x=(const char*)d_in[5*i]; r.k=(const char*)d_in[5*i+1]; r.v=(const char*)d_in[5*i+2]; r.wq=(const char*)d_in[5*i+3]; r.wo=(const char*)d_in[5*i+4];
    r.ws=(char*)d_ws; r.out=(char*)d_out; r.ws2=(char*)d_ws; }
  (void)hipFuncSetAttribute((const void*)fused_attn,hipFuncAttributeMaxDynamicSharedMemorySize,163840);
  fused_attn<<<256,256,163840,stream>>>(a);
}
```
